```python
import torch

def _softmax(x, axis=-1):
    return torch.softmax(x, dim=axis)
def _gelu(x):
    return torch.nn.functional.gelu(x, approximate="tanh")
def _silu(x):
    return torch.nn.functional.silu(x)
def _sigmoid(x):
    return torch.sigmoid(x)
def _layernorm(x, axis=-1, eps=1e-5):
    m = x.mean(dim=axis, keepdim=True)
    v = x.var(dim=axis, keepdim=True, unbiased=False)
    return (x - m) / torch.sqrt(v + eps)
def _scatter_add(tgt, idx, src, axis=0):
    return tgt.clone().index_add_(axis, idx, src)
def _segment_sum(src, seg, n):
    out = torch.zeros((n,) + src.shape[1:], dtype=src.dtype)
    return out.index_add_(0, seg, src)
def _pad(x, pw):
    flat = tuple(v for pair in reversed(pw) for v in pair)
    return torch.nn.functional.pad(x, flat)

kernel_name = 'standalone__modern_conv_block_convnext_v2__vision'

def setup_inputs():

    _w = (lambda a: (lambda t: t.to(torch.bfloat16) if t.is_floating_point() else t)(torch.as_tensor(a)))
    H = W = 512
    C = 96
    K = 3
    return {
        "x": _w((lambda *s: torch.randn(*s, dtype=torch.bfloat16) * 0.05)(1, H, W, C)),
        "dw": _w((lambda *s: torch.randn(*s, dtype=torch.bfloat16) * 0.05)(K, K, C)),
        "w1": _w((lambda *s: torch.randn(*s, dtype=torch.bfloat16) * 0.05)(C, 4 * C)),
        "w2": _w((lambda *s: torch.randn(*s, dtype=torch.bfloat16) * 0.05)(4 * C, C)),
        "grn_gamma": _w((lambda *s: torch.randn(*s, dtype=torch.bfloat16) * 0.05)(4 * C)),
        "grn_beta": _w((lambda *s: torch.randn(*s, dtype=torch.bfloat16) * 0.05)(4 * C)),
    }

def reference(x, dw, w1, w2, grn_gamma, grn_beta):
    B, H, W, C = x.shape
    K = dw.shape[0]
    pad = K // 2
    xp = _pad(x, ((0,0),(pad,pad),(pad,pad),(0,0)))
    out = torch.zeros_like(x)
    for i in range(K):
        for j in range(K):
            out = out + xp[:, i:i+H, j:j+W, :] * dw[i, j][None, None, None, :]
    h = _layernorm(out, axis=-1)
    h = torch.einsum('bhwc,ce->bhwe', h, w1)
    h = _gelu(h)
    gx = torch.sqrt((h * h).sum(axis=(1, 2), keepdims=True) + 1e-6)
    nx = gx / (gx.mean(axis=-1, keepdims=True) + 1e-6)
    h = grn_gamma * (h * nx) + grn_beta + h
    h = torch.einsum('bhwe,ec->bhwc', h, w2)
    return x + h
```

```cpp
#include <hip/hip_runtime.h>
#include <hip/hip_bf16.h>
#include <stdint.h>

__device__ const unsigned g_lut[3072] = {
0x00060002,0x000e000a,0x00160012,0x001e001a,0x00260022,0x002e002a,0x00360032,0x003e003a,0x00460042,0x004e004a,0x00560052,0x005e005a,
0x00660062,0x006e006a,0x00760072,0x007e007a,0x00860082,0x008e008a,0x00960092,0x009e009a,0x00a600a2,0x00ae00aa,0x00b600b2,0x00be00ba,
0x00c600c2,0x00ce00ca,0x00d600d2,0x00de00da,0x00e600e2,0x00ee00ea,0x00f600f2,0x00fe00fa,0x01060102,0x010e010a,0x01160112,0x011e011a,
0x01260122,0x012e012a,0x01360132,0x013e013a,0x01460142,0x014e014a,0x01560152,0x015e015a,0x01660162,0x016e016a,0x01760172,0x017e017a,
0x01860182,0x018e018a,0x01960192,0x019e019a,0x01a601a2,0x01ae01aa,0x01b601b2,0x01be01ba,0x01c601c2,0x01ce01ca,0x01d601d2,0x01de01da,
0x01e601e2,0x01ee01ea,0x01f601f2,0x01fe01fa,0x02040200,0x020c0208,0x02140210,0x021c0218,0x02240220,0x022c0228,0x02340230,0x023c0238,
0x02440240,0x024c0248,0x02540250,0x025c0258,0x02640260,0x026c0268,0x02740270,0x027c0278,0x02840280,0x028c0288,0x02940290,0x029c0298,
0x02a402a0,0x02ac02a8,0x02b402b0,0x02bc02b8,0x02c402c0,0x02cc02c8,0x02d402d0,0x02dc02d8,0x02e402e0,0x02ec02e8,0x02f402f0,0x02fc02f8,
0x03040300,0x030c0308,0x03140310,0x031c0318,0x03240320,0x032c0328,0x03340330,0x033c0338,0x03440340,0x034c0348,0x03540350,0x035c0358,
0x03640360,0x036c0368,0x03740370,0x037c0378,0x03840380,0x038c0388,0x03940390,0x039c0398,0x03a403a0,0x03ac03a8,0x03b403b0,0x03bc03b8,
0x03c403c0,0x03cc03c8,0x03d403d0,0x03dc03d8,0x03e403e0,0x03ec03e8,0x03f403f0,0x03fc03f8,0x04080400,0x04180410,0x04280420,0x04380430,
0x04480440,0x04580450,0x04680460,0x04780470,0x04880480,0x04980490,0x04a804a0,0x04b804b0,0x04c804c0,0x04d804d0,0x04e804e0,0x04f804f0,
0x05080500,0x05180510,0x05280520,0x05380530,0x05480540,0x05580550,0x05680560,0x05780570,0x05880580,0x05980590,0x05a805a0,0x05b805b0,
0x05c805c0,0x05d805d0,0x05e805e0,0x05f805f0,0x06080600,0x06180610,0x06280620,0x06380630,0x06480640,0x06580650,0x06680660,0x06780670,
0x06880680,0x06980690,0x06a806a0,0x06b806b0,0x06c806c0,0x06d806d0,0x06e806e0,0x06f806f0,0x07080700,0x07180710,0x07280720,0x07380730,
0x07480740,0x07580750,0x07680760,0x07780770,0x07880780,0x07980790,0x07a807a0,0x07b807b0,0x07c807c0,0x07d807d0,0x07e807e0,0x07f807f0,
0x08080800,0x08180810,0x08280820,0x08380830,0x08480840,0x08580850,0x08680860,0x08780870,0x08880880,0x08980890,0x08a808a0,0x08b808b0,
0x08c808c0,0x08d808d0,0x08e808e0,0x08f808f0,0x09080900,0x09180910,0x09280920,0x09380930,0x09480940,0x09580950,0x09680960,0x09780970,
0x09880980,0x09980990,0x09a809a0,0x09b809b0,0x09c809c0,0x09d809d0,0x09e809e0,0x09f809f0,0x0a080a00,0x0a180a10,0x0a280a20,0x0a380a30,
0x0a480a40,0x0a580a50,0x0a680a60,0x0a780a70,0x0a880a80,0x0a980a90,0x0aa80aa0,0x0ab80ab0,0x0ac80ac0,0x0ad80ad0,0x0ae80ae0,0x0af80af0,
0x0b080b00,0x0b180b10,0x0b280b20,0x0b380b30,0x0b480b40,0x0b580b50,0x0b680b60,0x0b780b70,0x0b880b80,0x0b980b90,0x0ba80ba0,0x0bb80bb0,
0x0bc80bc0,0x0bd80bd0,0x0be80be0,0x0bf80bf0,0x0c080c00,0x0c180c10,0x0c280c20,0x0c380c30,0x0c480c40,0x0c580c50,0x0c680c60,0x0c780c70,
0x0c880c80,0x0c980c90,0x0ca80ca0,0x0cb80cb0,0x0cc80cc0,0x0cd80cd0,0x0ce80ce0,0x0cf80cf0,0x0d080d00,0x0d180d10,0x0d280d20,0x0d380d30,
0x0d480d40,0x0d580d50,0x0d680d60,0x0d780d70,0x0d880d80,0x0d980d90,0x0da80da0,0x0db80db0,0x0dc80dc0,0x0dd80dd0,0x0de80de0,0x0df80df0,
0x0e080e00,0x0e180e10,0x0e280e20,0x0e380e30,0x0e480e40,0x0e580e50,0x0e680e60,0x0e780e70,0x0e880e80,0x0e980e90,0x0ea80ea0,0x0eb80eb0,
0x0ec80ec0,0x0ed80ed0,0x0ee80ee0,0x0ef80ef0,0x0f080f00,0x0f180f10,0x0f280f20,0x0f380f30,0x0f480f40,0x0f580f50,0x0f680f60,0x0f780f70,
0x0f880f80,0x0f980f90,0x0fa80fa0,0x0fb80fb0,0x0fc80fc0,0x0fd80fd0,0x0fe80fe0,0x0ff80ff0,0x10081000,0x10181010,0x10281020,0x10381030,
0x10481040,0x10581050,0x10681060,0x10781070,0x10881080,0x10981090,0x10a810a0,0x10b810b0,0x10c810c0,0x10d810d0,0x10e810e0,0x10f810f0,
0x11081100,0x11181110,0x11281120,0x11381130,0x11481140,0x11581150,0x11681160,0x11781170,0x11881180,0x11981190,0x11a811a0,0x11b811b0,
0x11c811c0,0x11d811d0,0x11e811e0,0x11f811f0,0x12081200,0x12181210,0x12281220,0x12381230,0x12481240,0x12581250,0x12681260,0x12781270,
0x12881280,0x12981290,0x12a812a0,0x12b812b0,0x12c812c0,0x12d812d0,0x12e812e0,0x12f812f0,0x13081300,0x13181310,0x13281320,0x13381330,
0x13481340,0x13581350,0x13681360,0x13781370,0x13881380,0x13981390,0x13a813a0,0x13b813b0,0x13c813c0,0x13d813d0,0x13e813e0,0x13f813f0,
0x14081400,0x14181410,0x14281420,0x14381430,0x14481440,0x14581450,0x14681460,0x14781470,0x14881480,0x14981490,0x14a814a0,0x14b814b0,
0x14c814c0,0x14d814d0,0x14e814e0,0x14f814f0,0x15081500,0x15181510,0x15281520,0x15381530,0x15481540,0x15581550,0x15681560,0x15781570,
0x15881580,0x15981590,0x15a815a0,0x15b815b0,0x15c815c0,0x15d815d0,0x15e815e0,0x15f815f0,0x16081600,0x16181610,0x16201618,0x16301628,
0x16401638,0x16501648,0x16601658,0x16701668,0x16801678,0x16901688,0x16a01698,0x16b016a8,0x16c016b8,0x16d016c8,0x16e016d8,0x16f016e8,
0x170016f8,0x17101708,0x17201718,0x17301728,0x17401738,0x17501748,0x17601758,0x17701768,0x17801778,0x17901788,0x17a01798,0x17b017a8,
0x17c017b8,0x17d017c8,0x17e017d8,0x17f017e8,0x180817f8,0x18181810,0x18281820,0x18381830,0x18481840,0x18501850,0x18601858,0x18701868,
0x18801878,0x18901888,0x18a01898,0x18b018a8,0x18c018b8,0x18d018c8,0x18e018d8,0x18f018e8,0x190018f8,0x19101908,0x19201918,0x19301928,
0x19401938,0x19501948,0x19601958,0x19701968,0x19801978,0x19901988,0x19a01998,0x19b019a8,0x19c019b8,0x19d019c8,0x19e019d8,0x19f019e8,
0x1a0019f8,0x1a101a08,0x1a181a10,0x1a281a20,0x1a381a30,0x1a481a40,0x1a581a50,0x1a681a60,0x1a781a70,0x1a881a80,0x1a981a90,0x1aa81aa0,
0x1ab81ab0,0x1ac81ac0,0x1ad81ad0,0x1ae81ae0,0x1af81af0,0x1b081b00,0x1b181b10,0x1b281b20,0x1b381b30,0x1b481b40,0x1b581b50,0x1b681b60,
0x1b701b68,0x1b801b78,0x1b901b88,0x1ba01b98,0x1bb01ba8,0x1bc01bb8,0x1bd01bc8,0x1be01bd8,0x1c001bf0,0x1c101c08,0x1c201c18,0x1c301c28,
0x1c401c38,0x1c481c48,0x1c581c50,0x1c681c60,0x1c781c70,0x1c881c80,0x1c981c90,0x1ca81ca0,0x1cb81cb0,0x1cc81cc0,0x1cd81cd0,0x1ce81ce0,
0x1cf81cf0,0x1d081d00,0x1d181d10,0x1d281d20,0x1d381d30,0x1d401d38,0x1d501d48,0x1d601d58,0x1d701d68,0x1d801d78,0x1d901d88,0x1da01d98,
0x1db01da8,0x1dc01db8,0x1dd01dc8,0x1de01dd8,0x1df01de8,0x1e001df8,0x1e081e00,0x1e181e10,0x1e281e20,0x1e381e30,0x1e481e40,0x1e581e50,
0x1e681e60,0x1e781e70,0x1e881e80,0x1e981e90,0x1ea81ea0,0x1eb01eb0,0x1ec01eb8,0x1ed01ec8,0x1ee01ed8,0x1ef01ee8,0x1f001ef8,0x1f101f08,
0x1f201f18,0x1f301f28,0x1f401f38,0x1f501f48,0x1f581f50,0x1f681f60,0x1f781f70,0x1f881f80,0x1f981f90,0x1fa81fa0,0x1fb81fb0,0x1fc81fc0,
0x1fe01fd0,0x20001ff0,0x20102008,0x20202018,0x20302028,0x20382038,0x20482040,0x20582050,0x20682060,0x20782070,0x20882080,0x20982090,
0x20a820a0,0x20b020b0,0x20c020b8,0x20d020c8,0x20e020d8,0x20f020e8,0x210020f8,0x21102108,0x21202118,0x21282120,0x21382130,0x21482140,
0x21582150,0x21682160,0x21782170,0x21882180,0x21902188,0x21a02198,0x21b021a8,0x21c021b8,0x21d021c8,0x21e021d8,0x21e821e0,0x21f821f0,
0x22082200,0x22182210,0x22282220,0x22382230,0x22402238,0x22502248,0x22602258,0x22702268,0x22802278,0x22882288,0x22982290,0x22a822a0,
0x22b822b0,0x22c822c0,0x22d822d0,0x22e022d8,0x22f022e8,0x230022f8,0x23102308,0x23202318,0x23282320,0x23382330,0x23482340,0x23582350,
0x23682360,0x23702368,0x23802378,0x23902388,0x23a82398,0x23c823b8,0x23e023d0,0x240023f0,0x24102408,0x24182418,0x24282420,0x24382430,
0x24482440,0x24502450,0x24602458,0x24702468,0x24802478,0x24882488,0x24982490,0x24a824a0,0x24b824b0,0x24c024c0,0x24d024c8,0x24e024d8,
0x24f024e8,0x24f824f0,0x25082500,0x25182510,0x25202520,0x25302528,0x25402538,0x25502548,0x25582550,0x25682560,0x25782570,0x25802578,
0x25902588,0x25a02598,0x25a825a0,0x25b825b0,0x25c825c0,0x25d025c8,0x25e025d8,0x25f025e8,0x25f825f0,0x26082600,0x26182610,0x26202618,
0x26302628,0x26402638,0x26482640,0x26582650,0x26602660,0x26702668,0x26802678,0x26882680,0x26982690,0x26a026a0,0x26b026a8,0x26c026b8,
0x26c826c8,0x26d826d0,0x26e826e0,0x26f026e8,0x270026f8,0x27082700,0x27182710,0x27202720,0x27382730,0x27502748,0x27682760,0x27882778,
0x27a02790,0x27b827a8,0x27d027c0,0x27e827d8,0x280027f0,0x28082808,0x28182810,0x28202820,0x28302828,0x28382838,0x28482840,0x28502850,
0x28602858,0x28682868,0x28782870,0x28802880,0x28902888,0x28982890,0x28a828a0,0x28b028a8,0x28b828b8,0x28c828c0,0x28d028d0,0x28e028d8,
0x28e828e0,0x28f028f0,0x290028f8,0x29082908,0x29182910,0x29202918,0x29282928,0x29382930,0x29402938,0x29482948,0x29582950,0x29602958,
0x29682968,0x29782970,0x29802978,0x29882988,0x29902990,0x29a02998,0x29a829a0,0x29b029b0,0x29c029b8,0x29c829c0,0x29d029c8,0x29d829d8,
0x29e829e0,0x29f029e8,0x29f829f0,0x2a002a00,0x2a102a08,0x2a182a10,0x2a202a18,0x2a282a28,0x2a302a30,0x2a382a38,0x2a482a40,0x2a502a48,
0x2a602a58,0x2a702a68,0x2a802a78,0x2a982a90,0x2aa82aa0,0x2ab82ab0,0x2ac82ac0,0x2ae02ad0,0x2af02ae8,0x2b002af8,0x2b102b08,0x2b202b18,
0x2b302b28,0x2b402b38,0x2b502b48,0x2b602b58,0x2b702b68,0x2b802b78,0x2b902b88,0x2ba02b98,0x2ba82ba0,0x2bb82bb0,0x2bc82bc0,0x2bd82bd0,
0x2be82be0,0x2bf02be8,0x2c002bf8,0x2c082c00,0x2c102c08,0x2c102c10,0x2c182c18,0x2c202c20,0x2c282c20,0x2c302c28,0x2c302c30,0x2c382c38,
0x2c402c40,0x2c482c40,0x2c482c48,0x2c502c50,0x2c582c50,0x2c582c58,0x2c602c60,0x2c682c60,0x2c682c68,0x2c702c70,0x2c782c70,0x2c782c78,
0x2c802c80,0x2c882c80,0x2c882c88,0x2c902c88,0x2c902c90,0x2c982c98,0x2c982c98,0x2ca02ca0,0x2ca02ca0,0x2ca82ca8,0x2ca82ca8,0x2cb02cb0,
0x2cb02cb0,0x2cb82cb8,0x2cb82cb8,0x2cc02cc0,0x2cc82cc0,0x2cc82cc8,0x2cd02cd0,0x2cd82cd8,0x2ce02cd8,0x2ce82ce0,0x2ce82ce8,0x2cf02ce8,
0x2cf02cf0,0x2cf82cf8,0x2cf82cf8,0x2d002d00,0x2d002d00,0x2d082d08,0x2d082d08,0x2d082d08,0x2d102d10,0x2d102d10,0x2d102d10,0x2d182d10,
0x2d182d18,0x2d182d18,0x2d182d18,0x2d182d18,0x2d182d18,0x2d182d18,0x2d182d18,0x2d182d18,0x2d182d18,0x2d182d18,0x2d182d18,0x2d182d18,
0x2d182d18,0x2d102d10,0x2d102d10,0x2d102d10,0x2d102d10,0x2d082d10,0x2d082d08,0x2d082d08,0x2d002d08,0x2d002d00,0x2d002d00,0x2cf82cf8,
0x2cf82cf8,0x2cf02cf0,0x2cf02cf0,0x2ce82ce8,0x2ce82ce8,0x2ce02ce0,0x2ce02ce0,0x2cd82cd8,0x2cd02cd8,0x2cd02cd0,0x2cc82cc8,0x2cc02cc8,
0x2cc02cc0,0x2cb82cb8,0x2cb02cb8,0x2cb02cb0,0x2ca82ca8,0x2ca02ca8,0x2c982ca0,0x2c982c98,0x2c882c90,0x2c802c88,0x2c702c78,0x2c602c68,
0x2c582c58,0x2c482c50,0x2c382c40,0x2c282c30,0x2c182c20,0x2c082c10,0x2bf82c00,0x2bd82be8,0x2bb82bc8,0x2b982ba8,0x2b782b88,0x2b582b68,
0x2b382b48,0x2b102b28,0x2af02b00,0x2ad02ae0,0x2ab02ac0,0x2a902aa0,0x2a702a80,0x2a502a60,0x2a302a40,0x2a102a20,0x29e829f8,0x29c829d8,
0x29a829b8,0x29882998,0x29682978,0x29482958,0x29282938,0x29102920,0x28f02900,0x28d028e0,0x28b028c0,0x289028a0,0x28782888,0x28582868,
0x28382848,0x28202830,0x28002810,0x27d027e8,0x279827b0,0x27602780,0x27302748,0x26f82710,0x26c826e0,0x269826b0,0x26602678,0x26302648,
0x26002618,0x25d825e8,0x25a825c0,0x25782590,0x25502568,0x25202538,0x24f82510,0x24d024e8,0x24a824c0,0x24802498,0x24582470,0x24382448,
0x23f02418,0x236023a8,0x22e02320,0x226022a0,0x21f02228,0x218021b8,0x21182148,0x20b020e8,0x20582088,0x20002028,0x1f601fb0,0x1ec81f10,
0x1e381e80,0x1db01df0,0x1d381d70,0x1cc01cf8,0x1c581c90,0x1be81c28,0x1b381b90,0x1a881ae0,0x19f01a40,0x196019a8,0x18e01920,0x186818a0,
0x17e81828,0x17201788,0x166816c0,0x15c01610,0x15281570,0x149814e0,0x14201458,0x135813c8,0x128812f0,0x11d01228,0x11281178,0x109010d8,
0x10081048,0x0f200f98,0x0e480eb0,0x0d880de0,0x0cd80d30,0x0c400c88,0x0b700bf8,0x0a800af8,0x09a80a10,0x08f00948,0x08480898,0x07700800,
0x067006e8,0x059005f8,0x04c80528,0x04200470,0x038c03d4,0x03080348,0x029802d0,0x02380268,0x01e4020c,0x019c01c0,0x015e017c,0x01280142,
0x00fb0110,0x00d400e6,0x00b200c2,0x009600a3,0x006e0083,0x004c005c,0x00350040,0x0024002c,0x0018001e,0x00100014,0x000b000e,0x00070009,
0x00050006,0x00030004,0x00020002,0x00010002,0x00010001,0x00000001,0x00000000,0x00000000,0x00000000,0x00000000,0x00000000,0x00000000,
0x00000000,0x00000000,0x00000000,0x00000000,0x00000000,0x00000000,0x00000000,0x00000000,0x00000000,0x00000000,0x00000000,0x00000000,
0x00000000,0x00000000,0x00000000,0x00000000,0x00000000,0x00000000,0x00000000,0x00000000,0x00000000,0x00000000,0x00000000,0x00000000,
0x00000000,0x00000000,0x00000000,0x00000000,0x00000000,0x00000000,0x00000000,0x00000000,0x00000000,0x00000000,0x00000000,0x00000000,
0x00000000,0x00000000,0x00000000,0x00000000,0x00000000,0x00000000,0x00000000,0x00000000,0x00000000,0x00000000,0x00000000,0x00000000,
0x00000000,0x00000000,0x00000000,0x00000000,0x00000000,0x00000000,0x00000000,0x00000000,0x00000000,0x00000000,0x00000000,0x00000000,
0x00000000,0x00000000,0x00000000,0x00000000,0x00000000,0x00000000,0x00000000,0x00000000,0x00000000,0x00000000,0x00000000,0x00000000,
0x00000000,0x00000000,0x00000000,0x00000000,0x00000000,0x00000000,0x00000000,0x00000000,0x00000000,0x00000000,0x00000000,0x00000000,
0x00000000,0x00000000,0x00000000,0x00000000,0x00000000,0x00000000,0x00000000,0x00000000,0x00000000,0x00000000,0x00000000,0x00000000,
0x00000000,0x00000000,0x00000000,0x00000000,0x00000000,0x00000000,0x00000000,0x00000000,0x00000000,0x00000000,0x00000000,0x00000000,
0x00000000,0x00000000,0x00000000,0x00000000,0x00000000,0x00000000,0x00000000,0x00000000,0x00000000,0x00000000,0x00000000,0x00000000,
0x00000000,0x00000000,0x00000000,0x80008000,0x80008000,0x80008000,0x80008000,0x80008000,0x80008000,0x80008000,0x80008000,0x80008000,
0x80008000,0x80008000,0x80008000,0x80008000,0x80008000,0x80008000,0x80008000,0x80008000,0x80008000,0x80008000,0x80008000,0x80008000,
0x80008000,0x80008000,0x80008000,0x80008000,0x80008000,0x80008000,0x80008000,0x80008000,0x80008000,0x80008000,0x80008000,0x80008000,
0x80008000,0x80008000,0x80008000,0x80008000,0x80008000,0x80008000,0x80008000,0x80008000,0x80008000,0x80008000,0x80008000,0x80008000,
0x80008000,0x80008000,0x80008000,0x80008000,0x80008000,0x80008000,0x80008000,0x80008000,0x80008000,0x80008000,0x80008000,0x80008000,
0x80008000,0x80008000,0x80008000,0x80008000,0x80008000,0x80008000,0x80008000,0x80008000,0x80008000,0x80008000,0x80008000,0x80008000,
0x80008000,0x80008000,0x80008000,0x80008000,0x80008000,0x80008000,0x80008000,0x80008000,0x80008000,0x80008000,0x80008000,0x80008000,
0x80008000,0x80008000,0x80008000,0x80008000,0x80008000,0x80008000,0x80008000,0x80008000,0x80008000,0x80008000,0x80008000,0x80008000,
0x80008000,0x80008000,0x80008000,0x80008000,0x80008000,0x80008000,0x80008000,0x80008000,0x80008000,0x80008000,0x80008000,0x80008000,
0x80008000,0x80008000,0x80008000,0x80008000,0x80008000,0x80008000,0x80008000,0x80008000,0x80008000,0x80008000,0x80008000,0x80008000,
0x80008000,0x80008000,0x80008000,0x80008000,0x80008000,0x80008000,0x80008000,0x80008000,0x80008000,0x80008000,0x80008000,0x80008000,
0x80008000,0x80008000,0x80008000,0x80008000,0x80008000,0x80008000,0x80008000,0x80008000,0x80008000,0x80008000,0x80008000,0x80008000,
0x80008000,0x80008000,0x80008000,0x80008000,0x80008000,0x80008000,0x80008000,0x80008000,0x80008000,0x80008000,0x80008000,0x80008000,
0x80008000,0x80008000,0x80008000,0x80008000,0x80008000,0x80008000,0x80008000,0x80008000,0x80008000,0x80008000,0x80008000,0x80008000,
0x80008000,0x80008000,0x80008000,0x80008000,0x80008000,0x80008000,0x80008000,0x80008000,0x80008000,0x80008000,0x80008000,0x80008000,
0x80008000,0x80008000,0x80008000,0x80008000,0x80008000,0x80008000,0x80008000,0x80008000,0x80008000,0x80008000,0x80008000,0x80008000,
0x80008000,0x80008000,0x80008000,0x80008000,0x80008000,0x80008000,0x80008000,0x80008000,0x80008000,0x80008000,0x80008000,0x80008000,
0x80008000,0x80008000,0x80008000,0x80008000,0x80008000,0x80008000,0x80008000,0x80008000,0x80008000,0x80008000,0x80008000,0x80008000,
0x80008000,0x80008000,0x80008000,0x80008000,0x80008000,0x80008000,0x80008000,0x80008000,0x80008000,0x80008000,0x80008000,0x80008000,
0x80008000,0x80008000,0x80008000,0x80008000,0x80008000,0x80008000,0x80008000,0x80008000,0x80008000,0x80008000,0x80008000,0x80008000,
0x80008000,0x80008000,0x80008000,0x80008000,0x80008000,0x80008000,0x80008000,0x80008000,0x80008000,0x80008000,0x80008000,0x80008000,
0x80008000,0x80008000,0x80008000,0x80008000,0x80008000,0x80008000,0x80008000,0x80008000,0x80008000,0x80008000,0x80008000,0x80008000,
0x80008000,0x80008000,0x80008000,0x80008000,0x80008000,0x80008000,0x80008000,0x80008000,0x80008000,0x80008000,0x80008000,0x80008000,
0x80008000,0x80008000,0x80008000,0x80008000,0x80008000,0x80008000,0x80008000,0x80008000,0x80008000,0x80008000,0x80008000,0x80008000,
0x80008000,0x80008000,0x80008000,0x80008000,0x80008000,0x80008000,0x80008000,0x80008000,0x80008000,0x80008000,0x80008000,0x80008000,
0x80008000,0x80008000,0x80008000,0x80008000,0x80008000,0x80008000,0x80008000,0x80008000,0x80008000,0x80008000,0x80008000,0x80008000,
0x80008000,0x80008000,0x80008000,0x80008000,0x80008000,0x80008000,0x80008000,0x80008000,0x80008000,0x80008000,0x80008000,0x80008000,
0x80008000,0x80008000,0x80008000,0x80008000,0x80008000,0x80008000,0x80008000,0x80008000,0x80008000,0x80008000,0x80008000,0x80008000,
0x80008000,0x80008000,0x80008000,0x80008000,0x80008000,0x80008000,0x80008000,0x80008000,0x80008000,0x80008000,0x80008000,0x80008000,
0x80008000,0x80008000,0x80008000,0x80008000,0x80008000,0x80008000,0x80008000,0x80008000,0x80008000,0x80008000,0x80008000,0x80008000,
0x80008000,0x80008000,0x80008000,0x80008000,0x80008000,0x80008000,0x80008000,0x80008000,0x80008000,0x80008000,0x80008000,0x80008000,
0x80068002,0x800e800a,0x80168012,0x801e801a,0x80268022,0x802e802a,0x80368032,0x803e803a,0x80468042,0x804e804a,0x80568052,0x805e805a,
0x80668062,0x806e806a,0x80768072,0x807e807a,0x80868082,0x808e808a,0x80968092,0x809e809a,0x80a680a2,0x80ae80aa,0x80b680b2,0x80be80ba,
0x80c680c2,0x80ce80ca,0x80d680d2,0x80de80da,0x80e680e2,0x80ee80ea,0x80f680f2,0x80fe80fa,0x81068102,0x810e810a,0x81168112,0x811e811a,
0x81268122,0x812e812a,0x81368132,0x813e813a,0x81468142,0x814e814a,0x81568152,0x815e815a,0x81668162,0x816e816a,0x81768172,0x817e817a,
0x81868182,0x818e818a,0x81968192,0x819e819a,0x81a681a2,0x81ae81aa,0x81b681b2,0x81be81ba,0x81c681c2,0x81ce81ca,0x81d681d2,0x81de81da,
0x81e681e2,0x81ee81ea,0x81f681f2,0x81fe81fa,0x82088204,0x8210820c,0x82188214,0x8220821c,0x82288224,0x8230822c,0x82388234,0x8240823c,
0x82488244,0x8250824c,0x82588254,0x8260825c,0x82688264,0x8270826c,0x82788274,0x8280827c,0x82888284,0x8290828c,0x82988294,0x82a0829c,
0x82a882a4,0x82b082ac,0x82b882b4,0x82c082bc,0x82c882c4,0x82d082cc,0x82d882d4,0x82e082dc,0x82e882e4,0x82f082ec,0x82f882f4,0x830082fc,
0x83088304,0x8310830c,0x83188314,0x8320831c,0x83288324,0x8330832c,0x83388334,0x8340833c,0x83488344,0x8350834c,0x83588354,0x8360835c,
0x83688364,0x8370836c,0x83788374,0x8380837c,0x83888384,0x8390838c,0x83988394,0x83a0839c,0x83a883a4,0x83b083ac,0x83b883b4,0x83c083bc,
0x83c883c4,0x83d083cc,0x83d883d4,0x83e083dc,0x83e883e4,0x83f083ec,0x83f883f4,0x840083fc,0x84108408,0x84208418,0x84308428,0x84408438,
0x84508448,0x84608458,0x84708468,0x84808478,0x84908488,0x84a08498,0x84b084a8,0x84c084b8,0x84d084c8,0x84e084d8,0x84f084e8,0x850084f8,
0x85108508,0x85208518,0x85308528,0x85408538,0x85508548,0x85608558,0x85708568,0x85808578,0x85908588,0x85a08598,0x85b085a8,0x85c085b8,
0x85d085c8,0x85e085d8,0x85f085e8,0x860085f8,0x86108608,0x86208618,0x86308628,0x86408638,0x86508648,0x86608658,0x86708668,0x86808678,
0x86908688,0x86a08698,0x86b086a8,0x86c086b8,0x86d086c8,0x86e086d8,0x86f086e8,0x870086f8,0x87108708,0x87208718,0x87308728,0x87408738,
0x87508748,0x87608758,0x87708768,0x87808778,0x87908788,0x87a08798,0x87b087a8,0x87c087b8,0x87d087c8,0x87e087d8,0x87f087e8,0x880087f8,
0x88108808,0x88208818,0x88308828,0x88408838,0x88508848,0x88608858,0x88708868,0x88808878,0x88908888,0x88a08898,0x88b088a8,0x88c088b8,
0x88d088c8,0x88e088d8,0x88f088e8,0x890088f8,0x89108908,0x89208918,0x89308928,0x89408938,0x89508948,0x89608958,0x89708968,0x89808978,
0x89908988,0x89a08998,0x89b089a8,0x89c089b8,0x89d089c8,0x89e089d8,0x89f089e8,0x8a0089f8,0x8a108a08,0x8a208a18,0x8a308a28,0x8a408a38,
0x8a508a48,0x8a608a58,0x8a708a68,0x8a808a78,0x8a908a88,0x8aa08a98,0x8ab08aa8,0x8ac08ab8,0x8ad08ac8,0x8ae08ad8,0x8af08ae8,0x8b008af8,
0x8b108b08,0x8b208b18,0x8b308b28,0x8b408b38,0x8b508b48,0x8b608b58,0x8b708b68,0x8b808b78,0x8b908b88,0x8ba08b98,0x8bb08ba8,0x8bc08bb8,
0x8bd08bc8,0x8be08bd8,0x8bf08be8,0x8c008bf8,0x8c108c08,0x8c208c18,0x8c308c28,0x8c408c38,0x8c508c48,0x8c608c58,0x8c708c68,0x8c808c78,
0x8c908c88,0x8ca08c98,0x8cb08ca8,0x8cc08cb8,0x8cd08cc8,0x8ce08cd8,0x8cf08ce8,0x8d008cf8,0x8d108d08,0x8d208d18,0x8d308d28,0x8d408d38,
0x8d508d48,0x8d608d58,0x8d708d68,0x8d808d78,0x8d908d88,0x8da08d98,0x8db08da8,0x8dc08db8,0x8dd08dc8,0x8de08dd8,0x8df08de8,0x8e008df8,
0x8e108e08,0x8e208e18,0x8e308e28,0x8e408e38,0x8e508e48,0x8e608e58,0x8e708e68,0x8e808e78,0x8e908e88,0x8ea08e98,0x8eb08ea8,0x8ec08eb8,
0x8ed08ec8,0x8ee08ed8,0x8ef08ee8,0x8f008ef8,0x8f108f08,0x8f208f18,0x8f308f28,0x8f408f38,0x8f508f48,0x8f608f58,0x8f708f68,0x8f808f78,
0x8f908f88,0x8fa08f98,0x8fb08fa8,0x8fc08fb8,0x8fd08fc8,0x8fe08fd8,0x8ff08fe8,0x90008ff8,0x90109008,0x90209018,0x90309028,0x90409038,
0x90509048,0x90609058,0x90709068,0x90809078,0x90909088,0x90a09098,0x90b090a8,0x90c090b8,0x90d090c8,0x90e090d8,0x90f090e8,0x910090f8,
0x91109108,0x91209118,0x91309128,0x91409138,0x91509148,0x91609158,0x91709168,0x91809178,0x91909188,0x91a09198,0x91b091a8,0x91c091b8,
0x91d091c8,0x91e091d8,0x91f091e8,0x920091f8,0x92109208,0x92209218,0x92309228,0x92409238,0x92509248,0x92609258,0x92709268,0x92809278,
0x92909288,0x92a09298,0x92b092a8,0x92c092b8,0x92d092c8,0x92e092d8,0x92f092e8,0x930092f8,0x93109308,0x93209318,0x93309328,0x93409338,
0x93509348,0x93609358,0x93709368,0x93809378,0x93909388,0x93a09398,0x93b093a8,0x93c093b8,0x93d093c8,0x93e093d8,0x93f093e8,0x940093f8,
0x94109408,0x94209418,0x94309428,0x94409438,0x94509448,0x94609458,0x94709468,0x94809478,0x94909488,0x94a09498,0x94b094a8,0x94c094b8,
0x94d094c8,0x94e094d8,0x94f094e8,0x950094f8,0x95109508,0x95209518,0x95309528,0x95409538,0x95509548,0x95609558,0x95709568,0x95809578,
0x95909588,0x95a09598,0x95b095a8,0x95c095b8,0x95d095c8,0x95e095d8,0x95f095e8,0x960095f8,0x96109608,0x96209618,0x96389630,0x96489640,
0x96589650,0x96689660,0x96789670,0x96889680,0x96989690,0x96a896a0,0x96b896b0,0x96c896c0,0x96d896d0,0x96e896e0,0x96f896f0,0x97089700,
0x97189710,0x97289720,0x97389730,0x97489740,0x97589750,0x97689760,0x97789770,0x97889780,0x97989790,0x97a897a0,0x97b897b0,0x97c897c0,
0x97d897d0,0x97e897e0,0x97f897f0,0x98089800,0x98109808,0x98209818,0x98309828,0x98409838,0x98509848,0x98689858,0x98789870,0x98889880,
0x98989890,0x98a898a0,0x98b898b0,0x98c898c0,0x98d898d0,0x98e898e0,0x98f898f0,0x99089900,0x99189910,0x99289920,0x99389930,0x99489940,
0x99589950,0x99689960,0x99789970,0x99889980,0x99989990,0x99a899a0,0x99b899b0,0x99c899c0,0x99d899d0,0x99e899e0,0x99f899f0,0x9a089a00,
0x9a189a10,0x9a289a20,0x9a409a38,0x9a509a48,0x9a609a58,0x9a709a68,0x9a809a78,0x9a909a88,0x9aa09a98,0x9ab09aa8,0x9ac09ab8,0x9ad09ac8,
0x9ae09ad8,0x9af09ae8,0x9b009af8,0x9b109b08,0x9b209b18,0x9b309b28,0x9b409b38,0x9b509b48,0x9b609b58,0x9b709b68,0x9b809b78,0x9b909b88,
0x9ba89ba0,0x9bb89bb0,0x9bc89bc0,0x9bd89bd0,0x9be89be0,0x9bf89bf0,0x9c009c00,0x9c089c08,0x9c189c10,0x9c289c20,0x9c389c30,0x9c489c40,
0x9c589c50,0x9c709c60,0x9c809c78,0x9c909c88,0x9ca09c98,0x9cb09ca8,0x9cc09cb8,0x9cd09cc8,0x9ce09cd8,0x9cf09ce8,0x9d009cf8,0x9d109d08,
0x9d209d18,0x9d309d28,0x9d409d38,0x9d509d48,0x9d609d58,0x9d789d70,0x9d889d80,0x9d989d90,0x9da89da0,0x9db89db0,0x9dc89dc0,0x9dd89dd0,
0x9de89de0,0x9df89df0,0x9e089e00,0x9e189e10,0x9e289e20,0x9e389e30,0x9e509e48,0x9e609e58,0x9e709e68,0x9e809e78,0x9e909e88,0x9ea09e98,
0x9eb09ea8,0x9ec09eb8,0x9ed09ec8,0x9ee09ed8,0x9ef09ee8,0x9f089ef8,0x9f189f10,0x9f289f20,0x9f389f30,0x9f489f40,0x9f589f50,0x9f689f60,
0x9f789f70,0x9f889f80,0x9f989f90,0x9fa89fa0,0x9fc09fb8,0x9fd09fc8,0x9fe09fd8,0x9ff09fe8,0xa0009ff8,0xa008a008,0xa010a010,0xa018a018,
0xa028a020,0xa038a030,0xa048a040,0xa058a050,0xa068a060,0xa080a070,0xa090a088,0xa0a0a098,0xa0b0a0a8,0xa0c0a0b8,0xa0d0a0c8,0xa0e0a0d8,
0xa0f0a0e8,0xa108a0f8,0xa118a110,0xa128a120,0xa138a130,0xa148a140,0xa158a150,0xa168a160,0xa178a170,0xa190a188,0xa1a0a198,0xa1b0a1a8,
0xa1c0a1b8,0xa1d0a1c8,0xa1e0a1d8,0xa1f0a1e8,0xa208a200,0xa218a210,0xa228a220,0xa238a230,0xa248a240,0xa258a250,0xa270a268,0xa280a278,
0xa290a288,0xa2a0a298,0xa2b0a2a8,0xa2c0a2b8,0xa2d8a2d0,0xa2e8a2e0,0xa2f8a2f0,0xa308a300,0xa318a310,0xa330a320,0xa340a338,0xa350a348,
0xa360a358,0xa370a368,0xa380a378,0xa398a390,0xa3a8a3a0,0xa3b8a3b0,0xa3c8a3c0,0xa3d8a3d0,0xa3f0a3e8,0xa400a3f8,0xa408a400,0xa410a410,
0xa418a418,0xa420a420,0xa428a428,0xa438a430,0xa440a438,0xa458a450,0xa468a460,0xa478a470,0xa488a480,0xa4a0a490,0xa4b0a4a8,0xa4c0a4b8,
0xa4d0a4c8,0xa4e8a4d8,0xa4f8a4f0,0xa508a500,0xa518a510,0xa530a520,0xa540a538,0xa550a548,0xa560a558,0xa578a568,0xa588a580,0xa598a590,
0xa5a8a5a0,0xa5c0a5b8,0xa5d0a5c8,0xa5e0a5d8,0xa5f8a5e8,0xa608a600,0xa618a610,0xa628a620,0xa640a638,0xa650a648,0xa660a658,0xa678a670,
0xa688a680,0xa698a690,0xa6b0a6a8,0xa6c0a6b8,0xa6d0a6c8,0xa6e8a6e0,0xa6f8a6f0,0xa708a700,0xa720a718,0xa730a728,0xa740a738,0xa758a750,
0xa768a760,0xa778a770,0xa790a788,0xa7a0a798,0xa7b8a7a8,0xa7c8a7c0,0xa7d8a7d0,0xa7f0a7e8,0xa800a7f8,0xa808a808,0xa810a810,0xa820a818,
0xa828a820,0xa830a828,0xa838a838,0xa840a840,0xa850a848,0xa858a850,0xa860a858,0xa868a868,0xa878a870,0xa890a888,0xa8a0a898,0xa8b8a8a8,
0xa8c8a8c0,0xa8e0a8d0,0xa8f0a8e8,0xa908a8f8,0xa918a910,0xa930a920,0xa940a938,0xa958a948,0xa968a960,0xa980a970,0xa990a988,0xa9a8a998,
0xa9b8a9b0,0xa9d0a9c0,0xa9e0a9d8,0xa9f8a9e8,0xaa08aa00,0xaa20aa18,0xaa30aa28,0xaa48aa40,0xaa60aa50,0xaa70aa68,0xaa88aa78,0xaa98aa90,
0xaab0aaa8,0xaac8aab8,0xaad8aad0,0xaaf0aae0,0xab00aaf8,0xab18ab10,0xab30ab20,0xab40ab38,0xab58ab50,0xab70ab60,0xab80ab78,0xab98ab90,
0xabb0aba0,0xabc0abb8,0xabd8abd0,0xabf0abe0,0xac00abf8,0xac10ac08,0xac18ac10,0xac20ac20,0xac30ac28,0xac38ac30,0xac40ac40,0xac50ac48,
0xac58ac58,0xac68ac60,0xac70ac68,0xac78ac78,0xac88ac80,0xac90ac88,0xaca0ac98,0xaca8aca0,0xacb0acb0,0xacc0acb8,0xacc8acc0,0xacd8acd0,
0xace8ace0,0xad00acf0,0xad18ad08,0xad30ad20,0xad48ad38,0xad58ad50,0xad70ad68,0xad88ad80,0xada0ad98,0xadb8adb0,0xadd0adc8,0xade8ade0,
0xae00adf8,0xae18ae10,0xae30ae28,0xae48ae40,0xae60ae58,0xae78ae70,0xae90ae88,0xaea8aea0,0xaec0aeb8,0xaed8aed0,0xaef0aee8,0xaf10af00,
0xaf28af18,0xaf40af30,0xaf58af48,0xaf70af68,0xaf88af80,0xafa8af98,0xafc0afb0,0xafd8afc8,0xaff0afe8,0xb008b000,0xb010b008,0xb020b018,
0xb030b028,0xb038b030,0xb048b040,0xb050b050,0xb060b058,0xb070b068,0xb078b078,0xb088b080,0xb098b090,0xb0a0b0a0,0xb0b0b0a8,0xb0c0b0b8,
0xb0d0b0c8,0xb0d8b0d0,0xb0e8b0e0,0xb0f8b0f0,0xb100b0f8,0xb110b108,0xb120b118,0xb130b128,0xb138b130,0xb148b140,0xb158b150,0xb168b160,
0xb170b168,0xb180b178,0xb190b188,0xb1a0b198,0xb1b8b1a8,0xb1d0b1c0,0xb1f0b1e0,0xb208b200,0xb228b218,0xb248b238,0xb260b258,0xb280b270,
0xb2a0b290,0xb2c0b2b0,0xb2d8b2c8,0xb2f8b2e8,0xb318b308,0xb338b328,0xb350b348,0xb370b360,0xb390b380,0xb3b0b3a0,0xb3d0b3c0,0xb3f0b3e0,
0xb408b400,0xb418b410,0xb428b420,0xb438b430,0xb448b440,0xb458b450,0xb468b460,0xb478b470,0xb488b480,0xb498b490,0xb4a8b4a0,0xb4b8b4b0,
0xb4c8b4c0,0xb4d8b4d0,0xb4e8b4e0,0xb4f8b4f0,0xb508b500,0xb518b510,0xb528b520,0xb538b530,0xb548b540,0xb560b550,0xb570b568,0xb580b578,
0xb590b588,0xb5a0b598,0xb5b0b5a8,0xb5c0b5b8,0xb5d0b5c8,0xb5e0b5d8,0xb5f8b5f0,0xb608b600,0xb618b610,0xb628b620,0xb638b630,0xb648b640,
0xb660b650,0xb670b668,0xb680b678,0xb690b688,0xb6a0b698,0xb6b0b6a8,0xb6c8b6c0,0xb6d8b6d0,0xb6f8b6e0,0xb718b708,0xb738b728,0xb760b750,
0xb780b770,0xb7a8b798,0xb7c8b7b8,0xb7f0b7d8,0xb808b800,0xb818b810,0xb830b820,0xb840b838,0xb850b848,0xb860b858,0xb878b868,0xb888b880,
0xb898b890,0xb8a8b8a0,0xb8c0b8b0,0xb8d0b8c8,0xb8e0b8d8,0xb8f0b8e8,0xb908b900,0xb918b910,0xb928b920,0xb938b930,0xb950b948,0xb960b958,
0xb970b968,0xb980b978,0xb998b990,0xb9a8b9a0,0xb9b8b9b0,0xb9c8b9c0,0xb9e0b9d8,0xb9f0b9e8,0xba00b9f8,0xba10ba08,0xba28ba18,0xba38ba30,
0xba48ba40,0xba58ba50,0xba68ba60,0xba80ba78,0xba90ba88,0xbaa0ba98,0xbab0baa8,0xbac8bab8,0xbad8bad0,0xbae8bae0,0xbaf8baf0,0xbb08bb00,
0xbb18bb10,0xbb30bb28,0xbb40bb38,0xbb50bb48,0xbb60bb58,0xbb70bb68,0xbb88bb78,0xbb98bb90,0xbba8bba0,0xbbb8bbb0,0xbbc8bbc0,0xbbd8bbd0,
0xbbf8bbe8,0xbc10bc08,0xbc20bc18,0xbc30bc28,0xbc40bc38,0xbc50bc48,0xbc60bc58,0xbc70bc68,0xbc80bc78,0xbc90bc88,0xbca8bca0,0xbcb8bcb0,
0xbcc8bcc0,0xbcd8bcd0,0xbce8bce0,0xbcf8bcf0,0xbd08bd00,0xbd18bd10,0xbd28bd20,0xbd38bd30,0xbd48bd40,0xbd58bd50,0xbd68bd60,0xbd78bd70,
0xbd88bd80,0xbd98bd90,0xbda8bda0,0xbdb8bdb0,0xbdc8bdc0,0xbdd8bdd0,0xbde8bde0,0xbdf8bdf0,0xbe08be00,0xbe18be10,0xbe28be20,0xbe38be30,
0xbe48be40,0xbe58be50,0xbe68be60,0xbe78be70,0xbe88be80,0xbe98be90,0xbea8bea0,0xbeb8beb0,0xbec8bec0,0xbed8bed0,0xbee8bee0,0xbef8bef0,
0xbf08bf00,0xbf18bf10,0xbf28bf20,0xbf38bf30,0xbf48bf40,0xbf58bf50,0xbf68bf60,0xbf78bf70,0xbf88bf80,0xbf98bf90,0xbfa8bfa0,0xbfb8bfb0,
0xbfc8bfc0,0xbfd8bfd0,0xbfe8bfe0,0xbff8bff0,0xc008c000,0xc018c010,0xc028c020,0xc038c030,0xc048c040,0xc058c050,0xc068c060,0xc078c070,
0xc088c080,0xc098c090,0xc0b0c0a0,0xc0c0c0b0,0xc0d0c0c0,0xc0e0c0d0,0xc0f0c0e0,0xc100c0f0,0xc110c100,0xc120c110,0xc130c120,0xc140c130,
0xc150c140,0xc160c150,0xc170c160,0xc180c170,0xc190c180,0xc1a0c190,0xc1b0c1a0,0xc1c0c1b0,0xc1d0c1c0,0xc1e0c1d0,0xc1f0c1e0,0xc200c1f0,
0xc210c200,0xc220c210,0xc230c220,0xc240c230,0xc250c240,0xc260c250,0xc270c260,0xc280c270,0xc290c280,0xc2a0c290,0xc2b0c2a0,0xc2c0c2b0,
0xc2d0c2c0,0xc2e0c2d0,0xc2f0c2e0,0xc300c2f0,0xc310c300,0xc320c310,0xc330c320,0xc340c330,0xc350c340,0xc360c350,0xc370c360,0xc380c370,
0xc390c380,0xc3a0c390,0xc3b0c3a0,0xc3c0c3b0,0xc3d0c3c0,0xc3e0c3d0,0xc3f0c3e0,0xc400c3f0,0xc410c400,0xc420c410,0xc430c420,0xc440c430,
0xc450c440,0xc460c450,0xc470c460,0xc480c470,0xc490c480,0xc4a0c490,0xc4b0c4a0,0xc4c0c4b0,0xc4d0c4c0,0xc4e0c4d0,0xc4f0c4e0,0xc500c4f0,
0xc510c500,0xc520c510,0xc530c520,0xc540c530,0xc550c540,0xc560c550,0xc570c560,0xc580c570,0xc590c580,0xc5a0c590,0xc5b0c5a0,0xc5c0c5b0,
0xc5d0c5c0,0xc5e0c5d0,0xc5f0c5e0,0xc600c5f0,0xc610c600,0xc620c610,0xc630c620,0xc640c630,0xc650c640,0xc660c650,0xc670c660,0xc680c670,
0xc690c680,0xc6a0c690,0xc6b0c6a0,0xc6c0c6b0,0xc6d0c6c0,0xc6e0c6d0,0xc6f0c6e0,0xc700c6f0,0xc710c700,0xc720c710,0xc730c720,0xc740c730,
0xc750c740,0xc760c750,0xc770c760,0xc780c770,0xc790c780,0xc7a0c790,0xc7b0c7a0,0xc7c0c7b0,0xc7d0c7c0,0xc7e0c7d0,0xc7f0c7e0,0xc800c7f0,
0xc810c800,0xc820c810,0xc830c820,0xc840c830,0xc850c840,0xc860c850,0xc870c860,0xc880c870,0xc890c880,0xc8a0c890,0xc8b0c8a0,0xc8c0c8b0,
0xc8d0c8c0,0xc8e0c8d0,0xc8f0c8e0,0xc900c8f0,0xc910c900,0xc920c910,0xc930c920,0xc940c930,0xc950c940,0xc960c950,0xc970c960,0xc980c970,
0xc990c980,0xc9a0c990,0xc9b0c9a0,0xc9c0c9b0,0xc9d0c9c0,0xc9e0c9d0,0xc9f0c9e0,0xca00c9f0,0xca10ca00,0xca20ca10,0xca30ca20,0xca40ca30,
0xca50ca40,0xca60ca50,0xca70ca60,0xca80ca70,0xca90ca80,0xcaa0ca90,0xcab0caa0,0xcac0cab0,0xcad0cac0,0xcae0cad0,0xcaf0cae0,0xcb00caf0,
0xcb10cb00,0xcb20cb10,0xcb30cb20,0xcb40cb30,0xcb50cb40,0xcb60cb50,0xcb70cb60,0xcb80cb70,0xcb90cb80,0xcba0cb90,0xcbb0cba0,0xcbc0cbb0,
0xcbd0cbc0,0xcbe0cbd0,0xcbf0cbe0,0xcc00cbf0,0xcc10cc00,0xcc20cc10,0xcc30cc20,0xcc40cc30,0xcc50cc40,0xcc60cc50,0xcc70cc60,0xcc80cc70,
0xcc90cc80,0xcca0cc90,0xccb0cca0,0xccc0ccb0,0xccd0ccc0,0xcce0ccd0,0xccf0cce0,0xcd00ccf0,0xcd10cd00,0xcd20cd10,0xcd30cd20,0xcd40cd30,
0xcd50cd40,0xcd60cd50,0xcd70cd60,0xcd80cd70,0xcd90cd80,0xcda0cd90,0xcdb0cda0,0xcdc0cdb0,0xcdd0cdc0,0xcde0cdd0,0xcdf0cde0,0xce00cdf0,
0xce10ce00,0xce20ce10,0xce30ce20,0xce40ce30,0xce50ce40,0xce60ce50,0xce70ce60,0xce80ce70,0xce90ce80,0xcea0ce90,0xceb0cea0,0xcec0ceb0,
0xced0cec0,0xcee0ced0,0xcef0cee0,0xcf00cef0,0xcf10cf00,0xcf20cf10,0xcf30cf20,0xcf40cf30,0xcf50cf40,0xcf60cf50,0xcf70cf60,0xcf80cf70,
0xcf90cf80,0xcfa0cf90,0xcfb0cfa0,0xcfc0cfb0,0xcfd0cfc0,0xcfe0cfd0,0xcff0cfe0,0xd000cff0,0xd010d000,0xd020d010,0xd030d020,0xd040d030,
0xd050d040,0xd060d050,0xd070d060,0xd080d070,0xd090d080,0xd0a0d090,0xd0b0d0a0,0xd0c0d0b0,0xd0d0d0c0,0xd0e0d0d0,0xd0f0d0e0,0xd100d0f0,
0xd110d100,0xd120d110,0xd130d120,0xd140d130,0xd150d140,0xd160d150,0xd170d160,0xd180d170,0xd190d180,0xd1a0d190,0xd1b0d1a0,0xd1c0d1b0,
0xd1d0d1c0,0xd1e0d1d0,0xd1f0d1e0,0xd200d1f0,0xd210d200,0xd220d210,0xd230d220,0xd240d230,0xd250d240,0xd260d250,0xd270d260,0xd280d270,
0xd290d280,0xd2a0d290,0xd2b0d2a0,0xd2c0d2b0,0xd2d0d2c0,0xd2e0d2d0,0xd2f0d2e0,0xd300d2f0,0xd310d300,0xd320d310,0xd330d320,0xd340d330,
0xd350d340,0xd360d350,0xd370d360,0xd380d370,0xd390d380,0xd3a0d390,0xd3b0d3a0,0xd3c0d3b0,0xd3d0d3c0,0xd3e0d3d0,0xd3f0d3e0,0xd400d3f0,
0xd410d400,0xd420d410,0xd430d420,0xd440d430,0xd450d440,0xd460d450,0xd470d460,0xd480d470,0xd490d480,0xd4a0d490,0xd4b0d4a0,0xd4c0d4b0,
0xd4d0d4c0,0xd4e0d4d0,0xd4f0d4e0,0xd500d4f0,0xd510d500,0xd520d510,0xd530d520,0xd540d530,0xd550d540,0xd560d550,0xd570d560,0xd580d570,
0xd590d580,0xd5a0d590,0xd5b0d5a0,0xd5c0d5b0,0xd5d0d5c0,0xd5e0d5d0,0xd5f0d5e0,0xd600d5f0,0xd610d600,0xd620d610,0xd630d620,0xd640d630,
0xd650d640,0xd660d650,0xd670d660,0xd680d670,0xd690d680,0xd6a0d690,0xd6b0d6a0,0xd6c0d6b0,0xd6d0d6c0,0xd6e0d6d0,0xd6f0d6e0,0xd700d6f0,
0xd710d700,0xd720d710,0xd730d720,0xd740d730,0xd750d740,0xd760d750,0xd770d760,0xd780d770,0xd790d780,0xd7a0d790,0xd7b0d7a0,0xd7c0d7b0,
0xd7d0d7c0,0xd7e0d7d0,0xd7f0d7e0,0xd800d7f0,0xd810d800,0xd820d810,0xd830d820,0xd840d830,0xd850d840,0xd860d850,0xd870d860,0xd880d870,
0xd890d880,0xd8a0d890,0xd8b0d8a0,0xd8c0d8b0,0xd8d0d8c0,0xd8e0d8d0,0xd8f0d8e0,0xd900d8f0,0xd910d900,0xd920d910,0xd930d920,0xd940d930,
0xd950d940,0xd960d950,0xd970d960,0xd980d970,0xd990d980,0xd9a0d990,0xd9b0d9a0,0xd9c0d9b0,0xd9d0d9c0,0xd9e0d9d0,0xd9f0d9e0,0xda00d9f0,
0xda10da00,0xda20da10,0xda30da20,0xda40da30,0xda50da40,0xda60da50,0xda70da60,0xda80da70,0xda90da80,0xdaa0da90,0xdab0daa0,0xdac0dab0,
0xdad0dac0,0xdae0dad0,0xdaf0dae0,0xdb00daf0,0xdb10db00,0xdb20db10,0xdb30db20,0xdb40db30,0xdb50db40,0xdb60db50,0xdb70db60,0xdb80db70,
0xdb90db80,0xdba0db90,0xdbb0dba0,0xdbc0dbb0,0xdbd0dbc0,0xdbe0dbd0,0xdbf0dbe0,0xdc00dbf0,0xdc10dc00,0xdc20dc10,0xdc30dc20,0xdc40dc30,
0xdc50dc40,0xdc60dc50,0xdc70dc60,0xdc80dc70,0xdc90dc80,0xdca0dc90,0xdcb0dca0,0xdcc0dcb0,0xdcd0dcc0,0xdce0dcd0,0xdcf0dce0,0xdd00dcf0,
0xdd10dd00,0xdd20dd10,0xdd30dd20,0xdd40dd30,0xdd50dd40,0xdd60dd50,0xdd70dd60,0xdd80dd70,0xdd90dd80,0xdda0dd90,0xddb0dda0,0xddc0ddb0,
0xddd0ddc0,0xdde0ddd0,0xddf0dde0,0xde00ddf0,0xde10de00,0xde20de10,0xde30de20,0xde40de30,0xde50de40,0xde60de50,0xde70de60,0xde80de70,
0xde90de80,0xdea0de90,0xdeb0dea0,0xdec0deb0,0xded0dec0,0xdee0ded0,0xdef0dee0,0xdf00def0,0xdf10df00,0xdf20df10,0xdf30df20,0xdf40df30,
0xdf50df40,0xdf60df50,0xdf70df60,0xdf80df70,0xdf90df80,0xdfa0df90,0xdfb0dfa0,0xdfc0dfb0,0xdfd0dfc0,0xdfe0dfd0,0xdff0dfe0,0xe000dff0
};

static __device__ __forceinline__ float bf2f(unsigned short v) { return __uint_as_float(((unsigned)v) << 16); }
static __device__ __forceinline__ float wsum(float v) {
#pragma unroll
  for (int o = 32; o > 0; o >>= 1) v += __shfl_xor(v, o);
  return v;
}
static __device__ __forceinline__ void slow_hidden(const unsigned short* x, const unsigned short* dw, const unsigned short* w1, float* ln,
                                                   int py, int px, int lane, float (&h)[6]) {
  float a0 = 0.f, a1 = 0.f;
  const bool two = lane < 32;
  for (int i = 0; i < 3; ++i)
    for (int j = 0; j < 3; ++j) {
      const int yy = py + i - 1, xx = px + j - 1;
      if (yy < 0 || yy >= 512 || xx < 0 || xx >= 512) continue;
      const unsigned short* p = x + ((size_t)yy * 512 + xx) * 96;
      const unsigned short* q = dw + (i * 3 + j) * 96;
      a0 = fmaf(bf2f(p[lane]), bf2f(q[lane]), a0);
      if (two) a1 = fmaf(bf2f(p[64 + lane]), bf2f(q[64 + lane]), a1);
    }
  const float m = wsum(a0 + a1) * (1.f / 96.f);
  const float d0 = a0 - m, d1 = two ? a1 - m : 0.f;
  const float rs = 1.f / sqrtf(wsum(d0 * d0 + d1 * d1) * (1.f / 96.f) + 1e-5f);
  __builtin_amdgcn_wave_barrier();
  ln[lane] = d0 * rs;
  if (two) ln[64 + lane] = d1 * rs;
  __builtin_amdgcn_fence(__ATOMIC_SEQ_CST, "workgroup");
#pragma unroll
  for (int i = 0; i < 6; ++i) h[i] = 0.f;
  for (int c = 0; c < 96; ++c) {
    const float v = ln[c];
#pragma unroll
    for (int i = 0; i < 6; ++i) h[i] = fmaf(v, bf2f(w1[c * 384 + lane + 64 * i]), h[i]);
  }
#pragma unroll
  for (int i = 0; i < 6; ++i) {
    const float t = h[i];
    h[i] = 0.5f * t * (1.f + tanhf(0.7978845608028654f * (t + 0.044715f * t * t * t)));
  }
  __builtin_amdgcn_fence(__ATOMIC_SEQ_CST, "workgroup");
}

__global__ void __launch_bounds__(512, 2) convnext_fused(const void* x_, const void* dw_, const void* w1_, const void* w2_,
                                                         const void* gamma_, const void* beta_, void* out_, void* ws_) {
  extern __shared__ __attribute__((aligned(16))) unsigned char smem[];
  const int w = __builtin_amdgcn_readfirstlane(threadIdx.x >> 6);
  const int b = blockIdx.x, idx = b >> 3;
  const int X0 = (idx & 15) * 32, Y0 = ((b & 7) * 2 + (idx >> 4)) * 32;
  unsigned status, tag;
  void* const __attribute__((address_space(4)))* ka = (void* const __attribute__((address_space(4)))*)__builtin_amdgcn_kernarg_segment_ptr();
  asm volatile(
  "s_load_dwordx16 s[8:23], %2, 0x0\n"
  "s_cmp_lg_u32 %4, 0\n"
  "s_cbranch_scc1 .Lnotop%=\n"
  "s_getpc_b64 s[76:77]\n"
  "s_mov_b32 s78, 5120\n"
  "s_mov_b32 s79, 0x20000\n"
  "v_mbcnt_lo_u32_b32 v11, -1, 0\n"
  "v_mbcnt_hi_u32_b32 v11, -1, v11\n"
  "v_lshlrev_b32 v11, 7, v11\n"
  "buffer_load_dword v11, v11, s[76:79], 0 offen\n"
  ".Lnotop%=:\n"
  "s_mov_b32 s71, m0\n"
  "s_mov_b32 s72, %4\n"
  "s_and_b32 s73, s72, 3\n"
  "s_mov_b32 s75, %7\n"
  "s_mov_b32 s95, 0\n"
  "s_mov_b32 s69, 0x3000000\n"
  "v_mbcnt_lo_u32_b32 v8, -1, 0\n"
  "v_mbcnt_hi_u32_b32 v8, -1, v8\n"
  "v_lshlrev_b32 v0, 4, v8\n"
  "v_add_u32 v1, 0x10000, v0\n"
  "v_mov_b32 v9, 0\n"
  "s_sub_u32 s60, %6, 1\n"
  "s_mul_i32 s60, s60, 0x18000\n"
  "s_lshl_b32 s92, s73, 6\n"
  "s_add_u32 s86, s92, 256\n"
  "v_mov_b32 v19, 0x200\n"
  "v_add_u32 v12, s92, v8\n"
  "v_add_u32 v66, s86, v8\n"
  "v_mul_u32_u24 v13, 0x13b2, v12\n"
  "v_mul_u32_u24 v67, 0x13b2, v66\n"
  "v_lshrrev_b32 v13, 16, v13\n"
  "v_lshrrev_b32 v67, 16, v67\n"
  "v_mul_u32_u24 v14, 13, v13\n"
  "v_mul_u32_u24 v68, 13, v67\n"
  "v_sub_u32 v14, v12, v14\n"
  "v_sub_u32 v68, v66, v68\n"
  "v_add_u32 v12, %5, v13\n"
  "v_add_u32 v66, %5, v67\n"
  "v_mul_u32_u24 v15, 192, v12\n"
  "v_mul_u32_u24 v69, 192, v66\n"
  "v_lshl_add_u32 v15, v14, 4, v15\n"
  "v_lshl_add_u32 v69, v68, 4, v69\n"
  "v_mov_b32 v16, 0x7fffff00\n"
  "v_mov_b32 v70, 0x7fffff00\n"
  "v_cmp_gt_u32 s[84:85], 12, v14\n"
  "v_cmp_gt_u32 vcc, 12, v68\n"
  "s_nop 0\n"
  "v_cndmask_b32 v15, v16, v15, s[84:85]\n"
  "v_cndmask_b32 v69, v70, v69, vcc\n"
  "v_cmp_gt_u32 s[84:85], 34, v13\n"
  "v_cmp_gt_u32 vcc, 34, v67\n"
  "s_nop 0\n"
  "v_cndmask_b32 v15, v16, v15, s[84:85]\n"
  "v_cndmask_b32 v69, v70, v69, vcc\n"
  "v_cmp_gt_u32 s[84:85], v19, v12\n"
  "v_cmp_gt_u32 vcc, 0x200, v66\n"
  "s_nop 0\n"
  "v_cndmask_b32 v4, v16, v15, s[84:85]\n"
  "v_cndmask_b32 v6, v70, v69, vcc\n"
  "s_waitcnt lgkmcnt(0)\n"
  "s_mov_b64 s[40:41], s[8:9]\n"
  "s_mov_b32 s42, 0x3000000\n"
  "s_mov_b32 s43, 0x20000\n"
  "s_mov_b64 s[80:81], s[22:23]\n"
  "s_mov_b32 s82, 20480\n"
  "s_mov_b32 s83, 0x20000\n"
  "buffer_load_dword v100, v9, s[80:83], 0 offen offset:0 sc1\n"
  "s_cmp_ge_u32 s72, 4\n"
  "s_cbranch_scc1 .LproB%=\n"
  "s_mov_b64 s[76:77], s[10:11]\n"
  "s_mov_b32 s78, 1728\n"
  "s_mov_b32 s79, 0x20000\n"
  "s_mul_i32 s93, s73, 576\n"
  "v_lshlrev_b32 v20, 2, v8\n"
  "s_lshl_b32 s92, s73, 6\n"
  "s_mov_b32 s78, 1730\n"
  "v_and_b32 v24, 7, v8\n"
  "v_lshrrev_b32 v25, 3, v8\n"
  "v_lshlrev_b32 v20, 5, v25\n"
  "v_lshl_add_u32 v20, v24, 1, v20\n"
  "s_min_u32 s93, s93, 1152\n"
  "s_mov_b32 exec_hi, 0xffff\n"
  "buffer_load_ushort v70, v20, s[76:79], s93 offen offset:0\n"
  "buffer_load_ushort v71, v20, s[76:79], s93 offen offset:192\n"
  "buffer_load_ushort v72, v20, s[76:79], s93 offen offset:384\n"
  "buffer_load_ushort v73, v20, s[76:79], s93 offen offset:16\n"
  "buffer_load_ushort v74, v20, s[76:79], s93 offen offset:208\n"
  "buffer_load_ushort v75, v20, s[76:79], s93 offen offset:400\n"
  "s_mov_b64 exec, -1\n"
  "s_lshl_b32 s61, s73, 10\n"
  "s_add_u32 m0, s61, 114944\n"
  "s_cmp_lt_u32 s60, s69\n"
  "s_cselect_b32 s94, s60, s69\n"
  "buffer_load_dwordx4 v4, s[40:43], s94 offen  lds\n"
  "s_mov_b32 s94, s60\n"
  "s_add_u32 m0, m0, 7168\n"
  "s_add_u32 s94, s94, 0x18000\n"
  "buffer_load_dwordx4 v4, s[40:43], s94 offen  lds\n"
  "s_add_u32 m0, m0, 7168\n"
  "s_add_u32 s94, s94, 0x18000\n"
  "buffer_load_dwordx4 v4, s[40:43], s94 offen  lds\n"
  "s_add_u32 m0, m0, 7168\n"
  "s_add_u32 s94, s94, 0x18000\n"
  "buffer_load_dwordx4 v4, s[40:43], s94 offen  lds\n"
  "s_add_u32 m0, m0, 7168\n"
  "s_add_u32 s94, s94, 0x18000\n"
  "buffer_load_dwordx4 v4, s[40:43], s94 offen  lds\n"
  "s_add_u32 m0, m0, 7168\n"
  "s_add_u32 s94, s94, 0x18000\n"
  "buffer_load_dwordx4 v4, s[40:43], s94 offen  lds\n"
  "s_cmp_eq_u32 s73, 3\n"
  "s_cbranch_scc1 .LpA2%=\n"
  "s_lshl_b32 s61, s73, 10\n"
  "s_add_u32 m0, s61, 119040\n"
  "s_cmp_lt_u32 s60, s69\n"
  "s_cselect_b32 s94, s60, s69\n"
  "buffer_load_dwordx4 v6, s[40:43], s94 offen  lds\n"
  "s_mov_b32 s94, s60\n"
  "s_add_u32 m0, m0, 7168\n"
  "s_add_u32 s94, s94, 0x18000\n"
  "buffer_load_dwordx4 v6, s[40:43], s94 offen  lds\n"
  "s_add_u32 m0, m0, 7168\n"
  "s_add_u32 s94, s94, 0x18000\n"
  "buffer_load_dwordx4 v6, s[40:43], s94 offen  lds\n"
  "s_add_u32 m0, m0, 7168\n"
  "s_add_u32 s94, s94, 0x18000\n"
  "buffer_load_dwordx4 v6, s[40:43], s94 offen  lds\n"
  "s_add_u32 m0, m0, 7168\n"
  "s_add_u32 s94, s94, 0x18000\n"
  "buffer_load_dwordx4 v6, s[40:43], s94 offen  lds\n"
  "s_add_u32 m0, m0, 7168\n"
  "s_add_u32 s94, s94, 0x18000\n"
  "buffer_load_dwordx4 v6, s[40:43], s94 offen  lds\n"
  ".LpA2%=:\n"
  ".Lcom%=:\n"
  "s_sub_u32 s66, 7, s72\n"
  "s_lshl_b32 s67, s73, 10\n"
  "s_add_u32 s67, s67, 93440\n"
  "s_mov_b32 s56, 0x44ea44ea\n"
  "s_mov_b32 s63, 0x3c2aaaab\n"
  "s_mov_b32 s64, 0x3727c5ac\n"
  "s_mov_b32 s65, 0xbeefde6e\n"
  "s_mov_b32 s55, 4\n"
  "s_mov_b32 s70, 7\n"
  "v_lshrrev_b32 v80, 4, v8\n"
  "s_lshr_b32 s62, s72, 1\n"
  "s_lshl_b32 s62, s62, 1\n"
  "s_add_u32 s62, s62, 2\n"
  "v_add_u32 v81, s62, v80\n"
  "v_and_b32 v81, 7, v81\n"
  "v_mul_u32_u24 v81, 7168, v81\n"
  "v_and_b32 v82, 15, v8\n"
  "s_and_b32 s62, s72, 1\n"
  "s_lshl_b32 s62, s62, 4\n"
  "v_add_u32 v83, s62, v82\n"
  "v_mul_u32_u24 v83, 208, v83\n"
  "v_add_u32 v81, v81, v83\n"
  "v_add_u32 v2, 100608, v81\n"
  "v_bfe_u32 v83, v82, 2, 1\n"
  "v_sub_u32 v83, v80, v83\n"
  "v_lshrrev_b32 v84, 3, v82\n"
  "v_and_b32 v85, 3, v82\n"
  "v_lshl_add_u32 v85, v84, 2, v85\n"
  "v_lshl_add_u32 v85, v83, 3, v85\n"
  "v_add_u32 v85, 1, v85\n"
  "v_mul_u32_u24 v84, 25, v84\n"
  "v_cmp_gt_u32 vcc, 3, v83\n"
  "s_nop 1\n"
  "v_cndmask_b32 v85, v84, v85, vcc\n"
  "v_lshlrev_b32 v85, 4, v85\n"
  "v_add_u32 v3, 86016, v85\n"
  "v_and_b32 v84, 1, v80\n"
  "v_mul_u32_u24 v84, 112, v84\n"
  "v_lshlrev_b32 v84, 23, v84\n"
  "v_sub_u32 v5, s65, v84\n"
  "v_mov_b32 v246, 0\n"
  "v_mov_b32 v247, 0\n"
  "s_add_u32 s68, s60, 2850816\n"
  "v_mov_b64 v[104:105], 0\n"
  "v_mov_b64 v[106:107], 0\n"
  "v_mov_b64 v[108:109], 0\n"
  "v_mov_b64 v[110:111], 0\n"
  "v_mov_b64 v[112:113], 0\n"
  "v_mov_b64 v[114:115], 0\n"
  "v_mov_b64 v[116:117], 0\n"
  "v_mov_b64 v[118:119], 0\n"
  "v_mov_b64 v[120:121], 0\n"
  "v_mov_b64 v[122:123], 0\n"
  "v_mov_b64 v[124:125], 0\n"
  "v_mov_b64 v[126:127], 0\n"
  "v_mov_b64 v[128:129], 0\n"
  "v_mov_b64 v[130:131], 0\n"
  "v_mov_b64 v[132:133], 0\n"
  "v_mov_b64 v[134:135], 0\n"
  "v_mov_b64 v[136:137], 0\n"
  "v_mov_b64 v[138:139], 0\n"
  "v_mov_b64 v[140:141], 0\n"
  "v_mov_b64 v[142:143], 0\n"
  "s_cmp_ge_u32 s72, 4\n"
  "s_cbranch_scc1 .LproB3%=\n"
  "v_mov_b32 v60, 0\n"
  "v_mov_b32 v61, 0\n"
  "v_mov_b32 v62, 0\n"
  "v_mov_b32 v63, 0\n"
  "s_lshl_b32 s94, s73, 10\n"
  "s_add_u32 s94, s94, 159744\n"
  "v_add_u32 v64, s94, v0\n"
  "ds_write_b128 v64, v[60:63]\n"
  "s_cmp_lg_u32 s73, 3\n"
  "s_cbranch_scc1 .LpA5%=\n"
  "v_mul_u32_u24 v64, 400, v8\n"
  "v_add_u32 v64, 86016, v64\n"
  "s_mov_b64 exec, 0x7ffff\n"
  "ds_write_b128 v64, v[60:63]\n"
  "s_mov_b64 exec, -1\n"
  "s_getpc_b64 s[76:77]\n"
  ".Lcp0%=:\n"
  "s_mov_b32 s78, .Lexit%=-.Lcp0%=\n"
  "s_bfe_u32 s94, s75, 0x30003\n"
  "s_lshl_b32 s94, s94, 13\n"
  "v_lshlrev_b32 v103, 3, v0\n"
  "buffer_load_dword v103, v103, s[76:79], s94 offen\n"
  "s_setprio 3\n"
  "s_waitcnt vmcnt(1) lgkmcnt(0)\n"
  "s_branch .LpA7%=\n"
  ".LpA5%=:\n"
  "v_mul_u32_u24 v25, 1200, v25\n"
  "v_lshl_add_u32 v25, v24, 4, v25\n"
  "s_lshl_b32 s94, s73, 7\n"
  "s_add_u32 s94, s94, 86032\n"
  "v_add_u32 v25, s94, v25\n"
  "v_cmp_gt_u32 vcc, 4, v24\n"
  "s_waitcnt vmcnt(12)\n"
  "s_mov_b32 exec_hi, 0xffff\n"
  "v_cndmask_b32 v36, 0, v70, vcc\n"
  "v_sub_u32 v37, v70, v36\n"
  "v_cndmask_b32 v38, 0, v71, vcc\n"
  "v_sub_u32 v39, v71, v38\n"
  "ds_write_b128 v25, v[36:39] offset:0\n"
  "v_cndmask_b32 v40, 0, v72, vcc\n"
  "v_sub_u32 v41, v72, v40\n"
  "v_cndmask_b32 v42, 0, v73, vcc\n"
  "v_sub_u32 v43, v73, v42\n"
  "ds_write_b128 v25, v[40:43] offset:400\n"
  "v_cndmask_b32 v44, 0, v74, vcc\n"
  "v_sub_u32 v45, v74, v44\n"
  "v_cndmask_b32 v46, 0, v75, vcc\n"
  "v_sub_u32 v47, v75, v46\n"
  "ds_write_b128 v25, v[44:47] offset:800\n"
  "s_mov_b64 exec, -1\n"
  ".LpA6%=:\n"
  "s_setprio 3\n"
  "s_waitcnt vmcnt(0) lgkmcnt(0)\n"
  ".LpA7%=:\n"
  "v_readfirstlane_b32 s74, v100\n"
  "s_nop 1\n"
  "s_add_u32 s74, s74, 1\n"
  "s_barrier\n"
  "s_branch .Lp1in%=\n"
  ".LproB%=:\n"
  "s_setprio 2\n"
  "v_lshrrev_b32 v12, 2, v8\n"
  "v_mul_u32_u24 v12, 768, v12\n"
  "v_and_b32 v13, 3, v8\n"
  "v_lshl_add_u32 v12, v13, 4, v12\n"
  "s_mov_b64 s[76:77], s[12:13]\n"
  "s_mov_b32 s78, 73728\n"
  "s_mov_b32 s79, 0x20000\n"
  "s_mov_b64 s[88:89], %3\n"
  "s_mov_b32 s90, 12288\n"
  "s_mov_b32 s91, 0x20000\n"
  "s_lshl_b32 s92, s73, 6\n"
  "s_mul_i32 s93, s73, 6144\n"
  "s_add_u32 s93, s93, 6144\n"
  "s_cmp_ge_u32 s73, 2\n"
  "s_cselect_b32 s86, 6144, 0\n"
  "s_add_u32 s86, s86, s93\n"
  "s_cmp_ge_u32 s73, 1\n"
  "s_cselect_b32 s87, 6144, 0\n"
  "s_add_u32 s87, s87, s93\n"
  "s_branch .Lcom%=\n"
  ".LproB3%=:\n"
  "v_lshrrev_b32 v14, 5, v8\n"
  "v_bfe_u32 v15, v8, 2, 2\n"
  "v_lshl_add_u32 v14, v14, 2, v15\n"
  "v_lshlrev_b32 v14, 6, v14\n"
  "v_bfe_u32 v15, v8, 4, 1\n"
  "v_lshl_add_u32 v14, v15, 5, v14\n"
  "v_lshl_add_u32 v14, v13, 3, v14\n"
  "v_add_u32 v18, s93, v14\n"
  "v_add_u32 v19, s86, v14\n"
  "v_add_u32 v20, s87, v14\n"
  "v_add_u32 v21, s93, v0\n"
  "v_add_u32 v22, s86, v0\n"
  "v_add_u32 v23, s87, v0\n"
  "s_add_u32 m0, s93, 0\n"
  "s_add_u32 s62, s92, 0\n"
  "buffer_load_dwordx4 v12, s[76:79], s62 offen lds\n"
  "s_add_u32 m0, s93, 1024\n"
  "s_add_u32 s62, s92, 12288\n"
  "buffer_load_dwordx4 v12, s[76:79], s62 offen lds\n"
  "s_add_u32 m0, s93, 2048\n"
  "s_add_u32 s62, s92, 24576\n"
  "buffer_load_dwordx4 v12, s[76:79], s62 offen lds\n"
  "s_add_u32 m0, s93, 3072\n"
  "s_add_u32 s62, s92, 36864\n"
  "buffer_load_dwordx4 v12, s[76:79], s62 offen lds\n"
  "s_add_u32 m0, s93, 4096\n"
  "s_add_u32 s62, s92, 49152\n"
  "buffer_load_dwordx4 v12, s[76:79], s62 offen lds\n"
  "s_add_u32 m0, s93, 5120\n"
  "s_add_u32 s62, s92, 61440\n"
  "buffer_load_dwordx4 v12, s[76:79], s62 offen lds\n"
  "s_barrier\n"
  "s_mul_i32 s62, s73, 3072\n"
  "s_cmp_ge_u32 s73, 2\n"
  "s_cselect_b32 s94, 59392, 0\n"
  "s_add_u32 m0, s62, s94\n"
  "s_nop 0\n"
  "buffer_load_dwordx4 v0, s[88:91], s62 offen offset:0 lds\n"
  "buffer_load_dwordx4 v0, s[88:91], s62 offen offset:1024 lds\n"
  "buffer_load_dwordx4 v0, s[88:91], s62 offen offset:2048 lds\n"
  "s_add_u32 m0, s93, 24576\n"
  "s_add_u32 s62, s92, 256\n"
  "buffer_load_dwordx4 v12, s[76:79], s62 offen lds\n"
  "s_add_u32 m0, s93, 25600\n"
  "s_add_u32 s62, s92, 12544\n"
  "buffer_load_dwordx4 v12, s[76:79], s62 offen lds\n"
  "s_add_u32 m0, s93, 26624\n"
  "s_add_u32 s62, s92, 24832\n"
  "buffer_load_dwordx4 v12, s[76:79], s62 offen lds\n"
  "s_add_u32 m0, s93, 27648\n"
  "s_add_u32 s62, s92, 37120\n"
  "buffer_load_dwordx4 v12, s[76:79], s62 offen lds\n"
  "s_add_u32 m0, s93, 28672\n"
  "s_add_u32 s62, s92, 49408\n"
  "buffer_load_dwordx4 v12, s[76:79], s62 offen lds\n"
  "s_add_u32 m0, s93, 29696\n"
  "s_add_u32 s62, s92, 61696\n"
  "buffer_load_dwordx4 v12, s[76:79], s62 offen lds\n"
  "s_waitcnt vmcnt(9)\n"
  "ds_read_b64_tr_b16 v[24:25], v18 offset:0\n"
  "ds_read_b64_tr_b16 v[26:27], v18 offset:512\n"
  "ds_read_b64_tr_b16 v[28:29], v18 offset:1024\n"
  "ds_read_b64_tr_b16 v[30:31], v18 offset:1536\n"
  "ds_read_b64_tr_b16 v[32:33], v18 offset:2048\n"
  "ds_read_b64_tr_b16 v[34:35], v18 offset:2560\n"
  "ds_read_b64_tr_b16 v[36:37], v18 offset:3072\n"
  "ds_read_b64_tr_b16 v[38:39], v18 offset:3584\n"
  "ds_read_b64_tr_b16 v[40:41], v18 offset:4096\n"
  "ds_read_b64_tr_b16 v[42:43], v18 offset:4608\n"
  "ds_read_b64_tr_b16 v[44:45], v18 offset:5120\n"
  "ds_read_b64_tr_b16 v[46:47], v18 offset:5632\n"
  "s_add_u32 m0, s86, 49152\n"
  "s_add_u32 s62, s92, 512\n"
  "buffer_load_dwordx4 v12, s[76:79], s62 offen lds\n"
  "s_add_u32 m0, s86, 50176\n"
  "s_add_u32 s62, s92, 12800\n"
  "buffer_load_dwordx4 v12, s[76:79], s62 offen lds\n"
  "s_add_u32 m0, s86, 51200\n"
  "s_add_u32 s62, s92, 25088\n"
  "buffer_load_dwordx4 v12, s[76:79], s62 offen lds\n"
  "s_add_u32 m0, s86, 52224\n"
  "s_add_u32 s62, s92, 37376\n"
  "buffer_load_dwordx4 v12, s[76:79], s62 offen lds\n"
  "s_add_u32 m0, s87, 53248\n"
  "s_add_u32 s62, s92, 49664\n"
  "buffer_load_dwordx4 v12, s[76:79], s62 offen lds\n"
  "s_add_u32 m0, s87, 54272\n"
  "s_add_u32 s62, s92, 61952\n"
  "buffer_load_dwordx4 v12, s[76:79], s62 offen lds\n"
  "s_waitcnt lgkmcnt(10)\n"
  "ds_write_b128 v21, v[24:27] offset:0\n"
  "s_waitcnt lgkmcnt(9)\n"
  "ds_write_b128 v21, v[28:31] offset:1024\n"
  "s_waitcnt lgkmcnt(8)\n"
  "ds_write_b128 v21, v[32:35] offset:2048\n"
  "s_waitcnt lgkmcnt(7)\n"
  "ds_write_b128 v21, v[36:39] offset:3072\n"
  "s_waitcnt lgkmcnt(6)\n"
  "ds_write_b128 v21, v[40:43] offset:4096\n"
  "s_waitcnt lgkmcnt(5)\n"
  "ds_write_b128 v21, v[44:47] offset:5120\n"
  "s_waitcnt vmcnt(12) lgkmcnt(0)\n"
  "v_readfirstlane_b32 s74, v100\n"
  "s_nop 1\n"
  "s_add_u32 s74, s74, 1\n"
  "s_mov_b64 vcc, 0\n"
  "s_barrier\n"
  "s_sub_u32 s70, s70, 1\n"
  "s_cmp_gt_u32 s70, 6\n"
  "s_cbranch_scc1 .Lq1%=\n"
  "s_mov_b64 vcc, -1\n"
  "s_bitcmp1_b32 s70, 0\n"
  "s_cselect_b32 s62, 28672, 0\n"
  "s_add_u32 s61, s67, s62\n"
  "s_mul_i32 s62, s70, 393216\n"
  "s_sub_u32 s60, s68, s62\n"
  ".Lq1%=:\n"
  "s_waitcnt vmcnt(0)\n"
  "ds_read_b64_tr_b16 v[48:49], v18 offset:24576\n"
  "ds_read_b64_tr_b16 v[50:51], v18 offset:25088\n"
  "ds_read_b64_tr_b16 v[52:53], v18 offset:25600\n"
  "ds_read_b64_tr_b16 v[54:55], v18 offset:26112\n"
  "ds_read_b64_tr_b16 v[56:57], v18 offset:26624\n"
  "ds_read_b64_tr_b16 v[58:59], v18 offset:27136\n"
  "ds_read_b64_tr_b16 v[60:61], v18 offset:27648\n"
  "ds_read_b64_tr_b16 v[62:63], v18 offset:28160\n"
  "ds_read_b64_tr_b16 v[64:65], v18 offset:28672\n"
  "ds_read_b64_tr_b16 v[66:67], v18 offset:29184\n"
  "ds_read_b64_tr_b16 v[68:69], v18 offset:29696\n"
  "ds_read_b64_tr_b16 v[70:71], v18 offset:30208\n"
  "s_cbranch_vccz .Ld2%=\n"
  "s_add_u32 s61, s61, 7168\n"
  "s_mov_b32 m0, s61\n"
  "s_add_u32 s60, s60, 0x18000\n"
  "buffer_load_dwordx4 v4, s[40:43], s60 offen  lds\n"
  "s_cmp_eq_u32 s73, 3\n"
  "s_cbranch_scc1 .Ld2%=\n"
  "s_add_u32 m0, s61, 4096\n"
  "s_nop 0\n"
  "buffer_load_dwordx4 v6, s[40:43], s60 offen  lds\n"
  ".Ld2%=:\n"
  "s_cbranch_vccz .Ld3%=\n"
  "s_add_u32 s61, s61, 7168\n"
  "s_mov_b32 m0, s61\n"
  "s_add_u32 s60, s60, 0x18000\n"
  "buffer_load_dwordx4 v4, s[40:43], s60 offen  lds\n"
  "s_cmp_eq_u32 s73, 3\n"
  "s_cbranch_scc1 .Ld3%=\n"
  "s_add_u32 m0, s61, 4096\n"
  "s_nop 0\n"
  "buffer_load_dwordx4 v6, s[40:43], s60 offen  lds\n"
  ".Ld3%=:\n"
  "s_cbranch_vccz .Ld4%=\n"
  "s_add_u32 s61, s61, 7168\n"
  "s_mov_b32 m0, s61\n"
  "s_add_u32 s60, s60, 0x18000\n"
  "buffer_load_dwordx4 v4, s[40:43], s60 offen  lds\n"
  "s_cmp_eq_u32 s73, 3\n"
  "s_cbranch_scc1 .Ld4%=\n"
  "s_add_u32 m0, s61, 4096\n"
  "s_nop 0\n"
  "buffer_load_dwordx4 v6, s[40:43], s60 offen  lds\n"
  ".Ld4%=:\n"
  "s_cbranch_vccz .Ld5%=\n"
  "s_add_u32 s61, s61, 7168\n"
  "s_mov_b32 m0, s61\n"
  "s_add_u32 s60, s60, 0x18000\n"
  "buffer_load_dwordx4 v4, s[40:43], s60 offen  lds\n"
  "s_cmp_eq_u32 s73, 3\n"
  "s_cbranch_scc1 .Ld5%=\n"
  "s_add_u32 m0, s61, 4096\n"
  "s_nop 0\n"
  "buffer_load_dwordx4 v6, s[40:43], s60 offen  lds\n"
  ".Ld5%=:\n"
  "s_waitcnt lgkmcnt(0)\n"
  "ds_read_b64_tr_b16 v[72:73], v19 offset:49152\n"
  "ds_read_b64_tr_b16 v[74:75], v19 offset:49664\n"
  "ds_read_b64_tr_b16 v[76:77], v19 offset:50176\n"
  "ds_read_b64_tr_b16 v[78:79], v19 offset:50688\n"
  "ds_read_b64_tr_b16 v[80:81], v19 offset:51200\n"
  "ds_read_b64_tr_b16 v[82:83], v19 offset:51712\n"
  "ds_read_b64_tr_b16 v[84:85], v19 offset:52224\n"
  "ds_read_b64_tr_b16 v[86:87], v19 offset:52736\n"
  "ds_read_b64_tr_b16 v[88:89], v20 offset:53248\n"
  "ds_read_b64_tr_b16 v[90:91], v20 offset:53760\n"
  "ds_read_b64_tr_b16 v[92:93], v20 offset:54272\n"
  "ds_read_b64_tr_b16 v[94:95], v20 offset:54784\n"
  "ds_write_b128 v21, v[48:51] offset:24576\n"
  "ds_write_b128 v21, v[52:55] offset:25600\n"
  "ds_write_b128 v21, v[56:59] offset:26624\n"
  "ds_write_b128 v21, v[60:63] offset:27648\n"
  "ds_write_b128 v21, v[64:67] offset:28672\n"
  "ds_write_b128 v21, v[68:71] offset:29696\n"
  "s_mov_b64 exec, 1\n"
  "v_mov_b32 v199, 160152\n"
  "v_mov_b32 v197, 1\n"
  "ds_add_u32 v199, v197\n"
  "s_mov_b64 exec, -1\n"
  "s_waitcnt lgkmcnt(0)\n"
  "ds_write_b128 v22, v[72:75] offset:49152\n"
  "ds_write_b128 v22, v[76:79] offset:50176\n"
  "ds_write_b128 v22, v[80:83] offset:51200\n"
  "ds_write_b128 v22, v[84:87] offset:52224\n"
  "ds_write_b128 v23, v[88:91] offset:53248\n"
  "ds_write_b128 v23, v[92:95] offset:54272\n"
  "s_mov_b64 exec, 1\n"
  "v_mov_b32 v199, 160156\n"
  "v_mov_b32 v197, 1\n"
  "ds_add_u32 v199, v197\n"
  "s_mov_b64 exec, -1\n"
  "s_setprio 0\n"
  "s_lshl_b32 s94, s75, 3\n"
  "s_add_u32 s94, s94, s72\n"
  "s_cmp_lg_u32 s94, 7\n"
  "s_cbranch_scc1 .Lnoz%=\n"
  "v_lshlrev_b32 v9, 8, v8\n"
  "s_mov_b64 exec, 0xffff\n"
  "buffer_store_dword v104, v9, s[80:83], 0 offen offset:512 sc1\n"
  "s_mov_b64 exec, -1\n"
  "s_mov_b32 s94, 8192\n"
  "buffer_store_dwordx4 v[104:107], v0, s[80:83], s94 offen sc1\n"
  "s_nop 1\n"
  "s_mov_b32 s94, 9216\n"
  "buffer_store_dwordx4 v[104:107], v0, s[80:83], s94 offen sc1\n"
  "s_nop 1\n"
  "s_mov_b32 s94, 10240\n"
  "buffer_store_dwordx4 v[104:107], v0, s[80:83], s94 offen sc1\n"
  "s_nop 1\n"
  "s_mov_b32 s94, 11264\n"
  "buffer_store_dwordx4 v[104:107], v0, s[80:83], s94 offen sc1\n"
  "s_nop 1\n"
  "s_mov_b32 s94, 12288\n"
  "buffer_store_dwordx4 v[104:107], v0, s[80:83], s94 offen sc1\n"
  "s_nop 1\n"
  "s_mov_b32 s94, 13312\n"
  "buffer_store_dwordx4 v[104:107], v0, s[80:83], s94 offen sc1\n"
  "s_nop 1\n"
  "s_mov_b32 s94, 14336\n"
  "buffer_store_dwordx4 v[104:107], v0, s[80:83], s94 offen sc1\n"
  "s_nop 1\n"
  "s_mov_b32 s94, 15360\n"
  "buffer_store_dwordx4 v[104:107], v0, s[80:83], s94 offen sc1\n"
  "s_nop 1\n"
  "s_mov_b32 s94, 16384\n"
  "buffer_store_dwordx4 v[104:107], v0, s[80:83], s94 offen sc1\n"
  "s_nop 1\n"
  "s_mov_b32 s94, 17408\n"
  "buffer_store_dwordx4 v[104:107], v0, s[80:83], s94 offen sc1\n"
  "s_nop 1\n"
  "s_mov_b32 s94, 18432\n"
  "buffer_store_dwordx4 v[104:107], v0, s[80:83], s94 offen sc1\n"
  "s_nop 1\n"
  "s_mov_b32 s94, 19456\n"
  "buffer_store_dwordx4 v[104:107], v0, s[80:83], s94 offen sc1\n"
  "s_waitcnt vmcnt(0)\n"
  "v_mov_b32 v11, s74\n"
  "v_mov_b32 v9, 0\n"
  "s_mov_b64 exec, 1\n"
  "buffer_store_dword v11, v9, s[80:83], 0 offen offset:256 sc1\n"
  "s_mov_b64 exec, -1\n"
  ".Lnoz%=:\n"
  "s_waitcnt vmcnt(0)\n"
  "s_mov_b64 exec, 1\n"
  "v_mov_b32 v199, 160148\n"
  "v_mov_b32 v197, 1\n"
  "ds_add_u32 v199, v197\n"
  "s_mov_b64 exec, -1\n"
  ".Lp1%=:\n"
  "s_cmp_lg_u32 s70, 5\n"
  "s_cbranch_scc1 .Lnmx%=\n"
  "s_mul_i32 s94, s73, 18432\n"
  "s_add_u32 s94, s94, 6144\n"
  "v_add_u32 v100, s94, v0\n"
  "s_cmp_eq_u32 s73, 3\n"
  "s_cselect_b32 s92, 6144, 0\n"
  "v_add_u32 v101, s92, v100\n"
  "s_mov_b32 s84, 0x7fff7fff\n"
  "ds_read_b128 v[56:59], v100 offset:0\n"
  "ds_read_b128 v[60:63], v100 offset:1024\n"
  "ds_read_b128 v[64:67], v100 offset:2048\n"
  "ds_read_b128 v[68:71], v100 offset:3072\n"
  "ds_read_b128 v[72:75], v101 offset:4096\n"
  "ds_read_b128 v[76:79], v101 offset:5120\n"
  "ds_read_b128 v[80:83], v101 offset:6144\n"
  "ds_read_b128 v[84:87], v101 offset:7168\n"
  "ds_read_b128 v[88:91], v101 offset:8192\n"
  "s_waitcnt lgkmcnt(8)\n"
  "v_pk_maximum3_f16 v92, v56, v56, v56 neg_lo:[0,0,1] neg_hi:[0,0,1]\n"
  "v_pk_maximum3_f16 v93, v57, v57, v57 neg_lo:[0,0,1] neg_hi:[0,0,1]\n"
  "v_pk_maximum3_f16 v94, v58, v58, v58 neg_lo:[0,0,1] neg_hi:[0,0,1]\n"
  "v_pk_maximum3_f16 v95, v59, v59, v59 neg_lo:[0,0,1] neg_hi:[0,0,1]\n"
  "s_waitcnt lgkmcnt(7)\n"
  "v_pk_maximum3_f16 v92, v92, v60, v60 neg_lo:[0,0,1] neg_hi:[0,0,1]\n"
  "v_pk_maximum3_f16 v93, v93, v61, v61 neg_lo:[0,0,1] neg_hi:[0,0,1]\n"
  "v_pk_maximum3_f16 v94, v94, v62, v62 neg_lo:[0,0,1] neg_hi:[0,0,1]\n"
  "v_pk_maximum3_f16 v95, v95, v63, v63 neg_lo:[0,0,1] neg_hi:[0,0,1]\n"
  "s_waitcnt lgkmcnt(6)\n"
  "v_pk_maximum3_f16 v92, v92, v64, v64 neg_lo:[0,0,1] neg_hi:[0,0,1]\n"
  "v_pk_maximum3_f16 v93, v93, v65, v65 neg_lo:[0,0,1] neg_hi:[0,0,1]\n"
  "v_pk_maximum3_f16 v94, v94, v66, v66 neg_lo:[0,0,1] neg_hi:[0,0,1]\n"
  "v_pk_maximum3_f16 v95, v95, v67, v67 neg_lo:[0,0,1] neg_hi:[0,0,1]\n"
  "s_waitcnt lgkmcnt(5)\n"
  "v_pk_maximum3_f16 v92, v92, v68, v68 neg_lo:[0,0,1] neg_hi:[0,0,1]\n"
  "v_pk_maximum3_f16 v93, v93, v69, v69 neg_lo:[0,0,1] neg_hi:[0,0,1]\n"
  "v_pk_maximum3_f16 v94, v94, v70, v70 neg_lo:[0,0,1] neg_hi:[0,0,1]\n"
  "v_pk_maximum3_f16 v95, v95, v71, v71 neg_lo:[0,0,1] neg_hi:[0,0,1]\n"
  "s_waitcnt lgkmcnt(4)\n"
  "v_pk_maximum3_f16 v92, v92, v72, v72 neg_lo:[0,0,1] neg_hi:[0,0,1]\n"
  "v_pk_maximum3_f16 v93, v93, v73, v73 neg_lo:[0,0,1] neg_hi:[0,0,1]\n"
  "v_pk_maximum3_f16 v94, v94, v74, v74 neg_lo:[0,0,1] neg_hi:[0,0,1]\n"
  "v_pk_maximum3_f16 v95, v95, v75, v75 neg_lo:[0,0,1] neg_hi:[0,0,1]\n"
  "s_waitcnt lgkmcnt(3)\n"
  "v_pk_maximum3_f16 v92, v92, v76, v76 neg_lo:[0,0,1] neg_hi:[0,0,1]\n"
  "v_pk_maximum3_f16 v93, v93, v77, v77 neg_lo:[0,0,1] neg_hi:[0,0,1]\n"
  "v_pk_maximum3_f16 v94, v94, v78, v78 neg_lo:[0,0,1] neg_hi:[0,0,1]\n"
  "v_pk_maximum3_f16 v95, v95, v79, v79 neg_lo:[0,0,1] neg_hi:[0,0,1]\n"
  "s_waitcnt lgkmcnt(2)\n"
  "v_pk_maximum3_f16 v92, v92, v80, v80 neg_lo:[0,0,1] neg_hi:[0,0,1]\n"
  "v_pk_maximum3_f16 v93, v93, v81, v81 neg_lo:[0,0,1] neg_hi:[0,0,1]\n"
  "v_pk_maximum3_f16 v94, v94, v82, v82 neg_lo:[0,0,1] neg_hi:[0,0,1]\n"
  "v_pk_maximum3_f16 v95, v95, v83, v83 neg_lo:[0,0,1] neg_hi:[0,0,1]\n"
  "s_waitcnt lgkmcnt(1)\n"
  "v_pk_maximum3_f16 v92, v92, v84, v84 neg_lo:[0,0,1] neg_hi:[0,0,1]\n"
  "v_pk_maximum3_f16 v93, v93, v85, v85 neg_lo:[0,0,1] neg_hi:[0,0,1]\n"
  "v_pk_maximum3_f16 v94, v94, v86, v86 neg_lo:[0,0,1] neg_hi:[0,0,1]\n"
  "v_pk_maximum3_f16 v95, v95, v87, v87 neg_lo:[0,0,1] neg_hi:[0,0,1]\n"
  "s_waitcnt lgkmcnt(0)\n"
  "v_pk_maximum3_f16 v92, v92, v88, v88 neg_lo:[0,0,1] neg_hi:[0,0,1]\n"
  "v_pk_maximum3_f16 v93, v93, v89, v89 neg_lo:[0,0,1] neg_hi:[0,0,1]\n"
  "v_pk_maximum3_f16 v94, v94, v90, v90 neg_lo:[0,0,1] neg_hi:[0,0,1]\n"
  "v_pk_maximum3_f16 v95, v95, v91, v91 neg_lo:[0,0,1] neg_hi:[0,0,1]\n"
  "ds_read_b128 v[56:59], v101 offset:9216\n"
  "ds_read_b128 v[60:63], v101 offset:10240\n"
  "ds_read_b128 v[64:67], v101 offset:11264\n"
  "ds_read_b128 v[68:71], v101 offset:12288\n"
  "ds_read_b128 v[72:75], v101 offset:13312\n"
  "ds_read_b128 v[76:79], v101 offset:14336\n"
  "ds_read_b128 v[80:83], v101 offset:15360\n"
  "ds_read_b128 v[84:87], v101 offset:16384\n"
  "ds_read_b128 v[88:91], v101 offset:17408\n"
  "s_waitcnt lgkmcnt(8)\n"
  "v_pk_maximum3_f16 v92, v92, v56, v56 neg_lo:[0,0,1] neg_hi:[0,0,1]\n"
  "v_pk_maximum3_f16 v93, v93, v57, v57 neg_lo:[0,0,1] neg_hi:[0,0,1]\n"
  "v_pk_maximum3_f16 v94, v94, v58, v58 neg_lo:[0,0,1] neg_hi:[0,0,1]\n"
  "v_pk_maximum3_f16 v95, v95, v59, v59 neg_lo:[0,0,1] neg_hi:[0,0,1]\n"
  "s_waitcnt lgkmcnt(7)\n"
  "v_pk_maximum3_f16 v92, v92, v60, v60 neg_lo:[0,0,1] neg_hi:[0,0,1]\n"
  "v_pk_maximum3_f16 v93, v93, v61, v61 neg_lo:[0,0,1] neg_hi:[0,0,1]\n"
  "v_pk_maximum3_f16 v94, v94, v62, v62 neg_lo:[0,0,1] neg_hi:[0,0,1]\n"
  "v_pk_maximum3_f16 v95, v95, v63, v63 neg_lo:[0,0,1] neg_hi:[0,0,1]\n"
  "s_waitcnt lgkmcnt(6)\n"
  "v_pk_maximum3_f16 v92, v92, v64, v64 neg_lo:[0,0,1] neg_hi:[0,0,1]\n"
  "v_pk_maximum3_f16 v93, v93, v65, v65 neg_lo:[0,0,1] neg_hi:[0,0,1]\n"
  "v_pk_maximum3_f16 v94, v94, v66, v66 neg_lo:[0,0,1] neg_hi:[0,0,1]\n"
  "v_pk_maximum3_f16 v95, v95, v67, v67 neg_lo:[0,0,1] neg_hi:[0,0,1]\n"
  "s_waitcnt lgkmcnt(5)\n"
  "v_pk_maximum3_f16 v92, v92, v68, v68 neg_lo:[0,0,1] neg_hi:[0,0,1]\n"
  "v_pk_maximum3_f16 v93, v93, v69, v69 neg_lo:[0,0,1] neg_hi:[0,0,1]\n"
  "v_pk_maximum3_f16 v94, v94, v70, v70 neg_lo:[0,0,1] neg_hi:[0,0,1]\n"
  "v_pk_maximum3_f16 v95, v95, v71, v71 neg_lo:[0,0,1] neg_hi:[0,0,1]\n"
  "s_waitcnt lgkmcnt(4)\n"
  "v_pk_maximum3_f16 v92, v92, v72, v72 neg_lo:[0,0,1] neg_hi:[0,0,1]\n"
  "v_pk_maximum3_f16 v93, v93, v73, v73 neg_lo:[0,0,1] neg_hi:[0,0,1]\n"
  "v_pk_maximum3_f16 v94, v94, v74, v74 neg_lo:[0,0,1] neg_hi:[0,0,1]\n"
  "v_pk_maximum3_f16 v95, v95, v75, v75 neg_lo:[0,0,1] neg_hi:[0,0,1]\n"
  "s_waitcnt lgkmcnt(3)\n"
  "v_pk_maximum3_f16 v92, v92, v76, v76 neg_lo:[0,0,1] neg_hi:[0,0,1]\n"
  "v_pk_maximum3_f16 v93, v93, v77, v77 neg_lo:[0,0,1] neg_hi:[0,0,1]\n"
  "v_pk_maximum3_f16 v94, v94, v78, v78 neg_lo:[0,0,1] neg_hi:[0,0,1]\n"
  "v_pk_maximum3_f16 v95, v95, v79, v79 neg_lo:[0,0,1] neg_hi:[0,0,1]\n"
  "s_waitcnt lgkmcnt(2)\n"
  "v_pk_maximum3_f16 v92, v92, v80, v80 neg_lo:[0,0,1] neg_hi:[0,0,1]\n"
  "v_pk_maximum3_f16 v93, v93, v81, v81 neg_lo:[0,0,1] neg_hi:[0,0,1]\n"
  "v_pk_maximum3_f16 v94, v94, v82, v82 neg_lo:[0,0,1] neg_hi:[0,0,1]\n"
  "v_pk_maximum3_f16 v95, v95, v83, v83 neg_lo:[0,0,1] neg_hi:[0,0,1]\n"
  "s_waitcnt lgkmcnt(1)\n"
  "v_pk_maximum3_f16 v92, v92, v84, v84 neg_lo:[0,0,1] neg_hi:[0,0,1]\n"
  "v_pk_maximum3_f16 v93, v93, v85, v85 neg_lo:[0,0,1] neg_hi:[0,0,1]\n"
  "v_pk_maximum3_f16 v94, v94, v86, v86 neg_lo:[0,0,1] neg_hi:[0,0,1]\n"
  "v_pk_maximum3_f16 v95, v95, v87, v87 neg_lo:[0,0,1] neg_hi:[0,0,1]\n"
  "s_waitcnt lgkmcnt(0)\n"
  "v_pk_maximum3_f16 v92, v92, v88, v88 neg_lo:[0,0,1] neg_hi:[0,0,1]\n"
  "v_pk_maximum3_f16 v93, v93, v89, v89 neg_lo:[0,0,1] neg_hi:[0,0,1]\n"
  "v_pk_maximum3_f16 v94, v94, v90, v90 neg_lo:[0,0,1] neg_hi:[0,0,1]\n"
  "v_pk_maximum3_f16 v95, v95, v91, v91 neg_lo:[0,0,1] neg_hi:[0,0,1]\n"
  "v_pk_maximum3_f16 v92, v92, v93, v94\n"
  "v_pk_maximum3_f16 v92, v92, v95, v95\n"
  "v_bfe_u32 v93, v92, 16, 15\n"
  "v_and_b32 v92, 0x7fff, v92\n"
  "v_max_u32 v92, v92, v93\n"
  "s_nop 1\n"
  "v_max_u32_dpp v92, v92, v92 row_ror:8 row_mask:0xf bank_mask:0xf\n"
  "s_nop 1\n"
  "v_max_u32_dpp v92, v92, v92 row_ror:4 row_mask:0xf bank_mask:0xf\n"
  "s_nop 1\n"
  "v_max_u32_dpp v92, v92, v92 row_ror:2 row_mask:0xf bank_mask:0xf\n"
  "s_nop 1\n"
  "v_max_u32_dpp v92, v92, v92 row_ror:1 row_mask:0xf bank_mask:0xf\n"
  "v_mov_b32 v93, 163340\n"
  "s_mov_b32 exec_lo, 0x00010001\n"
  "s_mov_b32 exec_hi, 0x00010001\n"
  "ds_max_u32 v93, v92\n"
  "s_mov_b64 exec, -1\n"
  ".Lnmx%=:\n"
  "s_lshl_b32 s94, s70, 2\n"
  "s_sub_u32 s94, 28, s94\n"
  "s_mov_b32 s96, 1048576\n"
  ".Lw6%=:\n"
  "v_mov_b32 v199, 160148\n"
  "ds_read_b32 v199, v199\n"
  "s_waitcnt lgkmcnt(0)\n"
  "v_readfirstlane_b32 s92, v199\n"
  "s_nop 1\n"
  "s_cmp_ge_u32 s92, s94\n"
  "s_cbranch_scc1 .Lwd6%=\n"
  "s_sleep 1\n"
  "s_sub_u32 s96, s96, 1\n"
  "s_cmp_lg_u32 s96, 0\n"
  "s_cbranch_scc1 .Lw6%=\n"
  ".Lwd6%=:\n"
  "s_cmp_lg_u32 s55, 1\n"
  "s_cbranch_scc1 .Lnck%=\n"
  "v_mov_b32 v199, 163340\n"
  "ds_read_b32 v199, v199\n"
  "s_waitcnt lgkmcnt(0)\n"
  "v_readfirstlane_b32 s94, v199\n"
  "s_nop 1\n"
  "s_cmp_ge_u32 s94, 0x4130\n"
  "s_cselect_b32 s95, 1, 0\n"
  "s_cmp_lt_u32 s94, 0x3b00\n"
  "s_cselect_b32 s95, 1, s95\n"
  "s_lshr_b32 s97, s94, 7\n"
  "s_lshl_b32 s97, s97, 1\n"
  "s_sub_u32 s97, 404, s97\n"
  "s_lshl_b32 s97, s97, 23\n"
  ".Lnck%=:\n"
  "s_setprio 3\n"
  "s_branch .Lp1in%=\n"
  ".p2alignl 6, 0xBF800000\n"
  ".Lp1in%=:\n"
  "ds_read_u16 v246, v246\n"
  "ds_read_u16 v247, v247\n"
  "ds_read_b128 v[216:219], v3 offset:0\n"
  "ds_read_b128 v[224:227], v2 offset:0\n"
  "ds_read_b128 v[228:231], v2 offset:208\n"
  "ds_read_b128 v[220:223], v3 offset:400\n"
  "ds_read_b128 v[232:235], v2 offset:416\n"
  "ds_read_b128 v[236:239], v2 offset:16\n"
  "v_bfe_u32 v164, v0, 4, 2\n"
  "v_mul_u32_u24 v164, 17, v164\n"
  "v_mov_b64 v[8:9], 0\n"
  "v_mov_b64 v[10:11], 0\n"
  "v_mov_b64 v[12:13], 0\n"
  "v_mov_b64 v[14:15], 0\n"
  "v_lshlrev_b32_sdwa v176, 1, v176 dst_sel:DWORD dst_unused:UNUSED_PAD src0_sel:DWORD src1_sel:WORD_1\n"
  "v_cvt_pk_f16_f32 v188, v168, v169\n"
  "v_lshlrev_b32_sdwa v177, 1, v177 dst_sel:DWORD dst_unused:UNUSED_PAD src0_sel:DWORD src1_sel:WORD_1\n"
  "v_cvt_pk_f16_f32 v189, v170, v171\n"
  "v_lshlrev_b32_sdwa v178, 1, v178 dst_sel:DWORD dst_unused:UNUSED_PAD src0_sel:DWORD src1_sel:WORD_1\n"
  "v_cvt_pk_f16_f32 v190, v172, v173\n"
  "v_lshlrev_b32_sdwa v179, 1, v179 dst_sel:DWORD dst_unused:UNUSED_PAD src0_sel:DWORD src1_sel:WORD_1\n"
  "v_cvt_pk_f16_f32 v191, v174, v175\n"
  "v_lshlrev_b32_sdwa v180, 1, v180 dst_sel:DWORD dst_unused:UNUSED_PAD src0_sel:DWORD src1_sel:WORD_1\n"
  "v_lshlrev_b32_sdwa v181, 1, v181 dst_sel:DWORD dst_unused:UNUSED_PAD src0_sel:DWORD src1_sel:WORD_1\n"
  "s_waitcnt lgkmcnt(4)\n"
  "v_smfmac_f32_16x16x32_bf16 v[8:11], v[216:217], v[224:227], v164\n"
  "s_waitcnt lgkmcnt(3)\n"
  "ds_read_b128 v[224:227], v2 offset:224\n"
  "ds_read_u16 v176, v176\n"
  "v_mov_b64 v[16:17], 0\n"
  "v_lshlrev_b32_sdwa v182, 1, v182 dst_sel:DWORD dst_unused:UNUSED_PAD src0_sel:DWORD src1_sel:WORD_1\n"
  "v_mov_b64 v[18:19], 0\n"
  "v_mfma_f32_4x4x4_16b_f16 v[144:147], v[184:185], v[184:185], v[144:147]\n"
  "v_lshlrev_b32_sdwa v183, 1, v183 dst_sel:DWORD dst_unused:UNUSED_PAD src0_sel:DWORD src1_sel:WORD_1\n"
  "v_smfmac_f32_16x16x32_bf16 v[8:11], v[218:219], v[228:231], v164\n"
  "s_waitcnt lgkmcnt(3)\n"
  "ds_read_b128 v[216:219], v3 offset:800\n"
  "ds_read_b128 v[228:231], v2 offset:432\n"
  "ds_read_u16 v177, v177\n"
  "v_pk_fma_f16 v208, v188, v188, s56\n"
  "v_pk_fma_f16 v209, v189, v189, s56\n"
  "v_pk_fma_f16 v210, v190, v190, s56\n"
  "v_mfma_f32_4x4x4_16b_f16 v[144:147], v[186:187], v[186:187], v[144:147]\n"
  "v_pk_fma_f16 v211, v191, v191, s56\n"
  "v_smfmac_f32_16x16x32_bf16 v[8:11], v[220:221], v[232:235], v164\n"
  "s_waitcnt lgkmcnt(5)\n"
  "ds_read_b128 v[232:235], v2 offset:32\n"
  "ds_read_u16 v178, v178\n"
  "v_pk_mul_f16 v208, v188, v208\n"
  "v_pk_mul_f16 v209, v189, v209\n"
  "v_pk_mul_f16 v210, v190, v210\n"
  "v_mfma_f32_4x4x4_16b_f16 v[144:147], v[240:241], v[240:241], v[144:147]\n"
  "v_pk_mul_f16 v211, v191, v211\n"
  "v_smfmac_f32_16x16x32_bf16 v[12:15], v[222:223], v[236:239], v164\n"
  "s_waitcnt lgkmcnt(4)\n"
  "ds_read_b128 v[220:223], v3 offset:1200\n"
  "ds_read_b128 v[236:239], v2 offset:240\n"
  "ds_read_u16 v179, v179\n"
  "v_mov_b64 v[20:21], 0\n"
  "v_exp_f16 v212, v208\n"
  "v_mfma_f32_4x4x4_16b_f16 v[144:147], v[242:243], v[242:243], v[144:147]\n"
  "v_mov_b64 v[22:23], 0\n"
  "v_smfmac_f32_16x16x32_bf16 v[12:15], v[216:217], v[224:227], v164\n"
  "s_waitcnt lgkmcnt(6)\n"
  "ds_read_b128 v[224:227], v2 offset:448\n"
  "ds_read_u16 v180, v180\n"
  "v_add_f32 v192, v8, v10\n"
  "v_mul_f32 v194, v8, v8\n"
  "v_add_f32 v193, v9, v11\n"
  "v_mfma_f32_4x4x4_16b_f16 v[144:147], v[244:245], v[244:245], v[144:147]\n"
  "v_exp_f16 v213, v209\n"
  "v_smfmac_f32_16x16x32_bf16 v[12:15], v[218:219], v[228:231], v164\n"
  "s_waitcnt lgkmcnt(4)\n"
  "ds_read_b128 v[216:219], v3 offset:1600\n"
  "ds_read_b128 v[228:231], v2 offset:48\n"
  "ds_read_u16 v181, v181\n"
  "v_mul_f32 v195, v9, v9\n"
  "v_exp_f16 v214, v210\n"
  "v_smfmac_f32_16x16x32_bf16 v[16:19], v[220:221], v[232:235], v164\n"
  "s_waitcnt lgkmcnt(6)\n"
  "ds_read_b128 v[232:235], v2 offset:256\n"
  "ds_read_u16 v182, v182\n"
  "v_mov_b64 v[24:25], 0\n"
  "v_exp_f16 v215, v211\n"
  "v_mov_b64 v[26:27], 0\n"
  "v_smfmac_f32_16x16x32_bf16 v[16:19], v[222:223], v[236:239], v164\n"
  "s_waitcnt lgkmcnt(4)\n"
  "ds_read_b128 v[220:223], v3 offset:2000\n"
  "ds_read_b128 v[236:239], v2 offset:464\n"
  "ds_read_u16 v183, v183\n"
  "v_fmac_f32 v194, v10, v10\n"
  "v_fmac_f32 v195, v11, v11\n"
  "v_add_f32 v192, v192, v12\n"
  "v_fmac_f32 v194, v12, v12\n"
  "v_smfmac_f32_16x16x32_bf16 v[16:19], v[216:217], v[224:227], v164\n"
  "s_waitcnt lgkmcnt(6)\n"
  "ds_read_b128 v[224:227], v2 offset:64\n"
  "v_add_f32 v193, v193, v13\n"
  "v_fmac_f32 v195, v13, v13\n"
  "v_add_f32 v192, v192, v14\n"
  "v_mfma_f32_4x4x4_16b_f16 v[144:147], v[246:247], v[246:247], v[144:147]\n"
  "v_exp_f16_sdwa v212, v208 dst_sel:WORD_1 dst_unused:UNUSED_PRESERVE src0_sel:WORD_1\n"
  "v_smfmac_f32_16x16x32_bf16 v[20:23], v[218:219], v[228:231], v164\n"
  "s_waitcnt lgkmcnt(3)\n"
  "ds_read_b128 v[216:219], v3 offset:2400\n"
  "ds_read_b128 v[228:231], v2 offset:272\n"
  "v_mov_b64 v[28:29], 0\n"
  "v_mov_b64 v[30:31], 0\n"
  "v_fmac_f32 v194, v14, v14\n"
  "v_smfmac_f32_16x16x32_bf16 v[20:23], v[220:221], v[232:235], v164\n"
  "s_waitcnt lgkmcnt(4)\n"
  "ds_read_b128 v[232:235], v2 offset:480\n"
  "v_add_f32 v193, v193, v15\n"
  "v_fmac_f32 v195, v15, v15\n"
  "v_add_f32 v192, v192, v16\n"
  "v_fmac_f32 v194, v16, v16\n"
  "v_smfmac_f32_16x16x32_bf16 v[20:23], v[222:223], v[236:239], v164\n"
  "s_waitcnt lgkmcnt(2)\n"
  "ds_read_b128 v[220:223], v3 offset:2800\n"
  "ds_read_b128 v[236:239], v2 offset:80\n"
  "v_add_f32 v193, v193, v17\n"
  "v_fmac_f32 v195, v17, v17\n"
  "v_add_f32 v192, v192, v18\n"
  "v_exp_f16_sdwa v213, v209 dst_sel:WORD_1 dst_unused:UNUSED_PRESERVE src0_sel:WORD_1\n"
  "v_smfmac_f32_16x16x32_bf16 v[24:27], v[216:217], v[224:227], v164\n"
  "s_waitcnt lgkmcnt(3)\n"
  "ds_read_b128 v[224:227], v2 offset:288\n"
  "v_mov_b64 v[200:201], 0\n"
  "v_mov_b64 v[202:203], 0\n"
  "v_mfma_f32_4x4x4_16b_f16 v[148:151], v[176:177], v[176:177], v[148:151]\n"
  "v_fmac_f32 v194, v18, v18\n"
  "v_smfmac_f32_16x16x32_bf16 v[24:27], v[218:219], v[228:231], v164\n"
  "s_waitcnt lgkmcnt(2)\n"
  "ds_read_b128 v[216:219], v3 offset:3200\n"
  "ds_read_b128 v[228:231], v2 offset:496\n"
  "v_add_f32 v193, v193, v19\n"
  "v_fmac_f32 v195, v19, v19\n"
  "v_add_f32 v192, v192, v20\n"
  "v_fmac_f32 v194, v20, v20\n"
  "v_smfmac_f32_16x16x32_bf16 v[24:27], v[220:221], v[232:235], v164\n"
  "s_waitcnt lgkmcnt(3)\n"
  "ds_read_b128 v[232:235], v2 offset:96\n"
  "v_add_f32 v193, v193, v21\n"
  "v_fmac_f32 v195, v21, v21\n"
  "v_add_f32 v192, v192, v22\n"
  "v_exp_f16_sdwa v214, v210 dst_sel:WORD_1 dst_unused:UNUSED_PRESERVE src0_sel:WORD_1\n"
  "v_smfmac_f32_16x16x32_bf16 v[28:31], v[222:223], v[236:239], v164\n"
  "s_waitcnt lgkmcnt(2)\n"
  "ds_read_b128 v[220:223], v3 offset:3600\n"
  "ds_read_b128 v[236:239], v2 offset:304\n"
  "v_mov_b64 v[204:205], 0\n"
  "v_mov_b64 v[206:207], 0\n"
  "v_mfma_f32_4x4x4_16b_f16 v[148:151], v[178:179], v[178:179], v[148:151]\n"
  "v_fmac_f32 v194, v22, v22\n"
  "v_smfmac_f32_16x16x32_bf16 v[28:31], v[216:217], v[224:227], v164\n"
  "s_waitcnt lgkmcnt(3)\n"
  "ds_read_b128 v[224:227], v2 offset:512\n"
  "v_add_f32 v193, v193, v23\n"
  "v_fmac_f32 v195, v23, v23\n"
  "v_add_f32 v192, v192, v24\n"
  "v_fmac_f32 v194, v24, v24\n"
  "v_smfmac_f32_16x16x32_bf16 v[28:31], v[218:219], v[228:231], v164\n"
  "s_waitcnt lgkmcnt(2)\n"
  "ds_read_b128 v[216:219], v3 offset:4000\n"
  "ds_read_b128 v[228:231], v2 offset:112\n"
  "v_add_f32 v193, v193, v25\n"
  "v_fmac_f32 v195, v25, v25\n"
  "v_add_f32 v192, v192, v26\n"
  "v_exp_f16_sdwa v215, v211 dst_sel:WORD_1 dst_unused:UNUSED_PRESERVE src0_sel:WORD_1\n"
  "v_smfmac_f32_16x16x32_bf16 v[200:203], v[220:221], v[232:235], v164\n"
  "s_waitcnt lgkmcnt(3)\n"
  "ds_read_b128 v[232:235], v2 offset:320\n"
  "v_mov_b64 v[240:241], 0\n"
  "v_mov_b64 v[242:243], 0\n"
  "v_mfma_f32_4x4x4_16b_f16 v[148:151], v[180:181], v[180:181], v[148:151]\n"
  "v_fmac_f32 v194, v26, v26\n"
  "v_smfmac_f32_16x16x32_bf16 v[200:203], v[222:223], v[236:239], v164\n"
  "s_waitcnt lgkmcnt(2)\n"
  "ds_read_b128 v[220:223], v3 offset:4400\n"
  "ds_read_b128 v[236:239], v2 offset:528\n"
  "v_add_f32 v193, v193, v27\n"
  "v_fmac_f32 v195, v27, v27\n"
  "v_add_f32 v192, v192, v28\n"
  "v_fmac_f32 v194, v28, v28\n"
  "v_smfmac_f32_16x16x32_bf16 v[200:203], v[216:217], v[224:227], v164\n"
  "s_waitcnt lgkmcnt(3)\n"
  "ds_read_b128 v[224:227], v2 offset:128\n"
  "v_add_f32 v193, v193, v29\n"
  "v_fmac_f32 v195, v29, v29\n"
  "v_add_f32 v192, v192, v30\n"
  "v_pk_add_f16 v212, v212, 1.0 op_sel_hi:[1,0]\n"
  "v_smfmac_f32_16x16x32_bf16 v[204:207], v[218:219], v[228:231], v164\n"
  "s_waitcnt lgkmcnt(2)\n"
  "ds_read_b128 v[216:219], v3 offset:4800\n"
  "ds_read_b128 v[228:231], v2 offset:336\n"
  "v_mov_b64 v[244:245], 0\n"
  "v_mov_b64 v[246:247], 0\n"
  "v_fmac_f32 v194, v30, v30\n"
  "v_mfma_f32_4x4x4_16b_f16 v[148:151], v[182:183], v[182:183], v[148:151]\n"
  "v_pk_add_f16 v213, v213, 1.0 op_sel_hi:[1,0]\n"
  "v_smfmac_f32_16x16x32_bf16 v[204:207], v[220:221], v[232:235], v164\n"
  "s_waitcnt lgkmcnt(3)\n"
  "ds_read_b128 v[232:235], v2 offset:544\n"
  "v_add_f32 v193, v193, v31\n"
  "v_fmac_f32 v195, v31, v31\n"
  "v_add_f32 v192, v192, v200\n"
  "v_fmac_f32 v194, v200, v200\n"
  "v_smfmac_f32_16x16x32_bf16 v[204:207], v[222:223], v[236:239], v164\n"
  "s_waitcnt lgkmcnt(2)\n"
  "ds_read_b128 v[220:223], v3 offset:5200\n"
  "ds_read_b128 v[236:239], v2 offset:144\n"
  "v_add_f32 v193, v193, v201\n"
  "v_fmac_f32 v195, v201, v201\n"
  "v_add_f32 v192, v192, v202\n"
  "v_pk_add_f16 v214, v214, 1.0 op_sel_hi:[1,0]\n"
  "v_smfmac_f32_16x16x32_bf16 v[240:243], v[216:217], v[224:227], v164\n"
  "s_waitcnt lgkmcnt(3)\n"
  "ds_read_b128 v[224:227], v2 offset:352\n"
  "v_mov_b64 v[248:249], 0\n"
  "v_mov_b64 v[250:251], 0\n"
  "v_fmac_f32 v194, v202, v202\n"
  "v_pk_add_f16 v215, v215, 1.0 op_sel_hi:[1,0]\n"
  "v_smfmac_f32_16x16x32_bf16 v[240:243], v[218:219], v[228:231], v164\n"
  "s_waitcnt lgkmcnt(2)\n"
  "ds_read_b128 v[216:219], v3 offset:5600\n"
  "ds_read_b128 v[228:231], v2 offset:560\n"
  "v_add_f32 v193, v193, v203\n"
  "v_fmac_f32 v195, v203, v203\n"
  "v_add_f32 v192, v192, v204\n"
  "v_fmac_f32 v194, v204, v204\n"
  "v_smfmac_f32_16x16x32_bf16 v[240:243], v[220:221], v[232:235], v164\n"
  "s_waitcnt lgkmcnt(3)\n"
  "ds_read_b128 v[232:235], v2 offset:160\n"
  "v_add_f32 v193, v193, v205\n"
  "v_fmac_f32 v195, v205, v205\n"
  "v_add_f32 v192, v192, v206\n"
  "v_rcp_f16 v208, v212\n"
  "v_smfmac_f32_16x16x32_bf16 v[244:247], v[222:223], v[236:239], v164\n"
  "s_waitcnt lgkmcnt(2)\n"
  "ds_read_b128 v[220:223], v3 offset:6000\n"
  "ds_read_b128 v[236:239], v2 offset:368\n"
  "v_mov_b64 v[252:253], 0\n"
  "v_mov_b64 v[254:255], 0\n"
  "v_fmac_f32 v194, v206, v206\n"
  "v_smfmac_f32_16x16x32_bf16 v[244:247], v[216:217], v[224:227], v164\n"
  "s_waitcnt lgkmcnt(3)\n"
  "ds_read_b128 v[224:227], v2 offset:576\n"
  "v_add_f32 v193, v193, v207\n"
  "v_fmac_f32 v195, v207, v207\n"
  "v_add_f32 v192, v192, v240\n"
  "v_fmac_f32 v194, v240, v240\n"
  "v_smfmac_f32_16x16x32_bf16 v[244:247], v[218:219], v[228:231], v164\n"
  "s_waitcnt lgkmcnt(2)\n"
  "ds_read_b128 v[216:219], v3 offset:6400\n"
  "ds_read_b128 v[228:231], v2 offset:176\n"
  "v_add_f32 v193, v193, v241\n"
  "v_fmac_f32 v195, v241, v241\n"
  "v_add_f32 v192, v192, v242\n"
  "v_rcp_f16 v209, v213\n"
  "v_smfmac_f32_16x16x32_bf16 v[248:251], v[220:221], v[232:235], v164\n"
  "s_waitcnt lgkmcnt(3)\n"
  "ds_read_b128 v[232:235], v2 offset:384\n"
  "v_fmac_f32 v194, v242, v242\n"
  "v_rcp_f16 v210, v214\n"
  "v_smfmac_f32_16x16x32_bf16 v[248:251], v[222:223], v[236:239], v164\n"
  "s_waitcnt lgkmcnt(2)\n"
  "ds_read_b128 v[220:223], v3 offset:6800\n"
  "ds_read_b128 v[236:239], v2 offset:592\n"
  "v_add_f32 v193, v193, v243\n"
  "v_fmac_f32 v195, v243, v243\n"
  "v_add_f32 v192, v192, v244\n"
  "v_fmac_f32 v194, v244, v244\n"
  "v_smfmac_f32_16x16x32_bf16 v[248:251], v[216:217], v[224:227], v164\n"
  "s_waitcnt lgkmcnt(3)\n"
  "v_add_f32 v193, v193, v245\n"
  "v_fmac_f32 v195, v245, v245\n"
  "v_add_f32 v192, v192, v246\n"
  "v_rcp_f16 v211, v215\n"
  "v_smfmac_f32_16x16x32_bf16 v[252:255], v[218:219], v[228:231], v164\n"
  "s_waitcnt lgkmcnt(1)\n"
  "v_fmac_f32 v194, v246, v246\n"
  "v_add_f32 v193, v193, v247\n"
  "v_fmac_f32 v195, v247, v247\n"
  "v_smfmac_f32_16x16x32_bf16 v[252:255], v[220:221], v[232:235], v164\n"
  "s_waitcnt lgkmcnt(0)\n"
  "v_add_f32 v192, v192, v248\n"
  "v_fmac_f32 v194, v248, v248\n"
  "v_add_f32 v193, v193, v249\n"
  "v_fmac_f32 v195, v249, v249\n"
  "v_smfmac_f32_16x16x32_bf16 v[252:255], v[222:223], v[236:239], v164\n"
  "v_add_f32 v192, v192, v250\n"
  "v_fmac_f32 v194, v250, v250\n"
  "v_add_f32 v193, v193, v251\n"
  "v_fmac_f32 v195, v251, v251\n"
  "s_mov_b64 vcc, 0\n"
  "s_cmp_lg_u32 s70, 7\n"
  "s_cbranch_scc1 .Lqs7%=\n"
  "s_barrier\n"
  "s_branch .Lqe7%=\n"
  ".Lqs7%=:\n"
  "s_mov_b64 exec, 1\n"
  "v_mov_b32 v199, 160144\n"
  "v_mov_b32 v197, 1\n"
  "ds_add_u32 v199, v197\n"
  "s_mov_b64 exec, -1\n"
  ".Lqe7%=:\n"
  "s_sub_u32 s70, s70, 1\n"
  "s_cmp_gt_u32 s70, 5\n"
  "s_cbranch_scc1 .Lq7%=\n"
  "s_mov_b64 vcc, -1\n"
  "s_bitcmp1_b32 s70, 0\n"
  "s_cselect_b32 s62, 28672, 0\n"
  "s_add_u32 s61, s67, s62\n"
  "s_mul_i32 s62, s70, 393216\n"
  "s_sub_u32 s60, s68, s62\n"
  ".Lq7%=:\n"
  "ds_read_b128 v[216:219], v0 offset:6144\n"
  "ds_read_b128 v[220:223], v0 offset:12288\n"
  "ds_read_b128 v[224:227], v0 offset:7168\n"
  "ds_read_b128 v[228:231], v0 offset:13312\n"
  "v_rcp_f16_sdwa v208, v212 dst_sel:WORD_1 dst_unused:UNUSED_PRESERVE src0_sel:WORD_1\n"
  "v_rcp_f16_sdwa v209, v213 dst_sel:WORD_1 dst_unused:UNUSED_PRESERVE src0_sel:WORD_1\n"
  "v_add_f32 v192, v192, v252\n"
  "v_fmac_f32 v194, v252, v252\n"
  "v_add_f32 v193, v193, v253\n"
  "v_fmac_f32 v195, v253, v253\n"
  "v_add_f32 v192, v192, v254\n"
  "v_fmac_f32 v194, v254, v254\n"
  "v_add_f32 v193, v193, v255\n"
  "v_fmac_f32 v195, v255, v255\n"
  "v_add_f32 v192, v192, v193\n"
  "v_rcp_f16_sdwa v210, v214 dst_sel:WORD_1 dst_unused:UNUSED_PRESERVE src0_sel:WORD_1\n"
  "v_add_f32 v194, v194, v195\n"
  "v_rcp_f16_sdwa v211, v215 dst_sel:WORD_1 dst_unused:UNUSED_PRESERVE src0_sel:WORD_1\n"
  "s_nop 0\n"
  "v_permlane32_swap_b32 v192, v194\n"
  "v_pk_mul_f16 v188, v188, v208\n"
  "v_add_f32 v192, v192, v194\n"
  "v_pk_mul_f16 v189, v189, v209\n"
  "v_mul_f32 v192, s63, v192\n"
  "v_pk_mul_f16 v190, v190, v210\n"
  "v_mov_b32 v194, v192\n"
  "v_pk_mul_f16 v191, v191, v211\n"
  "s_nop 0\n"
  "v_permlane32_swap_b32 v192, v194\n"
  "v_mfma_f32_4x4x4_16b_f16 v[148:151], v[188:189], v[188:189], v[148:151]\n"
  "v_fma_f32 v194, -v192, v192, v194\n"
  "s_nop 0\n"
  "v_mfma_f32_4x4x4_16b_f16 v[148:151], v[190:191], v[190:191], v[148:151]\n"
  "v_max_f32 v194, 0, v194\n"
  "v_add_f32 v194, s64, v194\n"
  "v_rsq_f32 v196, v194\n"
  "s_nop 0\n"
  "v_mul_f32 v196, v5, v196\n"
  "v_mul_f32 v198, v192, v196\n"
  "v_fma_f32 v8, v8, v196, -v198\n"
  "v_fma_f32 v9, v9, v196, -v198\n"
  "v_fma_f32 v10, v10, v196, -v198\n"
  "v_fma_f32 v11, v11, v196, -v198\n"
  "s_cbranch_vccz .Ld8%=\n"
  "s_lshl_b32 s94, s70, 2\n"
  "s_sub_u32 s94, 24, s94\n"
  "s_mov_b32 s96, 1048576\n"
  ".Lw9%=:\n"
  "v_mov_b32 v199, 160144\n"
  "ds_read_b32 v199, v199\n"
  "s_waitcnt lgkmcnt(0)\n"
  "v_readfirstlane_b32 s92, v199\n"
  "s_nop 1\n"
  "s_cmp_ge_u32 s92, s94\n"
  "s_cbranch_scc1 .Lwd9%=\n"
  "s_sleep 1\n"
  "s_sub_u32 s96, s96, 1\n"
  "s_cmp_lg_u32 s96, 0\n"
  "s_cbranch_scc1 .Lw9%=\n"
  ".Lwd9%=:\n"
  "s_add_u32 s61, s61, 7168\n"
  "s_mov_b32 m0, s61\n"
  "s_add_u32 s60, s60, 0x18000\n"
  "buffer_load_dwordx4 v4, s[40:43], s60 offen  lds\n"
  "s_cmp_eq_u32 s73, 3\n"
  "s_cbranch_scc1 .Ld8%=\n"
  "s_add_u32 m0, s61, 4096\n"
  "s_nop 0\n"
  "buffer_load_dwordx4 v6, s[40:43], s60 offen  lds\n"
  ".Ld8%=:\n"
  "v_cvt_pk_bf16_f32 v8, v8, v9\n"
  "v_cvt_pk_bf16_f32 v9, v10, v11\n"
  "v_fma_f32 v12, v12, v196, -v198\n"
  "v_fma_f32 v13, v13, v196, -v198\n"
  "v_fma_f32 v14, v14, v196, -v198\n"
  "v_fma_f32 v15, v15, v196, -v198\n"
  "v_cvt_pk_bf16_f32 v10, v12, v13\n"
  "v_cvt_pk_bf16_f32 v11, v14, v15\n"
  "v_fma_f32 v16, v16, v196, -v198\n"
  "s_cbranch_vccz .Ld10%=\n"
  "s_add_u32 s61, s61, 7168\n"
  "s_mov_b32 m0, s61\n"
  "s_add_u32 s60, s60, 0x18000\n"
  "buffer_load_dwordx4 v4, s[40:43], s60 offen  lds\n"
  "s_cmp_eq_u32 s73, 3\n"
  "s_cbranch_scc1 .Ld10%=\n"
  "s_add_u32 m0, s61, 4096\n"
  "s_nop 0\n"
  "buffer_load_dwordx4 v6, s[40:43], s60 offen  lds\n"
  ".Ld10%=:\n"
  "v_fma_f32 v17, v17, v196, -v198\n"
  "v_fma_f32 v18, v18, v196, -v198\n"
  "v_fma_f32 v19, v19, v196, -v198\n"
  "s_waitcnt lgkmcnt(3)\n"
  "v_mfma_f32_32x32x16_bf16 v[152:167], v[8:11], v[216:219], 0\n"
  "s_waitcnt lgkmcnt(2)\n"
  "ds_read_b128 v[216:219], v0 offset:8192\n"
  "v_cvt_pk_bf16_f32 v12, v16, v17\n"
  "v_cvt_pk_bf16_f32 v13, v18, v19\n"
  "v_fma_f32 v20, v20, v196, -v198\n"
  "s_cbranch_vccz .Ld11%=\n"
  "s_add_u32 s61, s61, 7168\n"
  "s_mov_b32 m0, s61\n"
  "s_add_u32 s60, s60, 0x18000\n"
  "buffer_load_dwordx4 v4, s[40:43], s60 offen  lds\n"
  "s_cmp_eq_u32 s73, 3\n"
  "s_cbranch_scc1 .Ld11%=\n"
  "s_add_u32 m0, s61, 4096\n"
  "s_nop 0\n"
  "buffer_load_dwordx4 v6, s[40:43], s60 offen  lds\n"
  ".Ld11%=:\n"
  "v_fma_f32 v21, v21, v196, -v198\n"
  "v_fma_f32 v22, v22, v196, -v198\n"
  "v_fma_f32 v23, v23, v196, -v198\n"
  "v_mfma_f32_32x32x16_bf16 v[168:183], v[8:11], v[220:223], 0\n"
  "s_waitcnt lgkmcnt(2)\n"
  "ds_read_b128 v[220:223], v0 offset:14336\n"
  "v_cvt_pk_bf16_f32 v14, v20, v21\n"
  "v_cvt_pk_bf16_f32 v15, v22, v23\n"
  "v_fma_f32 v24, v24, v196, -v198\n"
  "s_cbranch_vccz .Ld12%=\n"
  "s_add_u32 s61, s61, 7168\n"
  "s_mov_b32 m0, s61\n"
  "s_add_u32 s60, s60, 0x18000\n"
  "buffer_load_dwordx4 v4, s[40:43], s60 offen  lds\n"
  "s_cmp_eq_u32 s73, 3\n"
  "s_cbranch_scc1 .Ld12%=\n"
  "s_add_u32 m0, s61, 4096\n"
  "s_nop 0\n"
  "buffer_load_dwordx4 v6, s[40:43], s60 offen  lds\n"
  ".Ld12%=:\n"
  "v_fma_f32 v25, v25, v196, -v198\n"
  "v_fma_f32 v26, v26, v196, -v198\n"
  "v_fma_f32 v27, v27, v196, -v198\n"
  "v_mfma_f32_32x32x16_bf16 v[152:167], v[12:15], v[224:227], v[152:167]\n"
  "s_waitcnt lgkmcnt(2)\n"
  "ds_read_b128 v[224:227], v0 offset:9216\n"
  "v_cvt_pk_bf16_f32 v16, v24, v25\n"
  "v_cvt_pk_bf16_f32 v17, v26, v27\n"
  "v_fma_f32 v28, v28, v196, -v198\n"
  "v_fma_f32 v29, v29, v196, -v198\n"
  "v_fma_f32 v30, v30, v196, -v198\n"
  "v_fma_f32 v31, v31, v196, -v198\n"
  "v_mfma_f32_32x32x16_bf16 v[168:183], v[12:15], v[228:231], v[168:183]\n"
  "s_waitcnt lgkmcnt(2)\n"
  "ds_read_b128 v[228:231], v0 offset:15360\n"
  "v_cvt_pk_bf16_f32 v18, v28, v29\n"
  "v_cvt_pk_bf16_f32 v19, v30, v31\n"
  "v_fma_f32 v200, v200, v196, -v198\n"
  "v_fma_f32 v201, v201, v196, -v198\n"
  "v_fma_f32 v202, v202, v196, -v198\n"
  "v_fma_f32 v203, v203, v196, -v198\n"
  "v_mfma_f32_32x32x16_bf16 v[152:167], v[16:19], v[216:219], v[152:167]\n"
  "s_waitcnt lgkmcnt(2)\n"
  "ds_read_b128 v[216:219], v0 offset:10240\n"
  "v_cvt_pk_bf16_f32 v20, v200, v201\n"
  "v_cvt_pk_bf16_f32 v21, v202, v203\n"
  "v_fma_f32 v204, v204, v196, -v198\n"
  "v_fma_f32 v205, v205, v196, -v198\n"
  "v_fma_f32 v206, v206, v196, -v198\n"
  "v_fma_f32 v207, v207, v196, -v198\n"
  "v_mfma_f32_32x32x16_bf16 v[168:183], v[16:19], v[220:223], v[168:183]\n"
  "s_waitcnt lgkmcnt(2)\n"
  "ds_read_b128 v[220:223], v0 offset:11264\n"
  "v_cvt_pk_bf16_f32 v22, v204, v205\n"
  "v_cvt_pk_bf16_f32 v23, v206, v207\n"
  "v_fma_f32 v240, v240, v196, -v198\n"
  "v_fma_f32 v241, v241, v196, -v198\n"
  "v_fma_f32 v242, v242, v196, -v198\n"
  "v_fma_f32 v243, v243, v196, -v198\n"
  "v_mfma_f32_32x32x16_bf16 v[152:167], v[20:23], v[224:227], v[152:167]\n"
  "s_waitcnt lgkmcnt(2)\n"
  "ds_read_b128 v[224:227], v0 offset:16384\n"
  "v_cvt_pk_bf16_f32 v24, v240, v241\n"
  "v_cvt_pk_bf16_f32 v25, v242, v243\n"
  "v_fma_f32 v244, v244, v196, -v198\n"
  "v_fma_f32 v245, v245, v196, -v198\n"
  "v_fma_f32 v246, v246, v196, -v198\n"
  "v_fma_f32 v247, v247, v196, -v198\n"
  "v_mfma_f32_32x32x16_bf16 v[168:183], v[20:23], v[228:231], v[168:183]\n"
  "s_waitcnt lgkmcnt(2)\n"
  "ds_read_b128 v[228:231], v0 offset:17408\n"
  "v_cvt_pk_bf16_f32 v26, v244, v245\n"
  "v_cvt_pk_bf16_f32 v27, v246, v247\n"
  "v_fma_f32 v248, v248, v196, -v198\n"
  "v_fma_f32 v249, v249, v196, -v198\n"
  "v_fma_f32 v250, v250, v196, -v198\n"
  "v_fma_f32 v251, v251, v196, -v198\n"
  "v_mfma_f32_32x32x16_bf16 v[152:167], v[24:27], v[216:219], v[152:167]\n"
  "s_waitcnt lgkmcnt(2)\n"
  "ds_read_b128 v[216:219], v0 offset:18432\n"
  "v_cvt_pk_bf16_f32 v28, v248, v249\n"
  "v_cvt_pk_bf16_f32 v29, v250, v251\n"
  "v_fma_f32 v252, v252, v196, -v198\n"
  "v_fma_f32 v253, v253, v196, -v198\n"
  "v_fma_f32 v254, v254, v196, -v198\n"
  "v_fma_f32 v255, v255, v196, -v198\n"
  "v_cvt_pk_bf16_f32 v30, v252, v253\n"
  "v_cvt_pk_bf16_f32 v31, v254, v255\n"
  "s_nop 0\n"
  "s_nop 0\n"
  "v_mfma_f32_32x32x16_bf16 v[152:167], v[28:31], v[220:223], v[152:167]\n"
  "ds_read_b128 v[220:223], v0 offset:19456\n"
  "s_cmp_lg_u32 s55, 4\n"
  "s_cbranch_scc1 .Lc10%=\n"
  "v_mov_b64 v[32:33], v[8:9]\n"
  "v_mov_b64 v[34:35], v[10:11]\n"
  "v_mov_b64 v[36:37], v[12:13]\n"
  "v_mov_b64 v[38:39], v[14:15]\n"
  "v_mov_b64 v[40:41], v[16:17]\n"
  "v_mov_b64 v[42:43], v[18:19]\n"
  "v_mov_b64 v[44:45], v[20:21]\n"
  "v_mov_b64 v[46:47], v[22:23]\n"
  "v_mov_b64 v[48:49], v[24:25]\n"
  "v_mov_b64 v[50:51], v[26:27]\n"
  "v_mov_b64 v[52:53], v[28:29]\n"
  "v_mov_b64 v[54:55], v[30:31]\n"
  "s_branch .Lc1e%=\n"
  ".Lc10%=:\n"
  "s_cmp_lg_u32 s55, 3\n"
  "s_cbranch_scc1 .Lc11%=\n"
  "v_mov_b64 v[56:57], v[8:9]\n"
  "v_mov_b64 v[58:59], v[10:11]\n"
  "v_mov_b64 v[60:61], v[12:13]\n"
  "v_mov_b64 v[62:63], v[14:15]\n"
  "v_mov_b64 v[64:65], v[16:17]\n"
  "v_mov_b64 v[66:67], v[18:19]\n"
  "v_mov_b64 v[68:69], v[20:21]\n"
  "v_mov_b64 v[70:71], v[22:23]\n"
  "v_mov_b64 v[72:73], v[24:25]\n"
  "v_mov_b64 v[74:75], v[26:27]\n"
  "v_mov_b64 v[76:77], v[28:29]\n"
  "v_mov_b64 v[78:79], v[30:31]\n"
  "s_branch .Lc1e%=\n"
  ".Lc11%=:\n"
  "s_cmp_lg_u32 s55, 2\n"
  "s_cbranch_scc1 .Lc12%=\n"
  "v_mov_b64 v[80:81], v[8:9]\n"
  "v_mov_b64 v[82:83], v[10:11]\n"
  "v_mov_b64 v[84:85], v[12:13]\n"
  "v_mov_b64 v[86:87], v[14:15]\n"
  "v_mov_b64 v[88:89], v[16:17]\n"
  "v_mov_b64 v[90:91], v[18:19]\n"
  "v_mov_b64 v[92:93], v[20:21]\n"
  "v_mov_b64 v[94:95], v[22:23]\n"
  "v_mov_b64 v[96:97], v[24:25]\n"
  "v_mov_b64 v[98:99], v[26:27]\n"
  "v_mov_b64 v[100:101], v[28:29]\n"
  "v_mov_b64 v[102:103], v[30:31]\n"
  "s_branch .Lc1e%=\n"
  ".Lc12%=:\n"
  ".Lc1e%=:\n"
  "s_cmp_eq_u32 s55, 1\n"
  "s_cbranch_scc1 .Lsp%=\n"
  "s_waitcnt lgkmcnt(3)\n"
  "v_mfma_f32_32x32x16_bf16 v[168:183], v[24:27], v[224:227], v[168:183]\n"
  "s_waitcnt lgkmcnt(2)\n"
  "s_nop 5\n"
  "v_lshlrev_b32_sdwa v240, 1, v160 dst_sel:DWORD dst_unused:UNUSED_PAD src0_sel:DWORD src1_sel:WORD_1\n"
  "v_cvt_pk_f16_f32 v184, v152, v153\n"
  "v_lshlrev_b32_sdwa v241, 1, v161 dst_sel:DWORD dst_unused:UNUSED_PAD src0_sel:DWORD src1_sel:WORD_1\n"
  "v_cvt_pk_f16_f32 v185, v154, v155\n"
  "s_setprio 2\n"
  "v_lshlrev_b32_sdwa v242, 1, v162 dst_sel:DWORD dst_unused:UNUSED_PAD src0_sel:DWORD src1_sel:WORD_1\n"
  "v_cvt_pk_f16_f32 v186, v156, v157\n"
  "v_lshlrev_b32_sdwa v243, 1, v163 dst_sel:DWORD dst_unused:UNUSED_PAD src0_sel:DWORD src1_sel:WORD_1\n"
  "v_cvt_pk_f16_f32 v187, v158, v159\n"
  "v_lshlrev_b32_sdwa v244, 1, v164 dst_sel:DWORD dst_unused:UNUSED_PAD src0_sel:DWORD src1_sel:WORD_1\n"
  "ds_read_b128 v[224:227], v0 offset:20480\n"
  "v_lshlrev_b32_sdwa v245, 1, v165 dst_sel:DWORD dst_unused:UNUSED_PAD src0_sel:DWORD src1_sel:WORD_1\n"
  "v_mfma_f32_32x32x16_bf16 v[168:183], v[28:31], v[228:231], v[168:183]\n"
  "s_waitcnt lgkmcnt(2)\n"
  "v_lshlrev_b32_sdwa v246, 1, v166 dst_sel:DWORD dst_unused:UNUSED_PAD src0_sel:DWORD src1_sel:WORD_1\n"
  "v_lshlrev_b32_sdwa v247, 1, v167 dst_sel:DWORD dst_unused:UNUSED_PAD src0_sel:DWORD src1_sel:WORD_1\n"
  "v_pk_fma_f16 v208, v184, v184, s56\n"
  "v_pk_fma_f16 v209, v185, v185, s56\n"
  "v_pk_fma_f16 v210, v186, v186, s56\n"
  "v_pk_fma_f16 v211, v187, v187, s56\n"
  "v_pk_mul_f16 v208, v184, v208\n"
  "v_pk_mul_f16 v209, v185, v209\n"
  "v_pk_mul_f16 v210, v186, v210\n"
  "ds_read_b128 v[228:231], v0 offset:21504\n"
  "ds_read_u16 v240, v240\n"
  "v_pk_mul_f16 v211, v187, v211\n"
  "v_mfma_f32_32x32x16_bf16 v[152:167], v[8:11], v[216:219], 0\n"
  "s_waitcnt lgkmcnt(3)\n"
  "v_exp_f16 v212, v208\n"
  "v_exp_f16 v213, v209\n"
  "v_exp_f16 v214, v210\n"
  "v_exp_f16 v215, v211\n"
  "ds_read_b128 v[216:219], v0 offset:22528\n"
  "ds_read_u16 v241, v241\n"
  "v_exp_f16_sdwa v212, v208 dst_sel:WORD_1 dst_unused:UNUSED_PRESERVE src0_sel:WORD_1\n"
  "v_mfma_f32_32x32x16_bf16 v[152:167], v[12:15], v[220:223], v[152:167]\n"
  "s_waitcnt lgkmcnt(4)\n"
  "v_exp_f16_sdwa v213, v209 dst_sel:WORD_1 dst_unused:UNUSED_PRESERVE src0_sel:WORD_1\n"
  "v_exp_f16_sdwa v214, v210 dst_sel:WORD_1 dst_unused:UNUSED_PRESERVE src0_sel:WORD_1\n"
  "v_exp_f16_sdwa v215, v211 dst_sel:WORD_1 dst_unused:UNUSED_PRESERVE src0_sel:WORD_1\n"
  "v_pk_add_f16 v212, v212, 1.0 op_sel_hi:[1,0]\n"
  "v_pk_add_f16 v213, v213, 1.0 op_sel_hi:[1,0]\n"
  "v_pk_add_f16 v214, v214, 1.0 op_sel_hi:[1,0]\n"
  "ds_read_b128 v[220:223], v0 offset:23552\n"
  "ds_read_u16 v242, v242\n"
  "ds_read_u16 v243, v243\n"
  "v_pk_add_f16 v215, v215, 1.0 op_sel_hi:[1,0]\n"
  "v_mfma_f32_32x32x16_bf16 v[152:167], v[16:19], v[224:227], v[152:167]\n"
  "s_waitcnt lgkmcnt(6)\n"
  "v_rcp_f16 v208, v212\n"
  "v_rcp_f16 v209, v213\n"
  "v_rcp_f16 v210, v214\n"
  "v_rcp_f16 v211, v215\n"
  "ds_read_b128 v[224:227], v0 offset:24576\n"
  "ds_read_u16 v244, v244\n"
  "v_rcp_f16_sdwa v208, v212 dst_sel:WORD_1 dst_unused:UNUSED_PRESERVE src0_sel:WORD_1\n"
  "v_mfma_f32_32x32x16_bf16 v[152:167], v[20:23], v[228:231], v[152:167]\n"
  "s_waitcnt lgkmcnt(6)\n"
  "v_rcp_f16_sdwa v209, v213 dst_sel:WORD_1 dst_unused:UNUSED_PRESERVE src0_sel:WORD_1\n"
  "v_rcp_f16_sdwa v210, v214 dst_sel:WORD_1 dst_unused:UNUSED_PRESERVE src0_sel:WORD_1\n"
  "v_rcp_f16_sdwa v211, v215 dst_sel:WORD_1 dst_unused:UNUSED_PRESERVE src0_sel:WORD_1\n"
  "v_pk_mul_f16 v184, v184, v208\n"
  "v_pk_mul_f16 v185, v185, v209\n"
  "v_pk_mul_f16 v186, v186, v210\n"
  "ds_read_b128 v[228:231], v0 offset:25600\n"
  "ds_read_u16 v245, v245\n"
  "v_pk_mul_f16 v187, v187, v211\n"
  "v_mfma_f32_32x32x16_bf16 v[152:167], v[24:27], v[216:219], v[152:167]\n"
  "s_waitcnt lgkmcnt(6)\n"
  "v_lshlrev_b32_sdwa v248, 1, v176 dst_sel:DWORD dst_unused:UNUSED_PAD src0_sel:DWORD src1_sel:WORD_1\n"
  "v_cvt_pk_f16_f32 v188, v168, v169\n"
  "v_lshlrev_b32_sdwa v249, 1, v177 dst_sel:DWORD dst_unused:UNUSED_PAD src0_sel:DWORD src1_sel:WORD_1\n"
  "v_cvt_pk_f16_f32 v189, v170, v171\n"
  "v_lshlrev_b32_sdwa v250, 1, v178 dst_sel:DWORD dst_unused:UNUSED_PAD src0_sel:DWORD src1_sel:WORD_1\n"
  "v_cvt_pk_f16_f32 v190, v172, v173\n"
  "v_lshlrev_b32_sdwa v251, 1, v179 dst_sel:DWORD dst_unused:UNUSED_PAD src0_sel:DWORD src1_sel:WORD_1\n"
  "v_cvt_pk_f16_f32 v191, v174, v175\n"
  "v_lshlrev_b32_sdwa v252, 1, v180 dst_sel:DWORD dst_unused:UNUSED_PAD src0_sel:DWORD src1_sel:WORD_1\n"
  "v_mfma_f32_4x4x4_16b_f16 v[104:107], v[184:185], v[184:185], v[104:107]\n"
  "ds_read_b128 v[216:219], v0 offset:26624\n"
  "ds_read_u16 v246, v246\n"
  "ds_read_u16 v247, v247\n"
  "v_lshlrev_b32_sdwa v253, 1, v181 dst_sel:DWORD dst_unused:UNUSED_PAD src0_sel:DWORD src1_sel:WORD_1\n"
  "v_mfma_f32_32x32x16_bf16 v[152:167], v[28:31], v[220:223], v[152:167]\n"
  "s_waitcnt lgkmcnt(6)\n"
  "v_lshlrev_b32_sdwa v254, 1, v182 dst_sel:DWORD dst_unused:UNUSED_PAD src0_sel:DWORD src1_sel:WORD_1\n"
  "v_lshlrev_b32_sdwa v255, 1, v183 dst_sel:DWORD dst_unused:UNUSED_PAD src0_sel:DWORD src1_sel:WORD_1\n"
  "v_pk_fma_f16 v208, v188, v188, s56\n"
  "v_pk_fma_f16 v209, v189, v189, s56\n"
  "v_pk_fma_f16 v210, v190, v190, s56\n"
  "v_pk_fma_f16 v211, v191, v191, s56\n"
  "v_pk_mul_f16 v208, v188, v208\n"
  "v_pk_mul_f16 v209, v189, v209\n"
  "v_pk_mul_f16 v210, v190, v210\n"
  "v_mfma_f32_4x4x4_16b_f16 v[104:107], v[186:187], v[186:187], v[104:107]\n"
  "ds_read_b128 v[220:223], v0 offset:27648\n"
  "ds_read_u16 v248, v248\n"
  "v_pk_mul_f16 v211, v191, v211\n"
  "v_mfma_f32_32x32x16_bf16 v[168:183], v[8:11], v[224:227], 0\n"
  "s_waitcnt lgkmcnt(6)\n"
  "v_exp_f16 v212, v208\n"
  "v_exp_f16 v213, v209\n"
  "v_exp_f16 v214, v210\n"
  "v_exp_f16 v215, v211\n"
  "v_mfma_f32_4x4x4_16b_f16 v[104:107], v[240:241], v[240:241], v[104:107]\n"
  "ds_read_b128 v[224:227], v0 offset:28672\n"
  "ds_read_u16 v249, v249\n"
  "v_exp_f16_sdwa v212, v208 dst_sel:WORD_1 dst_unused:UNUSED_PRESERVE src0_sel:WORD_1\n"
  "v_mfma_f32_32x32x16_bf16 v[168:183], v[12:15], v[228:231], v[168:183]\n"
  "s_waitcnt lgkmcnt(6)\n"
  "v_exp_f16_sdwa v213, v209 dst_sel:WORD_1 dst_unused:UNUSED_PRESERVE src0_sel:WORD_1\n"
  "v_exp_f16_sdwa v214, v210 dst_sel:WORD_1 dst_unused:UNUSED_PRESERVE src0_sel:WORD_1\n"
  "v_exp_f16_sdwa v215, v211 dst_sel:WORD_1 dst_unused:UNUSED_PRESERVE src0_sel:WORD_1\n"
  "v_pk_add_f16 v212, v212, 1.0 op_sel_hi:[1,0]\n"
  "v_pk_add_f16 v213, v213, 1.0 op_sel_hi:[1,0]\n"
  "v_pk_add_f16 v214, v214, 1.0 op_sel_hi:[1,0]\n"
  "v_mfma_f32_4x4x4_16b_f16 v[104:107], v[242:243], v[242:243], v[104:107]\n"
  "ds_read_b128 v[228:231], v0 offset:29696\n"
  "ds_read_u16 v250, v250\n"
  "ds_read_u16 v251, v251\n"
  "v_pk_add_f16 v215, v215, 1.0 op_sel_hi:[1,0]\n"
  "v_mfma_f32_32x32x16_bf16 v[168:183], v[16:19], v[216:219], v[168:183]\n"
  "s_waitcnt lgkmcnt(6)\n"
  "v_rcp_f16 v208, v212\n"
  "v_rcp_f16 v209, v213\n"
  "v_rcp_f16 v210, v214\n"
  "v_rcp_f16 v211, v215\n"
  "v_mfma_f32_4x4x4_16b_f16 v[104:107], v[244:245], v[244:245], v[104:107]\n"
  "s_cmp_lt_i32 s70, 5\n"
  "s_cbranch_scc1 .Lnwr0%=\n"
  "s_mov_b32 s94, 4\n"
  "s_mov_b32 s96, 1048576\n"
  ".Lw13%=:\n"
  "v_mov_b32 v199, 160152\n"
  "ds_read_b32 v199, v199\n"
  "s_waitcnt lgkmcnt(0)\n"
  "v_readfirstlane_b32 s92, v199\n"
  "s_nop 1\n"
  "s_cmp_ge_u32 s92, s94\n"
  "s_cbranch_scc1 .Lwd13%=\n"
  "s_sleep 1\n"
  "s_sub_u32 s96, s96, 1\n"
  "s_cmp_lg_u32 s96, 0\n"
  "s_cbranch_scc1 .Lw13%=\n"
  ".Lwd13%=:\n"
  ".Lnwr0%=:\n"
  "ds_read_b128 v[216:219], v0 offset:30720\n"
  "ds_read_u16 v252, v252\n"
  "v_rcp_f16_sdwa v208, v212 dst_sel:WORD_1 dst_unused:UNUSED_PRESERVE src0_sel:WORD_1\n"
  "v_mfma_f32_32x32x16_bf16 v[168:183], v[20:23], v[220:223], v[168:183]\n"
  "s_waitcnt lgkmcnt(6)\n"
  "v_rcp_f16_sdwa v209, v213 dst_sel:WORD_1 dst_unused:UNUSED_PRESERVE src0_sel:WORD_1\n"
  "s_setprio 1\n"
  "v_rcp_f16_sdwa v210, v214 dst_sel:WORD_1 dst_unused:UNUSED_PRESERVE src0_sel:WORD_1\n"
  "v_rcp_f16_sdwa v211, v215 dst_sel:WORD_1 dst_unused:UNUSED_PRESERVE src0_sel:WORD_1\n"
  "v_pk_mul_f16 v188, v188, v208\n"
  "v_pk_mul_f16 v189, v189, v209\n"
  "v_pk_mul_f16 v190, v190, v210\n"
  "v_mfma_f32_4x4x4_16b_f16 v[104:107], v[246:247], v[246:247], v[104:107]\n"
  "ds_read_b128 v[220:223], v0 offset:31744\n"
  "ds_read_u16 v253, v253\n"
  "v_pk_mul_f16 v191, v191, v211\n"
  "v_mfma_f32_32x32x16_bf16 v[168:183], v[24:27], v[224:227], v[168:183]\n"
  "s_waitcnt lgkmcnt(6)\n"
  "v_lshlrev_b32_sdwa v240, 1, v160 dst_sel:DWORD dst_unused:UNUSED_PAD src0_sel:DWORD src1_sel:WORD_1\n"
  "v_cvt_pk_f16_f32 v184, v152, v153\n"
  "s_cmp_lg_u32 s55, 4\n"
  "s_cbranch_scc1 .Lnz8%=\n"
  "v_mov_b64 v[144:145], 0\n"
  "v_mov_b64 v[146:147], 0\n"
  "v_mov_b64 v[148:149], 0\n"
  "v_mov_b64 v[150:151], 0\n"
  ".Lnz8%=:\n"
  "s_cbranch_vccz .Lnp1%=\n"
  "s_waitcnt vmcnt(0)\n"
  "s_mov_b64 exec, 1\n"
  "v_mov_b32 v199, 160148\n"
  "v_mov_b32 v197, 1\n"
  "ds_add_u32 v199, v197\n"
  "s_mov_b64 exec, -1\n"
  ".Lnp1%=:\n"
  "v_lshlrev_b32_sdwa v241, 1, v161 dst_sel:DWORD dst_unused:UNUSED_PAD src0_sel:DWORD src1_sel:WORD_1\n"
  "v_cvt_pk_f16_f32 v185, v154, v155\n"
  "v_lshlrev_b32_sdwa v242, 1, v162 dst_sel:DWORD dst_unused:UNUSED_PAD src0_sel:DWORD src1_sel:WORD_1\n"
  "v_cvt_pk_f16_f32 v186, v156, v157\n"
  "v_lshlrev_b32_sdwa v243, 1, v163 dst_sel:DWORD dst_unused:UNUSED_PAD src0_sel:DWORD src1_sel:WORD_1\n"
  "v_cvt_pk_f16_f32 v187, v158, v159\n"
  "v_lshlrev_b32_sdwa v244, 1, v164 dst_sel:DWORD dst_unused:UNUSED_PAD src0_sel:DWORD src1_sel:WORD_1\n"
  "v_mfma_f32_4x4x4_16b_f16 v[108:111], v[188:189], v[188:189], v[108:111]\n"
  "ds_read_b128 v[224:227], v0 offset:32768\n"
  "ds_read_u16 v254, v254\n"
  "ds_read_u16 v255, v255\n"
  "v_lshlrev_b32_sdwa v245, 1, v165 dst_sel:DWORD dst_unused:UNUSED_PAD src0_sel:DWORD src1_sel:WORD_1\n"
  "v_mfma_f32_32x32x16_bf16 v[168:183], v[28:31], v[228:231], v[168:183]\n"
  "s_waitcnt lgkmcnt(6)\n"
  "v_lshlrev_b32_sdwa v246, 1, v166 dst_sel:DWORD dst_unused:UNUSED_PAD src0_sel:DWORD src1_sel:WORD_1\n"
  "v_lshlrev_b32_sdwa v247, 1, v167 dst_sel:DWORD dst_unused:UNUSED_PAD src0_sel:DWORD src1_sel:WORD_1\n"
  "v_pk_fma_f16 v208, v184, v184, s56\n"
  "v_pk_fma_f16 v209, v185, v185, s56\n"
  "v_pk_fma_f16 v210, v186, v186, s56\n"
  "v_pk_fma_f16 v211, v187, v187, s56\n"
  "v_pk_mul_f16 v208, v184, v208\n"
  "v_pk_mul_f16 v209, v185, v209\n"
  "v_pk_mul_f16 v210, v186, v210\n"
  "v_mfma_f32_4x4x4_16b_f16 v[108:111], v[190:191], v[190:191], v[108:111]\n"
  "ds_read_b128 v[228:231], v0 offset:33792\n"
  "ds_read_u16 v240, v240\n"
  "v_pk_mul_f16 v211, v187, v211\n"
  "v_mfma_f32_32x32x16_bf16 v[152:167], v[8:11], v[216:219], 0\n"
  "s_waitcnt lgkmcnt(6)\n"
  "v_exp_f16 v212, v208\n"
  "v_exp_f16 v213, v209\n"
  "v_exp_f16 v214, v210\n"
  "v_exp_f16 v215, v211\n"
  "v_mfma_f32_4x4x4_16b_f16 v[108:111], v[248:249], v[248:249], v[108:111]\n"
  "ds_read_b128 v[216:219], v0 offset:34816\n"
  "ds_read_u16 v241, v241\n"
  "v_exp_f16_sdwa v212, v208 dst_sel:WORD_1 dst_unused:UNUSED_PRESERVE src0_sel:WORD_1\n"
  "v_mfma_f32_32x32x16_bf16 v[152:167], v[12:15], v[220:223], v[152:167]\n"
  "s_waitcnt lgkmcnt(6)\n"
  "v_exp_f16_sdwa v213, v209 dst_sel:WORD_1 dst_unused:UNUSED_PRESERVE src0_sel:WORD_1\n"
  "v_exp_f16_sdwa v214, v210 dst_sel:WORD_1 dst_unused:UNUSED_PRESERVE src0_sel:WORD_1\n"
  "v_exp_f16_sdwa v215, v211 dst_sel:WORD_1 dst_unused:UNUSED_PRESERVE src0_sel:WORD_1\n"
  "v_pk_add_f16 v212, v212, 1.0 op_sel_hi:[1,0]\n"
  "v_pk_add_f16 v213, v213, 1.0 op_sel_hi:[1,0]\n"
  "v_pk_add_f16 v214, v214, 1.0 op_sel_hi:[1,0]\n"
  "v_mfma_f32_4x4x4_16b_f16 v[108:111], v[250:251], v[250:251], v[108:111]\n"
  "ds_read_b128 v[220:223], v0 offset:35840\n"
  "ds_read_u16 v242, v242\n"
  "ds_read_u16 v243, v243\n"
  "v_pk_add_f16 v215, v215, 1.0 op_sel_hi:[1,0]\n"
  "v_mfma_f32_32x32x16_bf16 v[152:167], v[16:19], v[224:227], v[152:167]\n"
  "s_waitcnt lgkmcnt(6)\n"
  "v_rcp_f16 v208, v212\n"
  "v_rcp_f16 v209, v213\n"
  "v_rcp_f16 v210, v214\n"
  "v_rcp_f16 v211, v215\n"
  "v_mfma_f32_4x4x4_16b_f16 v[108:111], v[252:253], v[252:253], v[108:111]\n"
  "ds_read_b128 v[224:227], v0 offset:36864\n"
  "ds_read_u16 v244, v244\n"
  "v_rcp_f16_sdwa v208, v212 dst_sel:WORD_1 dst_unused:UNUSED_PRESERVE src0_sel:WORD_1\n"
  "v_mfma_f32_32x32x16_bf16 v[152:167], v[20:23], v[228:231], v[152:167]\n"
  "s_waitcnt lgkmcnt(6)\n"
  "v_rcp_f16_sdwa v209, v213 dst_sel:WORD_1 dst_unused:UNUSED_PRESERVE src0_sel:WORD_1\n"
  "v_rcp_f16_sdwa v210, v214 dst_sel:WORD_1 dst_unused:UNUSED_PRESERVE src0_sel:WORD_1\n"
  "v_rcp_f16_sdwa v211, v215 dst_sel:WORD_1 dst_unused:UNUSED_PRESERVE src0_sel:WORD_1\n"
  "v_pk_mul_f16 v184, v184, v208\n"
  "v_pk_mul_f16 v185, v185, v209\n"
  "v_pk_mul_f16 v186, v186, v210\n"
  "v_mfma_f32_4x4x4_16b_f16 v[108:111], v[254:255], v[254:255], v[108:111]\n"
  "ds_read_b128 v[228:231], v0 offset:37888\n"
  "ds_read_u16 v245, v245\n"
  "v_pk_mul_f16 v187, v187, v211\n"
  "v_mfma_f32_32x32x16_bf16 v[152:167], v[24:27], v[216:219], v[152:167]\n"
  "s_waitcnt lgkmcnt(6)\n"
  "v_lshlrev_b32_sdwa v248, 1, v176 dst_sel:DWORD dst_unused:UNUSED_PAD src0_sel:DWORD src1_sel:WORD_1\n"
  "v_cvt_pk_f16_f32 v188, v168, v169\n"
  "v_lshlrev_b32_sdwa v249, 1, v177 dst_sel:DWORD dst_unused:UNUSED_PAD src0_sel:DWORD src1_sel:WORD_1\n"
  "v_cvt_pk_f16_f32 v189, v170, v171\n"
  "v_lshlrev_b32_sdwa v250, 1, v178 dst_sel:DWORD dst_unused:UNUSED_PAD src0_sel:DWORD src1_sel:WORD_1\n"
  "v_cvt_pk_f16_f32 v190, v172, v173\n"
  "v_lshlrev_b32_sdwa v251, 1, v179 dst_sel:DWORD dst_unused:UNUSED_PAD src0_sel:DWORD src1_sel:WORD_1\n"
  "v_cvt_pk_f16_f32 v191, v174, v175\n"
  "v_lshlrev_b32_sdwa v252, 1, v180 dst_sel:DWORD dst_unused:UNUSED_PAD src0_sel:DWORD src1_sel:WORD_1\n"
  "v_mfma_f32_4x4x4_16b_f16 v[112:115], v[184:185], v[184:185], v[112:115]\n"
  "ds_read_b128 v[216:219], v0 offset:38912\n"
  "ds_read_u16 v246, v246\n"
  "ds_read_u16 v247, v247\n"
  "v_lshlrev_b32_sdwa v253, 1, v181 dst_sel:DWORD dst_unused:UNUSED_PAD src0_sel:DWORD src1_sel:WORD_1\n"
  "v_mfma_f32_32x32x16_bf16 v[152:167], v[28:31], v[220:223], v[152:167]\n"
  "s_waitcnt lgkmcnt(6)\n"
  "v_lshlrev_b32_sdwa v254, 1, v182 dst_sel:DWORD dst_unused:UNUSED_PAD src0_sel:DWORD src1_sel:WORD_1\n"
  "v_lshlrev_b32_sdwa v255, 1, v183 dst_sel:DWORD dst_unused:UNUSED_PAD src0_sel:DWORD src1_sel:WORD_1\n"
  "v_pk_fma_f16 v208, v188, v188, s56\n"
  "v_pk_fma_f16 v209, v189, v189, s56\n"
  "v_pk_fma_f16 v210, v190, v190, s56\n"
  "v_pk_fma_f16 v211, v191, v191, s56\n"
  "v_pk_mul_f16 v208, v188, v208\n"
  "v_pk_mul_f16 v209, v189, v209\n"
  "v_pk_mul_f16 v210, v190, v210\n"
  "v_mfma_f32_4x4x4_16b_f16 v[112:115], v[186:187], v[186:187], v[112:115]\n"
  "ds_read_b128 v[220:223], v0 offset:39936\n"
  "ds_read_u16 v248, v248\n"
  "v_pk_mul_f16 v211, v191, v211\n"
  "v_mfma_f32_32x32x16_bf16 v[168:183], v[8:11], v[224:227], 0\n"
  "s_waitcnt lgkmcnt(6)\n"
  "v_exp_f16 v212, v208\n"
  "v_exp_f16 v213, v209\n"
  "v_exp_f16 v214, v210\n"
  "v_exp_f16 v215, v211\n"
  "v_mfma_f32_4x4x4_16b_f16 v[112:115], v[240:241], v[240:241], v[112:115]\n"
  "ds_read_b128 v[224:227], v0 offset:40960\n"
  "ds_read_u16 v249, v249\n"
  "v_exp_f16_sdwa v212, v208 dst_sel:WORD_1 dst_unused:UNUSED_PRESERVE src0_sel:WORD_1\n"
  "v_mfma_f32_32x32x16_bf16 v[168:183], v[12:15], v[228:231], v[168:183]\n"
  "s_waitcnt lgkmcnt(6)\n"
  "v_exp_f16_sdwa v213, v209 dst_sel:WORD_1 dst_unused:UNUSED_PRESERVE src0_sel:WORD_1\n"
  "v_exp_f16_sdwa v214, v210 dst_sel:WORD_1 dst_unused:UNUSED_PRESERVE src0_sel:WORD_1\n"
  "v_exp_f16_sdwa v215, v211 dst_sel:WORD_1 dst_unused:UNUSED_PRESERVE src0_sel:WORD_1\n"
  "v_pk_add_f16 v212, v212, 1.0 op_sel_hi:[1,0]\n"
  "v_pk_add_f16 v213, v213, 1.0 op_sel_hi:[1,0]\n"
  "v_pk_add_f16 v214, v214, 1.0 op_sel_hi:[1,0]\n"
  "v_mfma_f32_4x4x4_16b_f16 v[112:115], v[242:243], v[242:243], v[112:115]\n"
  "ds_read_b128 v[228:231], v0 offset:41984\n"
  "ds_read_u16 v250, v250\n"
  "ds_read_u16 v251, v251\n"
  "s_setprio 0\n"
  "v_pk_add_f16 v215, v215, 1.0 op_sel_hi:[1,0]\n"
  "v_mfma_f32_32x32x16_bf16 v[168:183], v[16:19], v[216:219], v[168:183]\n"
  "s_waitcnt lgkmcnt(6)\n"
  "v_rcp_f16 v208, v212\n"
  "v_rcp_f16 v209, v213\n"
  "v_rcp_f16 v210, v214\n"
  "v_rcp_f16 v211, v215\n"
  "v_mfma_f32_4x4x4_16b_f16 v[112:115], v[244:245], v[244:245], v[112:115]\n"
  "ds_read_b128 v[216:219], v0 offset:43008\n"
  "ds_read_u16 v252, v252\n"
  "v_rcp_f16_sdwa v208, v212 dst_sel:WORD_1 dst_unused:UNUSED_PRESERVE src0_sel:WORD_1\n"
  "v_mfma_f32_32x32x16_bf16 v[168:183], v[20:23], v[220:223], v[168:183]\n"
  "s_waitcnt lgkmcnt(6)\n"
  "v_rcp_f16_sdwa v209, v213 dst_sel:WORD_1 dst_unused:UNUSED_PRESERVE src0_sel:WORD_1\n"
  "v_rcp_f16_sdwa v210, v214 dst_sel:WORD_1 dst_unused:UNUSED_PRESERVE src0_sel:WORD_1\n"
  "v_rcp_f16_sdwa v211, v215 dst_sel:WORD_1 dst_unused:UNUSED_PRESERVE src0_sel:WORD_1\n"
  "v_pk_mul_f16 v188, v188, v208\n"
  "v_pk_mul_f16 v189, v189, v209\n"
  "v_pk_mul_f16 v190, v190, v210\n"
  "v_mfma_f32_4x4x4_16b_f16 v[112:115], v[246:247], v[246:247], v[112:115]\n"
  "ds_read_b128 v[220:223], v0 offset:44032\n"
  "ds_read_u16 v253, v253\n"
  "v_pk_mul_f16 v191, v191, v211\n"
  "v_mfma_f32_32x32x16_bf16 v[168:183], v[24:27], v[224:227], v[168:183]\n"
  "s_waitcnt lgkmcnt(6)\n"
  "v_lshlrev_b32_sdwa v240, 1, v160 dst_sel:DWORD dst_unused:UNUSED_PAD src0_sel:DWORD src1_sel:WORD_1\n"
  "v_cvt_pk_f16_f32 v184, v152, v153\n"
  "v_lshlrev_b32_sdwa v241, 1, v161 dst_sel:DWORD dst_unused:UNUSED_PAD src0_sel:DWORD src1_sel:WORD_1\n"
  "v_cvt_pk_f16_f32 v185, v154, v155\n"
  "v_lshlrev_b32_sdwa v242, 1, v162 dst_sel:DWORD dst_unused:UNUSED_PAD src0_sel:DWORD src1_sel:WORD_1\n"
  "v_cvt_pk_f16_f32 v186, v156, v157\n"
  "v_lshlrev_b32_sdwa v243, 1, v163 dst_sel:DWORD dst_unused:UNUSED_PAD src0_sel:DWORD src1_sel:WORD_1\n"
  "v_cvt_pk_f16_f32 v187, v158, v159\n"
  "v_lshlrev_b32_sdwa v244, 1, v164 dst_sel:DWORD dst_unused:UNUSED_PAD src0_sel:DWORD src1_sel:WORD_1\n"
  "v_mfma_f32_4x4x4_16b_f16 v[116:119], v[188:189], v[188:189], v[116:119]\n"
  "ds_read_b128 v[224:227], v0 offset:45056\n"
  "ds_read_u16 v254, v254\n"
  "ds_read_u16 v255, v255\n"
  "v_lshlrev_b32_sdwa v245, 1, v165 dst_sel:DWORD dst_unused:UNUSED_PAD src0_sel:DWORD src1_sel:WORD_1\n"
  "v_mfma_f32_32x32x16_bf16 v[168:183], v[28:31], v[228:231], v[168:183]\n"
  "s_waitcnt lgkmcnt(6)\n"
  "v_lshlrev_b32_sdwa v246, 1, v166 dst_sel:DWORD dst_unused:UNUSED_PAD src0_sel:DWORD src1_sel:WORD_1\n"
  "v_lshlrev_b32_sdwa v247, 1, v167 dst_sel:DWORD dst_unused:UNUSED_PAD src0_sel:DWORD src1_sel:WORD_1\n"
  "v_pk_fma_f16 v208, v184, v184, s56\n"
  "v_pk_fma_f16 v209, v185, v185, s56\n"
  "v_pk_fma_f16 v210, v186, v186, s56\n"
  "v_pk_fma_f16 v211, v187, v187, s56\n"
  "v_pk_mul_f16 v208, v184, v208\n"
  "v_pk_mul_f16 v209, v185, v209\n"
  "v_pk_mul_f16 v210, v186, v210\n"
  "v_mfma_f32_4x4x4_16b_f16 v[116:119], v[190:191], v[190:191], v[116:119]\n"
  "ds_read_b128 v[228:231], v0 offset:46080\n"
  "ds_read_u16 v240, v240\n"
  "v_pk_mul_f16 v211, v187, v211\n"
  "v_mfma_f32_32x32x16_bf16 v[152:167], v[8:11], v[216:219], 0\n"
  "s_waitcnt lgkmcnt(6)\n"
  "v_exp_f16 v212, v208\n"
  "v_exp_f16 v213, v209\n"
  "v_exp_f16 v214, v210\n"
  "v_exp_f16 v215, v211\n"
  "v_mfma_f32_4x4x4_16b_f16 v[116:119], v[248:249], v[248:249], v[116:119]\n"
  "ds_read_b128 v[216:219], v0 offset:47104\n"
  "ds_read_u16 v241, v241\n"
  "v_exp_f16_sdwa v212, v208 dst_sel:WORD_1 dst_unused:UNUSED_PRESERVE src0_sel:WORD_1\n"
  "v_mfma_f32_32x32x16_bf16 v[152:167], v[12:15], v[220:223], v[152:167]\n"
  "s_waitcnt lgkmcnt(6)\n"
  "v_exp_f16_sdwa v213, v209 dst_sel:WORD_1 dst_unused:UNUSED_PRESERVE src0_sel:WORD_1\n"
  "v_exp_f16_sdwa v214, v210 dst_sel:WORD_1 dst_unused:UNUSED_PRESERVE src0_sel:WORD_1\n"
  "v_exp_f16_sdwa v215, v211 dst_sel:WORD_1 dst_unused:UNUSED_PRESERVE src0_sel:WORD_1\n"
  "v_pk_add_f16 v212, v212, 1.0 op_sel_hi:[1,0]\n"
  "v_pk_add_f16 v213, v213, 1.0 op_sel_hi:[1,0]\n"
  "v_pk_add_f16 v214, v214, 1.0 op_sel_hi:[1,0]\n"
  "v_mfma_f32_4x4x4_16b_f16 v[116:119], v[250:251], v[250:251], v[116:119]\n"
  "ds_read_b128 v[220:223], v0 offset:48128\n"
  "ds_read_u16 v242, v242\n"
  "ds_read_u16 v243, v243\n"
  "v_pk_add_f16 v215, v215, 1.0 op_sel_hi:[1,0]\n"
  "v_mfma_f32_32x32x16_bf16 v[152:167], v[16:19], v[224:227], v[152:167]\n"
  "s_waitcnt lgkmcnt(6)\n"
  "v_rcp_f16 v208, v212\n"
  "v_rcp_f16 v209, v213\n"
  "v_rcp_f16 v210, v214\n"
  "v_rcp_f16 v211, v215\n"
  "v_mfma_f32_4x4x4_16b_f16 v[116:119], v[252:253], v[252:253], v[116:119]\n"
  "ds_read_b128 v[224:227], v0 offset:49152\n"
  "ds_read_u16 v244, v244\n"
  "v_rcp_f16_sdwa v208, v212 dst_sel:WORD_1 dst_unused:UNUSED_PRESERVE src0_sel:WORD_1\n"
  "v_mfma_f32_32x32x16_bf16 v[152:167], v[20:23], v[228:231], v[152:167]\n"
  "s_waitcnt lgkmcnt(6)\n"
  "v_rcp_f16_sdwa v209, v213 dst_sel:WORD_1 dst_unused:UNUSED_PRESERVE src0_sel:WORD_1\n"
  "v_rcp_f16_sdwa v210, v214 dst_sel:WORD_1 dst_unused:UNUSED_PRESERVE src0_sel:WORD_1\n"
  "v_rcp_f16_sdwa v211, v215 dst_sel:WORD_1 dst_unused:UNUSED_PRESERVE src0_sel:WORD_1\n"
  "v_pk_mul_f16 v184, v184, v208\n"
  "v_pk_mul_f16 v185, v185, v209\n"
  "v_pk_mul_f16 v186, v186, v210\n"
  "v_mfma_f32_4x4x4_16b_f16 v[116:119], v[254:255], v[254:255], v[116:119]\n"
  "ds_read_b128 v[228:231], v0 offset:50176\n"
  "ds_read_u16 v245, v245\n"
  "v_pk_mul_f16 v187, v187, v211\n"
  "v_mfma_f32_32x32x16_bf16 v[152:167], v[24:27], v[216:219], v[152:167]\n"
  "s_waitcnt lgkmcnt(6)\n"
  "v_lshlrev_b32_sdwa v248, 1, v176 dst_sel:DWORD dst_unused:UNUSED_PAD src0_sel:DWORD src1_sel:WORD_1\n"
  "v_cvt_pk_f16_f32 v188, v168, v169\n"
  "v_lshlrev_b32_sdwa v249, 1, v177 dst_sel:DWORD dst_unused:UNUSED_PAD src0_sel:DWORD src1_sel:WORD_1\n"
  "v_cvt_pk_f16_f32 v189, v170, v171\n"
  "v_lshlrev_b32_sdwa v250, 1, v178 dst_sel:DWORD dst_unused:UNUSED_PAD src0_sel:DWORD src1_sel:WORD_1\n"
  "v_cvt_pk_f16_f32 v190, v172, v173\n"
  "v_lshlrev_b32_sdwa v251, 1, v179 dst_sel:DWORD dst_unused:UNUSED_PAD src0_sel:DWORD src1_sel:WORD_1\n"
  "v_cvt_pk_f16_f32 v191, v174, v175\n"
  "v_lshlrev_b32_sdwa v252, 1, v180 dst_sel:DWORD dst_unused:UNUSED_PAD src0_sel:DWORD src1_sel:WORD_1\n"
  "v_mfma_f32_4x4x4_16b_f16 v[120:123], v[184:185], v[184:185], v[120:123]\n"
  "ds_read_b128 v[216:219], v0 offset:51200\n"
  "ds_read_u16 v246, v246\n"
  "ds_read_u16 v247, v247\n"
  "v_lshlrev_b32_sdwa v253, 1, v181 dst_sel:DWORD dst_unused:UNUSED_PAD src0_sel:DWORD src1_sel:WORD_1\n"
  "v_mfma_f32_32x32x16_bf16 v[152:167], v[28:31], v[220:223], v[152:167]\n"
  "s_waitcnt lgkmcnt(6)\n"
  "v_lshlrev_b32_sdwa v254, 1, v182 dst_sel:DWORD dst_unused:UNUSED_PAD src0_sel:DWORD src1_sel:WORD_1\n"
  "v_lshlrev_b32_sdwa v255, 1, v183 dst_sel:DWORD dst_unused:UNUSED_PAD src0_sel:DWORD src1_sel:WORD_1\n"
  "v_pk_fma_f16 v208, v188, v188, s56\n"
  "v_pk_fma_f16 v209, v189, v189, s56\n"
  "v_pk_fma_f16 v210, v190, v190, s56\n"
  "v_pk_fma_f16 v211, v191, v191, s56\n"
  "v_pk_mul_f16 v208, v188, v208\n"
  "v_pk_mul_f16 v209, v189, v209\n"
  "v_pk_mul_f16 v210, v190, v210\n"
  "v_mfma_f32_4x4x4_16b_f16 v[120:123], v[186:187], v[186:187], v[120:123]\n"
  "ds_read_b128 v[220:223], v0 offset:52224\n"
  "ds_read_u16 v248, v248\n"
  "v_pk_mul_f16 v211, v191, v211\n"
  "v_mfma_f32_32x32x16_bf16 v[168:183], v[8:11], v[224:227], 0\n"
  "s_waitcnt lgkmcnt(6)\n"
  "v_exp_f16 v212, v208\n"
  "v_exp_f16 v213, v209\n"
  "v_exp_f16 v214, v210\n"
  "v_exp_f16 v215, v211\n"
  "v_mfma_f32_4x4x4_16b_f16 v[120:123], v[240:241], v[240:241], v[120:123]\n"
  "ds_read_b128 v[224:227], v0 offset:53248\n"
  "ds_read_u16 v249, v249\n"
  "v_exp_f16_sdwa v212, v208 dst_sel:WORD_1 dst_unused:UNUSED_PRESERVE src0_sel:WORD_1\n"
  "v_mfma_f32_32x32x16_bf16 v[168:183], v[12:15], v[228:231], v[168:183]\n"
  "s_waitcnt lgkmcnt(6)\n"
  "v_exp_f16_sdwa v213, v209 dst_sel:WORD_1 dst_unused:UNUSED_PRESERVE src0_sel:WORD_1\n"
  "v_exp_f16_sdwa v214, v210 dst_sel:WORD_1 dst_unused:UNUSED_PRESERVE src0_sel:WORD_1\n"
  "v_exp_f16_sdwa v215, v211 dst_sel:WORD_1 dst_unused:UNUSED_PRESERVE src0_sel:WORD_1\n"
  "v_pk_add_f16 v212, v212, 1.0 op_sel_hi:[1,0]\n"
  "v_pk_add_f16 v213, v213, 1.0 op_sel_hi:[1,0]\n"
  "v_pk_add_f16 v214, v214, 1.0 op_sel_hi:[1,0]\n"
  "v_mfma_f32_4x4x4_16b_f16 v[120:123], v[242:243], v[242:243], v[120:123]\n"
  "ds_read_b128 v[228:231], v0 offset:54272\n"
  "ds_read_u16 v250, v250\n"
  "ds_read_u16 v251, v251\n"
  "v_pk_add_f16 v215, v215, 1.0 op_sel_hi:[1,0]\n"
  "v_mfma_f32_32x32x16_bf16 v[168:183], v[16:19], v[216:219], v[168:183]\n"
  "s_waitcnt lgkmcnt(6)\n"
  "v_rcp_f16 v208, v212\n"
  "v_rcp_f16 v209, v213\n"
  "v_rcp_f16 v210, v214\n"
  "v_rcp_f16 v211, v215\n"
  "v_mfma_f32_4x4x4_16b_f16 v[120:123], v[244:245], v[244:245], v[120:123]\n"
  "s_cmp_lt_i32 s70, 5\n"
  "s_cbranch_scc1 .Lnwr1%=\n"
  "s_mov_b32 s94, 4\n"
  "s_mov_b32 s96, 1048576\n"
  ".Lw14%=:\n"
  "v_mov_b32 v199, 160156\n"
  "ds_read_b32 v199, v199\n"
  "s_waitcnt lgkmcnt(0)\n"
  "v_readfirstlane_b32 s92, v199\n"
  "s_nop 1\n"
  "s_cmp_ge_u32 s92, s94\n"
  "s_cbranch_scc1 .Lwd14%=\n"
  "s_sleep 1\n"
  "s_sub_u32 s96, s96, 1\n"
  "s_cmp_lg_u32 s96, 0\n"
  "s_cbranch_scc1 .Lw14%=\n"
  ".Lwd14%=:\n"
  ".Lnwr1%=:\n"
  "ds_read_b128 v[216:219], v0 offset:55296\n"
  "ds_read_u16 v252, v252\n"
  "v_rcp_f16_sdwa v208, v212 dst_sel:WORD_1 dst_unused:UNUSED_PRESERVE src0_sel:WORD_1\n"
  "v_mfma_f32_32x32x16_bf16 v[168:183], v[20:23], v[220:223], v[168:183]\n"
  "s_waitcnt lgkmcnt(6)\n"
  "s_cmp_lt_i32 s70, 0\n"
  "s_cbranch_scc1 .Lq15%=\n"
  "s_sub_u32 s70, s70, 1\n"
  "s_cmp_lg_u32 s66, 0\n"
  "s_cbranch_scc1 .Lq15%=\n"
  "s_cmp_gt_u32 s70, 2\n"
  "s_cbranch_scc1 .Lq15%=\n"
  "s_mov_b64 s[76:77], s[14:15]\n"
  "s_mov_b32 s78, 73728\n"
  "s_mov_b32 s79, 0x20000\n"
  "s_cmp_eq_u32 s70, 2\n"
  "s_cbranch_scc1 .Lqt15%=\n"
  "s_getpc_b64 s[76:77]\n"
  ".Lqp15%=:\n"
  "s_mov_b32 s78, .Lexit%=-.Lqp15%=\n"
  ".Lqt15%=:\n"
  "s_lshr_b32 s62, s75, 3\n"
  "s_and_b32 s62, s62, 15\n"
  "s_lshl_b32 s62, s62, 13\n"
  "v_lshlrev_b32 v6, 3, v0\n"
  "buffer_load_dword v6, v6, s[76:79], s62 offen\n"
  ".Lq15%=:\n"
  "s_setprio 2\n"
  "s_bitcmp1_b32 s72, 2\n"
  "s_cbranch_scc0 .Lpr16%=\n"
  "s_setprio 3\n"
  ".Lpr16%=:\n"
  "v_rcp_f16_sdwa v209, v213 dst_sel:WORD_1 dst_unused:UNUSED_PRESERVE src0_sel:WORD_1\n"
  "v_rcp_f16_sdwa v210, v214 dst_sel:WORD_1 dst_unused:UNUSED_PRESERVE src0_sel:WORD_1\n"
  "v_rcp_f16_sdwa v211, v215 dst_sel:WORD_1 dst_unused:UNUSED_PRESERVE src0_sel:WORD_1\n"
  "v_pk_mul_f16 v188, v188, v208\n"
  "v_pk_mul_f16 v189, v189, v209\n"
  "v_pk_mul_f16 v190, v190, v210\n"
  "v_mfma_f32_4x4x4_16b_f16 v[120:123], v[246:247], v[246:247], v[120:123]\n"
  "ds_read_b128 v[220:223], v0 offset:56320\n"
  "ds_read_u16 v253, v253\n"
  "v_pk_mul_f16 v191, v191, v211\n"
  "v_mfma_f32_32x32x16_bf16 v[168:183], v[24:27], v[224:227], v[168:183]\n"
  "s_waitcnt lgkmcnt(6)\n"
  "v_lshlrev_b32_sdwa v240, 1, v160 dst_sel:DWORD dst_unused:UNUSED_PAD src0_sel:DWORD src1_sel:WORD_1\n"
  "v_cvt_pk_f16_f32 v184, v152, v153\n"
  "v_lshlrev_b32_sdwa v241, 1, v161 dst_sel:DWORD dst_unused:UNUSED_PAD src0_sel:DWORD src1_sel:WORD_1\n"
  "v_cvt_pk_f16_f32 v185, v154, v155\n"
  "v_lshlrev_b32_sdwa v242, 1, v162 dst_sel:DWORD dst_unused:UNUSED_PAD src0_sel:DWORD src1_sel:WORD_1\n"
  "v_cvt_pk_f16_f32 v186, v156, v157\n"
  "v_lshlrev_b32_sdwa v243, 1, v163 dst_sel:DWORD dst_unused:UNUSED_PAD src0_sel:DWORD src1_sel:WORD_1\n"
  "v_cvt_pk_f16_f32 v187, v158, v159\n"
  "v_lshlrev_b32_sdwa v244, 1, v164 dst_sel:DWORD dst_unused:UNUSED_PAD src0_sel:DWORD src1_sel:WORD_1\n"
  "v_mfma_f32_4x4x4_16b_f16 v[124:127], v[188:189], v[188:189], v[124:127]\n"
  "ds_read_b128 v[224:227], v0 offset:57344\n"
  "ds_read_u16 v254, v254\n"
  "ds_read_u16 v255, v255\n"
  "v_lshlrev_b32_sdwa v245, 1, v165 dst_sel:DWORD dst_unused:UNUSED_PAD src0_sel:DWORD src1_sel:WORD_1\n"
  "v_mfma_f32_32x32x16_bf16 v[168:183], v[28:31], v[228:231], v[168:183]\n"
  "s_waitcnt lgkmcnt(6)\n"
  "v_lshlrev_b32_sdwa v246, 1, v166 dst_sel:DWORD dst_unused:UNUSED_PAD src0_sel:DWORD src1_sel:WORD_1\n"
  "v_lshlrev_b32_sdwa v247, 1, v167 dst_sel:DWORD dst_unused:UNUSED_PAD src0_sel:DWORD src1_sel:WORD_1\n"
  "v_pk_fma_f16 v208, v184, v184, s56\n"
  "v_pk_fma_f16 v209, v185, v185, s56\n"
  "v_pk_fma_f16 v210, v186, v186, s56\n"
  "v_pk_fma_f16 v211, v187, v187, s56\n"
  "v_pk_mul_f16 v208, v184, v208\n"
  "v_pk_mul_f16 v209, v185, v209\n"
  "v_pk_mul_f16 v210, v186, v210\n"
  "v_mfma_f32_4x4x4_16b_f16 v[124:127], v[190:191], v[190:191], v[124:127]\n"
  "ds_read_b128 v[228:231], v0 offset:58368\n"
  "ds_read_u16 v240, v240\n"
  "v_pk_mul_f16 v211, v187, v211\n"
  "v_mfma_f32_32x32x16_bf16 v[152:167], v[8:11], v[216:219], 0\n"
  "s_waitcnt lgkmcnt(6)\n"
  "v_exp_f16 v212, v208\n"
  "v_exp_f16 v213, v209\n"
  "v_exp_f16 v214, v210\n"
  "v_exp_f16 v215, v211\n"
  "v_mfma_f32_4x4x4_16b_f16 v[124:127], v[248:249], v[248:249], v[124:127]\n"
  "ds_read_b128 v[216:219], v0 offset:59392\n"
  "ds_read_u16 v241, v241\n"
  "v_exp_f16_sdwa v212, v208 dst_sel:WORD_1 dst_unused:UNUSED_PRESERVE src0_sel:WORD_1\n"
  "v_mfma_f32_32x32x16_bf16 v[152:167], v[12:15], v[220:223], v[152:167]\n"
  "s_waitcnt lgkmcnt(6)\n"
  "v_exp_f16_sdwa v213, v209 dst_sel:WORD_1 dst_unused:UNUSED_PRESERVE src0_sel:WORD_1\n"
  "v_exp_f16_sdwa v214, v210 dst_sel:WORD_1 dst_unused:UNUSED_PRESERVE src0_sel:WORD_1\n"
  "v_exp_f16_sdwa v215, v211 dst_sel:WORD_1 dst_unused:UNUSED_PRESERVE src0_sel:WORD_1\n"
  "v_pk_add_f16 v212, v212, 1.0 op_sel_hi:[1,0]\n"
  "v_pk_add_f16 v213, v213, 1.0 op_sel_hi:[1,0]\n"
  "v_pk_add_f16 v214, v214, 1.0 op_sel_hi:[1,0]\n"
  "v_mfma_f32_4x4x4_16b_f16 v[124:127], v[250:251], v[250:251], v[124:127]\n"
  "ds_read_b128 v[220:223], v0 offset:60416\n"
  "ds_read_u16 v242, v242\n"
  "ds_read_u16 v243, v243\n"
  "v_pk_add_f16 v215, v215, 1.0 op_sel_hi:[1,0]\n"
  "v_mfma_f32_32x32x16_bf16 v[152:167], v[16:19], v[224:227], v[152:167]\n"
  "s_waitcnt lgkmcnt(6)\n"
  "v_rcp_f16 v208, v212\n"
  "v_rcp_f16 v209, v213\n"
  "v_rcp_f16 v210, v214\n"
  "v_rcp_f16 v211, v215\n"
  "v_mfma_f32_4x4x4_16b_f16 v[124:127], v[252:253], v[252:253], v[124:127]\n"
  "ds_read_b128 v[224:227], v0 offset:61440\n"
  "ds_read_u16 v244, v244\n"
  "v_rcp_f16_sdwa v208, v212 dst_sel:WORD_1 dst_unused:UNUSED_PRESERVE src0_sel:WORD_1\n"
  "v_mfma_f32_32x32x16_bf16 v[152:167], v[20:23], v[228:231], v[152:167]\n"
  "s_waitcnt lgkmcnt(6)\n"
  "v_rcp_f16_sdwa v209, v213 dst_sel:WORD_1 dst_unused:UNUSED_PRESERVE src0_sel:WORD_1\n"
  "v_rcp_f16_sdwa v210, v214 dst_sel:WORD_1 dst_unused:UNUSED_PRESERVE src0_sel:WORD_1\n"
  "v_rcp_f16_sdwa v211, v215 dst_sel:WORD_1 dst_unused:UNUSED_PRESERVE src0_sel:WORD_1\n"
  "v_pk_mul_f16 v184, v184, v208\n"
  "v_pk_mul_f16 v185, v185, v209\n"
  "v_pk_mul_f16 v186, v186, v210\n"
  "v_mfma_f32_4x4x4_16b_f16 v[124:127], v[254:255], v[254:255], v[124:127]\n"
  "ds_read_b128 v[228:231], v0 offset:62464\n"
  "ds_read_u16 v245, v245\n"
  "v_pk_mul_f16 v187, v187, v211\n"
  "v_mfma_f32_32x32x16_bf16 v[152:167], v[24:27], v[216:219], v[152:167]\n"
  "s_waitcnt lgkmcnt(6)\n"
  "v_lshlrev_b32_sdwa v248, 1, v176 dst_sel:DWORD dst_unused:UNUSED_PAD src0_sel:DWORD src1_sel:WORD_1\n"
  "v_cvt_pk_f16_f32 v188, v168, v169\n"
  "v_lshlrev_b32_sdwa v249, 1, v177 dst_sel:DWORD dst_unused:UNUSED_PAD src0_sel:DWORD src1_sel:WORD_1\n"
  "v_cvt_pk_f16_f32 v189, v170, v171\n"
  "v_lshlrev_b32_sdwa v250, 1, v178 dst_sel:DWORD dst_unused:UNUSED_PAD src0_sel:DWORD src1_sel:WORD_1\n"
  "v_cvt_pk_f16_f32 v190, v172, v173\n"
  "v_lshlrev_b32_sdwa v251, 1, v179 dst_sel:DWORD dst_unused:UNUSED_PAD src0_sel:DWORD src1_sel:WORD_1\n"
  "v_cvt_pk_f16_f32 v191, v174, v175\n"
  "v_lshlrev_b32_sdwa v252, 1, v180 dst_sel:DWORD dst_unused:UNUSED_PAD src0_sel:DWORD src1_sel:WORD_1\n"
  "v_mfma_f32_4x4x4_16b_f16 v[128:131], v[184:185], v[184:185], v[128:131]\n"
  "ds_read_b128 v[216:219], v0 offset:63488\n"
  "ds_read_u16 v246, v246\n"
  "ds_read_u16 v247, v247\n"
  "v_lshlrev_b32_sdwa v253, 1, v181 dst_sel:DWORD dst_unused:UNUSED_PAD src0_sel:DWORD src1_sel:WORD_1\n"
  "v_mfma_f32_32x32x16_bf16 v[152:167], v[28:31], v[220:223], v[152:167]\n"
  "s_waitcnt lgkmcnt(6)\n"
  "v_lshlrev_b32_sdwa v254, 1, v182 dst_sel:DWORD dst_unused:UNUSED_PAD src0_sel:DWORD src1_sel:WORD_1\n"
  "v_lshlrev_b32_sdwa v255, 1, v183 dst_sel:DWORD dst_unused:UNUSED_PAD src0_sel:DWORD src1_sel:WORD_1\n"
  "v_pk_fma_f16 v208, v188, v188, s56\n"
  "v_pk_fma_f16 v209, v189, v189, s56\n"
  "v_pk_fma_f16 v210, v190, v190, s56\n"
  "v_pk_fma_f16 v211, v191, v191, s56\n"
  "v_pk_mul_f16 v208, v188, v208\n"
  "v_pk_mul_f16 v209, v189, v209\n"
  "v_pk_mul_f16 v210, v190, v210\n"
  "v_mfma_f32_4x4x4_16b_f16 v[128:131], v[186:187], v[186:187], v[128:131]\n"
  "ds_read_b128 v[220:223], v0 offset:64512\n"
  "ds_read_u16 v248, v248\n"
  "v_pk_mul_f16 v211, v191, v211\n"
  "v_mfma_f32_32x32x16_bf16 v[168:183], v[8:11], v[224:227], 0\n"
  "s_waitcnt lgkmcnt(6)\n"
  "v_exp_f16 v212, v208\n"
  "v_exp_f16 v213, v209\n"
  "v_exp_f16 v214, v210\n"
  "v_exp_f16 v215, v211\n"
  "v_mfma_f32_4x4x4_16b_f16 v[128:131], v[240:241], v[240:241], v[128:131]\n"
  "ds_read_b128 v[224:227], v1 offset:6144\n"
  "ds_read_u16 v249, v249\n"
  "v_exp_f16_sdwa v212, v208 dst_sel:WORD_1 dst_unused:UNUSED_PRESERVE src0_sel:WORD_1\n"
  "v_mfma_f32_32x32x16_bf16 v[168:183], v[12:15], v[228:231], v[168:183]\n"
  "s_waitcnt lgkmcnt(6)\n"
  "v_exp_f16_sdwa v213, v209 dst_sel:WORD_1 dst_unused:UNUSED_PRESERVE src0_sel:WORD_1\n"
  "s_setprio 1\n"
  "s_bitcmp1_b32 s72, 2\n"
  "s_cbranch_scc0 .Lpr17%=\n"
  "s_setprio 2\n"
  ".Lpr17%=:\n"
  "v_exp_f16_sdwa v214, v210 dst_sel:WORD_1 dst_unused:UNUSED_PRESERVE src0_sel:WORD_1\n"
  "v_exp_f16_sdwa v215, v211 dst_sel:WORD_1 dst_unused:UNUSED_PRESERVE src0_sel:WORD_1\n"
  "v_pk_add_f16 v212, v212, 1.0 op_sel_hi:[1,0]\n"
  "v_pk_add_f16 v213, v213, 1.0 op_sel_hi:[1,0]\n"
  "v_pk_add_f16 v214, v214, 1.0 op_sel_hi:[1,0]\n"
  "v_mfma_f32_4x4x4_16b_f16 v[128:131], v[242:243], v[242:243], v[128:131]\n"
  "ds_read_b128 v[228:231], v1 offset:7168\n"
  "ds_read_u16 v250, v250\n"
  "ds_read_u16 v251, v251\n"
  "v_pk_add_f16 v215, v215, 1.0 op_sel_hi:[1,0]\n"
  "v_mfma_f32_32x32x16_bf16 v[168:183], v[16:19], v[216:219], v[168:183]\n"
  "s_waitcnt lgkmcnt(6)\n"
  "v_rcp_f16 v208, v212\n"
  "v_rcp_f16 v209, v213\n"
  "v_rcp_f16 v210, v214\n"
  "v_rcp_f16 v211, v215\n"
  "v_mfma_f32_4x4x4_16b_f16 v[128:131], v[244:245], v[244:245], v[128:131]\n"
  "ds_read_b128 v[216:219], v1 offset:8192\n"
  "ds_read_u16 v252, v252\n"
  "v_rcp_f16_sdwa v208, v212 dst_sel:WORD_1 dst_unused:UNUSED_PRESERVE src0_sel:WORD_1\n"
  "v_mfma_f32_32x32x16_bf16 v[168:183], v[20:23], v[220:223], v[168:183]\n"
  "s_waitcnt lgkmcnt(6)\n"
  "v_rcp_f16_sdwa v209, v213 dst_sel:WORD_1 dst_unused:UNUSED_PRESERVE src0_sel:WORD_1\n"
  "v_rcp_f16_sdwa v210, v214 dst_sel:WORD_1 dst_unused:UNUSED_PRESERVE src0_sel:WORD_1\n"
  "v_rcp_f16_sdwa v211, v215 dst_sel:WORD_1 dst_unused:UNUSED_PRESERVE src0_sel:WORD_1\n"
  "v_pk_mul_f16 v188, v188, v208\n"
  "v_pk_mul_f16 v189, v189, v209\n"
  "v_pk_mul_f16 v190, v190, v210\n"
  "v_mfma_f32_4x4x4_16b_f16 v[128:131], v[246:247], v[246:247], v[128:131]\n"
  "ds_read_b128 v[220:223], v1 offset:9216\n"
  "ds_read_u16 v253, v253\n"
  "v_pk_mul_f16 v191, v191, v211\n"
  "v_mfma_f32_32x32x16_bf16 v[168:183], v[24:27], v[224:227], v[168:183]\n"
  "s_waitcnt lgkmcnt(6)\n"
  "v_lshlrev_b32_sdwa v240, 1, v160 dst_sel:DWORD dst_unused:UNUSED_PAD src0_sel:DWORD src1_sel:WORD_1\n"
  "v_cvt_pk_f16_f32 v184, v152, v153\n"
  "v_lshlrev_b32_sdwa v241, 1, v161 dst_sel:DWORD dst_unused:UNUSED_PAD src0_sel:DWORD src1_sel:WORD_1\n"
  "v_cvt_pk_f16_f32 v185, v154, v155\n"
  "v_lshlrev_b32_sdwa v242, 1, v162 dst_sel:DWORD dst_unused:UNUSED_PAD src0_sel:DWORD src1_sel:WORD_1\n"
  "v_cvt_pk_f16_f32 v186, v156, v157\n"
  "v_lshlrev_b32_sdwa v243, 1, v163 dst_sel:DWORD dst_unused:UNUSED_PAD src0_sel:DWORD src1_sel:WORD_1\n"
  "v_cvt_pk_f16_f32 v187, v158, v159\n"
  "v_lshlrev_b32_sdwa v244, 1, v164 dst_sel:DWORD dst_unused:UNUSED_PAD src0_sel:DWORD src1_sel:WORD_1\n"
  "v_mfma_f32_4x4x4_16b_f16 v[132:135], v[188:189], v[188:189], v[132:135]\n"
  "ds_read_b128 v[224:227], v1 offset:10240\n"
  "ds_read_u16 v254, v254\n"
  "ds_read_u16 v255, v255\n"
  "v_lshlrev_b32_sdwa v245, 1, v165 dst_sel:DWORD dst_unused:UNUSED_PAD src0_sel:DWORD src1_sel:WORD_1\n"
  "v_mfma_f32_32x32x16_bf16 v[168:183], v[28:31], v[228:231], v[168:183]\n"
  "s_waitcnt lgkmcnt(6)\n"
  "v_lshlrev_b32_sdwa v246, 1, v166 dst_sel:DWORD dst_unused:UNUSED_PAD src0_sel:DWORD src1_sel:WORD_1\n"
  "v_lshlrev_b32_sdwa v247, 1, v167 dst_sel:DWORD dst_unused:UNUSED_PAD src0_sel:DWORD src1_sel:WORD_1\n"
  "v_pk_fma_f16 v208, v184, v184, s56\n"
  "v_pk_fma_f16 v209, v185, v185, s56\n"
  "v_pk_fma_f16 v210, v186, v186, s56\n"
  "v_pk_fma_f16 v211, v187, v187, s56\n"
  "v_pk_mul_f16 v208, v184, v208\n"
  "v_pk_mul_f16 v209, v185, v209\n"
  "v_pk_mul_f16 v210, v186, v210\n"
  "v_mfma_f32_4x4x4_16b_f16 v[132:135], v[190:191], v[190:191], v[132:135]\n"
  "ds_read_b128 v[228:231], v1 offset:11264\n"
  "ds_read_u16 v240, v240\n"
  "v_pk_mul_f16 v211, v187, v211\n"
  "v_mfma_f32_32x32x16_bf16 v[152:167], v[8:11], v[216:219], 0\n"
  "s_waitcnt lgkmcnt(6)\n"
  "v_exp_f16 v212, v208\n"
  "v_exp_f16 v213, v209\n"
  "v_exp_f16 v214, v210\n"
  "v_exp_f16 v215, v211\n"
  "v_mfma_f32_4x4x4_16b_f16 v[132:135], v[248:249], v[248:249], v[132:135]\n"
  "ds_read_b128 v[216:219], v1 offset:12288\n"
  "ds_read_u16 v241, v241\n"
  "v_exp_f16_sdwa v212, v208 dst_sel:WORD_1 dst_unused:UNUSED_PRESERVE src0_sel:WORD_1\n"
  "v_mfma_f32_32x32x16_bf16 v[152:167], v[12:15], v[220:223], v[152:167]\n"
  "s_waitcnt lgkmcnt(6)\n"
  "v_exp_f16_sdwa v213, v209 dst_sel:WORD_1 dst_unused:UNUSED_PRESERVE src0_sel:WORD_1\n"
  "v_exp_f16_sdwa v214, v210 dst_sel:WORD_1 dst_unused:UNUSED_PRESERVE src0_sel:WORD_1\n"
  "v_exp_f16_sdwa v215, v211 dst_sel:WORD_1 dst_unused:UNUSED_PRESERVE src0_sel:WORD_1\n"
  "v_pk_add_f16 v212, v212, 1.0 op_sel_hi:[1,0]\n"
  "v_pk_add_f16 v213, v213, 1.0 op_sel_hi:[1,0]\n"
  "v_pk_add_f16 v214, v214, 1.0 op_sel_hi:[1,0]\n"
  "v_mfma_f32_4x4x4_16b_f16 v[132:135], v[250:251], v[250:251], v[132:135]\n"
  "ds_read_b128 v[220:223], v1 offset:13312\n"
  "ds_read_u16 v242, v242\n"
  "ds_read_u16 v243, v243\n"
  "v_pk_add_f16 v215, v215, 1.0 op_sel_hi:[1,0]\n"
  "v_mfma_f32_32x32x16_bf16 v[152:167], v[16:19], v[224:227], v[152:167]\n"
  "s_waitcnt lgkmcnt(6)\n"
  "v_rcp_f16 v208, v212\n"
  "v_rcp_f16 v209, v213\n"
  "v_rcp_f16 v210, v214\n"
  "v_rcp_f16 v211, v215\n"
  "v_mfma_f32_4x4x4_16b_f16 v[132:135], v[252:253], v[252:253], v[132:135]\n"
  "ds_read_b128 v[224:227], v1 offset:14336\n"
  "ds_read_u16 v244, v244\n"
  "v_rcp_f16_sdwa v208, v212 dst_sel:WORD_1 dst_unused:UNUSED_PRESERVE src0_sel:WORD_1\n"
  "v_mfma_f32_32x32x16_bf16 v[152:167], v[20:23], v[228:231], v[152:167]\n"
  "s_waitcnt lgkmcnt(6)\n"
  "v_rcp_f16_sdwa v209, v213 dst_sel:WORD_1 dst_unused:UNUSED_PRESERVE src0_sel:WORD_1\n"
  "v_rcp_f16_sdwa v210, v214 dst_sel:WORD_1 dst_unused:UNUSED_PRESERVE src0_sel:WORD_1\n"
  "v_rcp_f16_sdwa v211, v215 dst_sel:WORD_1 dst_unused:UNUSED_PRESERVE src0_sel:WORD_1\n"
  "v_pk_mul_f16 v184, v184, v208\n"
  "v_pk_mul_f16 v185, v185, v209\n"
  "v_pk_mul_f16 v186, v186, v210\n"
  "v_mfma_f32_4x4x4_16b_f16 v[132:135], v[254:255], v[254:255], v[132:135]\n"
  "ds_read_b128 v[228:231], v1 offset:15360\n"
  "ds_read_u16 v245, v245\n"
  "v_pk_mul_f16 v187, v187, v211\n"
  "v_mfma_f32_32x32x16_bf16 v[152:167], v[24:27], v[216:219], v[152:167]\n"
  "s_waitcnt lgkmcnt(6)\n"
  "v_lshlrev_b32_sdwa v248, 1, v176 dst_sel:DWORD dst_unused:UNUSED_PAD src0_sel:DWORD src1_sel:WORD_1\n"
  "v_cvt_pk_f16_f32 v188, v168, v169\n"
  "v_lshlrev_b32_sdwa v249, 1, v177 dst_sel:DWORD dst_unused:UNUSED_PAD src0_sel:DWORD src1_sel:WORD_1\n"
  "v_cvt_pk_f16_f32 v189, v170, v171\n"
  "v_lshlrev_b32_sdwa v250, 1, v178 dst_sel:DWORD dst_unused:UNUSED_PAD src0_sel:DWORD src1_sel:WORD_1\n"
  "v_cvt_pk_f16_f32 v190, v172, v173\n"
  "v_lshlrev_b32_sdwa v251, 1, v179 dst_sel:DWORD dst_unused:UNUSED_PAD src0_sel:DWORD src1_sel:WORD_1\n"
  "v_cvt_pk_f16_f32 v191, v174, v175\n"
  "v_lshlrev_b32_sdwa v252, 1, v180 dst_sel:DWORD dst_unused:UNUSED_PAD src0_sel:DWORD src1_sel:WORD_1\n"
  "v_mfma_f32_4x4x4_16b_f16 v[136:139], v[184:185], v[184:185], v[136:139]\n"
  "ds_read_b128 v[216:219], v1 offset:16384\n"
  "ds_read_u16 v246, v246\n"
  "ds_read_u16 v247, v247\n"
  "v_lshlrev_b32_sdwa v253, 1, v181 dst_sel:DWORD dst_unused:UNUSED_PAD src0_sel:DWORD src1_sel:WORD_1\n"
  "v_mfma_f32_32x32x16_bf16 v[152:167], v[28:31], v[220:223], v[152:167]\n"
  "s_waitcnt lgkmcnt(6)\n"
  "v_lshlrev_b32_sdwa v254, 1, v182 dst_sel:DWORD dst_unused:UNUSED_PAD src0_sel:DWORD src1_sel:WORD_1\n"
  "v_lshlrev_b32_sdwa v255, 1, v183 dst_sel:DWORD dst_unused:UNUSED_PAD src0_sel:DWORD src1_sel:WORD_1\n"
  "v_pk_fma_f16 v208, v188, v188, s56\n"
  "v_pk_fma_f16 v209, v189, v189, s56\n"
  "v_pk_fma_f16 v210, v190, v190, s56\n"
  "v_pk_fma_f16 v211, v191, v191, s56\n"
  "v_pk_mul_f16 v208, v188, v208\n"
  "s_setprio 1\n"
  "v_pk_mul_f16 v209, v189, v209\n"
  "v_pk_mul_f16 v210, v190, v210\n"
  "v_mfma_f32_4x4x4_16b_f16 v[136:139], v[186:187], v[186:187], v[136:139]\n"
  "ds_read_b128 v[220:223], v1 offset:17408\n"
  "ds_read_u16 v248, v248\n"
  "v_pk_mul_f16 v211, v191, v211\n"
  "v_mfma_f32_32x32x16_bf16 v[168:183], v[8:11], v[224:227], 0\n"
  "s_waitcnt lgkmcnt(6)\n"
  "v_exp_f16 v212, v208\n"
  "v_exp_f16 v213, v209\n"
  "v_exp_f16 v214, v210\n"
  "v_exp_f16 v215, v211\n"
  "v_mfma_f32_4x4x4_16b_f16 v[136:139], v[240:241], v[240:241], v[136:139]\n"
  "ds_read_b128 v[224:227], v1 offset:18432\n"
  "ds_read_u16 v249, v249\n"
  "v_exp_f16_sdwa v212, v208 dst_sel:WORD_1 dst_unused:UNUSED_PRESERVE src0_sel:WORD_1\n"
  "v_mfma_f32_32x32x16_bf16 v[168:183], v[12:15], v[228:231], v[168:183]\n"
  "s_waitcnt lgkmcnt(6)\n"
  "v_exp_f16_sdwa v213, v209 dst_sel:WORD_1 dst_unused:UNUSED_PRESERVE src0_sel:WORD_1\n"
  "v_exp_f16_sdwa v214, v210 dst_sel:WORD_1 dst_unused:UNUSED_PRESERVE src0_sel:WORD_1\n"
  "v_exp_f16_sdwa v215, v211 dst_sel:WORD_1 dst_unused:UNUSED_PRESERVE src0_sel:WORD_1\n"
  "v_pk_add_f16 v212, v212, 1.0 op_sel_hi:[1,0]\n"
  "v_pk_add_f16 v213, v213, 1.0 op_sel_hi:[1,0]\n"
  "v_pk_add_f16 v214, v214, 1.0 op_sel_hi:[1,0]\n"
  "v_mfma_f32_4x4x4_16b_f16 v[136:139], v[242:243], v[242:243], v[136:139]\n"
  "ds_read_b128 v[228:231], v1 offset:19456\n"
  "ds_read_u16 v250, v250\n"
  "ds_read_u16 v251, v251\n"
  "v_pk_add_f16 v215, v215, 1.0 op_sel_hi:[1,0]\n"
  "v_mfma_f32_32x32x16_bf16 v[168:183], v[16:19], v[216:219], v[168:183]\n"
  "s_waitcnt lgkmcnt(6)\n"
  "v_rcp_f16 v208, v212\n"
  "v_rcp_f16 v209, v213\n"
  "v_rcp_f16 v210, v214\n"
  "v_rcp_f16 v211, v215\n"
  "v_mfma_f32_4x4x4_16b_f16 v[136:139], v[244:245], v[244:245], v[136:139]\n"
  "ds_read_u16 v252, v252\n"
  "v_rcp_f16_sdwa v208, v212 dst_sel:WORD_1 dst_unused:UNUSED_PRESERVE src0_sel:WORD_1\n"
  "v_mfma_f32_32x32x16_bf16 v[168:183], v[20:23], v[220:223], v[168:183]\n"
  "s_waitcnt lgkmcnt(5)\n"
  "v_rcp_f16_sdwa v209, v213 dst_sel:WORD_1 dst_unused:UNUSED_PRESERVE src0_sel:WORD_1\n"
  "v_rcp_f16_sdwa v210, v214 dst_sel:WORD_1 dst_unused:UNUSED_PRESERVE src0_sel:WORD_1\n"
  "v_rcp_f16_sdwa v211, v215 dst_sel:WORD_1 dst_unused:UNUSED_PRESERVE src0_sel:WORD_1\n"
  "v_pk_mul_f16 v188, v188, v208\n"
  "v_pk_mul_f16 v189, v189, v209\n"
  "v_pk_mul_f16 v190, v190, v210\n"
  "v_mfma_f32_4x4x4_16b_f16 v[136:139], v[246:247], v[246:247], v[136:139]\n"
  "ds_read_u16 v253, v253\n"
  "v_pk_mul_f16 v191, v191, v211\n"
  "v_mfma_f32_32x32x16_bf16 v[168:183], v[24:27], v[224:227], v[168:183]\n"
  "s_waitcnt lgkmcnt(4)\n"
  "v_lshlrev_b32_sdwa v240, 1, v160 dst_sel:DWORD dst_unused:UNUSED_PAD src0_sel:DWORD src1_sel:WORD_1\n"
  "v_cvt_pk_f16_f32 v184, v152, v153\n"
  "v_lshlrev_b32_sdwa v241, 1, v161 dst_sel:DWORD dst_unused:UNUSED_PAD src0_sel:DWORD src1_sel:WORD_1\n"
  "v_cvt_pk_f16_f32 v185, v154, v155\n"
  "v_lshlrev_b32_sdwa v242, 1, v162 dst_sel:DWORD dst_unused:UNUSED_PAD src0_sel:DWORD src1_sel:WORD_1\n"
  "v_cvt_pk_f16_f32 v186, v156, v157\n"
  "v_lshlrev_b32_sdwa v243, 1, v163 dst_sel:DWORD dst_unused:UNUSED_PAD src0_sel:DWORD src1_sel:WORD_1\n"
  "v_cvt_pk_f16_f32 v187, v158, v159\n"
  "v_lshlrev_b32_sdwa v244, 1, v164 dst_sel:DWORD dst_unused:UNUSED_PAD src0_sel:DWORD src1_sel:WORD_1\n"
  "v_mfma_f32_4x4x4_16b_f16 v[140:143], v[188:189], v[188:189], v[140:143]\n"
  "ds_read_u16 v254, v254\n"
  "ds_read_u16 v255, v255\n"
  "v_lshlrev_b32_sdwa v245, 1, v165 dst_sel:DWORD dst_unused:UNUSED_PAD src0_sel:DWORD src1_sel:WORD_1\n"
  "v_mfma_f32_32x32x16_bf16 v[168:183], v[28:31], v[228:231], v[168:183]\n"
  "s_waitcnt lgkmcnt(0)\n"
  "v_lshlrev_b32_sdwa v246, 1, v166 dst_sel:DWORD dst_unused:UNUSED_PAD src0_sel:DWORD src1_sel:WORD_1\n"
  "v_lshlrev_b32_sdwa v247, 1, v167 dst_sel:DWORD dst_unused:UNUSED_PAD src0_sel:DWORD src1_sel:WORD_1\n"
  "v_pk_fma_f16 v208, v184, v184, s56\n"
  "v_pk_fma_f16 v209, v185, v185, s56\n"
  "v_pk_fma_f16 v210, v186, v186, s56\n"
  "v_pk_fma_f16 v211, v187, v187, s56\n"
  "v_pk_mul_f16 v208, v184, v208\n"
  "v_pk_mul_f16 v209, v185, v209\n"
  "v_pk_mul_f16 v210, v186, v210\n"
  "v_mfma_f32_4x4x4_16b_f16 v[140:143], v[190:191], v[190:191], v[140:143]\n"
  "ds_read_u16 v240, v240\n"
  "v_pk_mul_f16 v211, v187, v211\n"
  "v_exp_f16 v212, v208\n"
  "v_exp_f16 v213, v209\n"
  "v_exp_f16 v214, v210\n"
  "v_exp_f16 v215, v211\n"
  "v_mfma_f32_4x4x4_16b_f16 v[140:143], v[248:249], v[248:249], v[140:143]\n"
  "ds_read_u16 v241, v241\n"
  "v_exp_f16_sdwa v212, v208 dst_sel:WORD_1 dst_unused:UNUSED_PRESERVE src0_sel:WORD_1\n"
  "v_exp_f16_sdwa v213, v209 dst_sel:WORD_1 dst_unused:UNUSED_PRESERVE src0_sel:WORD_1\n"
  "v_exp_f16_sdwa v214, v210 dst_sel:WORD_1 dst_unused:UNUSED_PRESERVE src0_sel:WORD_1\n"
  "v_exp_f16_sdwa v215, v211 dst_sel:WORD_1 dst_unused:UNUSED_PRESERVE src0_sel:WORD_1\n"
  "v_pk_add_f16 v212, v212, 1.0 op_sel_hi:[1,0]\n"
  "v_pk_add_f16 v213, v213, 1.0 op_sel_hi:[1,0]\n"
  "v_pk_add_f16 v214, v214, 1.0 op_sel_hi:[1,0]\n"
  "v_mfma_f32_4x4x4_16b_f16 v[140:143], v[250:251], v[250:251], v[140:143]\n"
  "ds_read_u16 v242, v242\n"
  "ds_read_u16 v243, v243\n"
  "v_pk_add_f16 v215, v215, 1.0 op_sel_hi:[1,0]\n"
  "v_rcp_f16 v208, v212\n"
  "v_rcp_f16 v209, v213\n"
  "v_rcp_f16 v210, v214\n"
  "v_rcp_f16 v211, v215\n"
  "v_mfma_f32_4x4x4_16b_f16 v[140:143], v[252:253], v[252:253], v[140:143]\n"
  "ds_read_u16 v244, v244\n"
  "v_rcp_f16_sdwa v208, v212 dst_sel:WORD_1 dst_unused:UNUSED_PRESERVE src0_sel:WORD_1\n"
  "v_rcp_f16_sdwa v209, v213 dst_sel:WORD_1 dst_unused:UNUSED_PRESERVE src0_sel:WORD_1\n"
  "v_rcp_f16_sdwa v210, v214 dst_sel:WORD_1 dst_unused:UNUSED_PRESERVE src0_sel:WORD_1\n"
  "v_rcp_f16_sdwa v211, v215 dst_sel:WORD_1 dst_unused:UNUSED_PRESERVE src0_sel:WORD_1\n"
  "v_pk_mul_f16 v184, v184, v208\n"
  "v_pk_mul_f16 v185, v185, v209\n"
  "v_pk_mul_f16 v186, v186, v210\n"
  "v_mfma_f32_4x4x4_16b_f16 v[140:143], v[254:255], v[254:255], v[140:143]\n"
  "ds_read_u16 v245, v245\n"
  "v_pk_mul_f16 v187, v187, v211\n"
  "s_sub_u32 s55, s55, 1\n"
  "s_cmp_lg_u32 s55, 0\n"
  "s_cbranch_scc1 .Lp1%=\n"
  ".Lsp%=:\n"
  "s_cbranch_vccnz .Lsp1%=\n"
  "s_waitcnt vmcnt(0)\n"
  ".Lsp1%=:\n"
  "buffer_load_dword v5, off, s[80:83], 0 offset:256 sc1\n"
  "v_bfe_u32 v6, v0, 4, 5\n"
  "v_lshlrev_b32 v6, 3, v6\n"
  "v_add_u32 v6, 160256, v6\n"
  "s_mov_b32 s86, 0x22222222\n"
  "s_mov_b32 s87, s86\n"
  "s_lshl_b64 s[88:89], s[86:87], 1\n"
  "s_lshl_b64 s[90:91], s[86:87], 2\n"
  "s_mov_b32 s84, 0x2f800000\n"
  "s_mov_b32 s85, 0x4f800000\n"
  "s_waitcnt lgkmcnt(3)\n"
  "v_mfma_f32_32x32x16_bf16 v[168:183], v[24:27], v[224:227], v[168:183]\n"
  "s_waitcnt lgkmcnt(2)\n"
  "s_nop 5\n"
  "v_lshlrev_b32_sdwa v240, 1, v160 dst_sel:DWORD dst_unused:UNUSED_PAD src0_sel:DWORD src1_sel:WORD_1\n"
  "v_cvt_pk_f16_f32 v184, v152, v153\n"
  "v_lshlrev_b32_sdwa v241, 1, v161 dst_sel:DWORD dst_unused:UNUSED_PAD src0_sel:DWORD src1_sel:WORD_1\n"
  "v_cvt_pk_f16_f32 v185, v154, v155\n"
  "s_setprio 2\n"
  "s_bitcmp1_b32 s72, 2\n"
  "s_cbranch_scc0 .Lpr18%=\n"
  "s_setprio 2\n"
  ".Lpr18%=:\n"
  "v_lshlrev_b32_sdwa v242, 1, v162 dst_sel:DWORD dst_unused:UNUSED_PAD src0_sel:DWORD src1_sel:WORD_1\n"
  "v_cvt_pk_f16_f32 v186, v156, v157\n"
  "v_lshlrev_b32_sdwa v243, 1, v163 dst_sel:DWORD dst_unused:UNUSED_PAD src0_sel:DWORD src1_sel:WORD_1\n"
  "v_cvt_pk_f16_f32 v187, v158, v159\n"
  "v_lshlrev_b32_sdwa v244, 1, v164 dst_sel:DWORD dst_unused:UNUSED_PAD src0_sel:DWORD src1_sel:WORD_1\n"
  "ds_read_b128 v[224:227], v0 offset:20480\n"
  "ds_read_u16 v240, v240\n"
  "ds_read_u16_d16_hi v241, v241\n"
  "v_lshlrev_b32_sdwa v245, 1, v165 dst_sel:DWORD dst_unused:UNUSED_PAD src0_sel:DWORD src1_sel:WORD_1\n"
  "v_mfma_f32_32x32x16_bf16 v[168:183], v[28:31], v[228:231], v[168:183]\n"
  "s_waitcnt lgkmcnt(4)\n"
  "v_lshlrev_b32_sdwa v246, 1, v166 dst_sel:DWORD dst_unused:UNUSED_PAD src0_sel:DWORD src1_sel:WORD_1\n"
  "v_lshlrev_b32_sdwa v247, 1, v167 dst_sel:DWORD dst_unused:UNUSED_PAD src0_sel:DWORD src1_sel:WORD_1\n"
  "v_pk_fma_f16 v208, v184, v184, s56\n"
  "v_pk_fma_f16 v209, v185, v185, s56\n"
  "v_pk_fma_f16 v210, v186, v186, s56\n"
  "v_pk_fma_f16 v211, v187, v187, s56\n"
  "v_pk_mul_f16 v208, v184, v208\n"
  "v_pk_mul_f16 v209, v185, v209\n"
  "v_pk_mul_f16 v210, v186, v210\n"
  "ds_read_b128 v[228:231], v0 offset:21504\n"
  "ds_read_u16 v242, v242\n"
  "ds_read_u16_d16_hi v243, v243\n"
  "v_pk_mul_f16 v211, v187, v211\n"
  "v_mfma_f32_32x32x16_bf16 v[152:167], v[8:11], v[216:219], 0\n"
  "s_waitcnt lgkmcnt(6)\n"
  "v_exp_f16 v212, v208\n"
  "v_exp_f16 v213, v209\n"
  "v_exp_f16 v214, v210\n"
  "v_exp_f16 v215, v211\n"
  "ds_read_b128 v[216:219], v0 offset:22528\n"
  "ds_read_u16 v244, v244\n"
  "v_exp_f16_sdwa v212, v208 dst_sel:WORD_1 dst_unused:UNUSED_PRESERVE src0_sel:WORD_1\n"
  "v_mfma_f32_32x32x16_bf16 v[152:167], v[12:15], v[220:223], v[152:167]\n"
  "s_waitcnt lgkmcnt(7)\n"
  "v_exp_f16_sdwa v213, v209 dst_sel:WORD_1 dst_unused:UNUSED_PRESERVE src0_sel:WORD_1\n"
  "v_exp_f16_sdwa v214, v210 dst_sel:WORD_1 dst_unused:UNUSED_PRESERVE src0_sel:WORD_1\n"
  "v_exp_f16_sdwa v215, v211 dst_sel:WORD_1 dst_unused:UNUSED_PRESERVE src0_sel:WORD_1\n"
  "v_pk_add_f16 v212, v212, 1.0 op_sel_hi:[1,0]\n"
  "v_pk_add_f16 v213, v213, 1.0 op_sel_hi:[1,0]\n"
  "v_pk_add_f16 v214, v214, 1.0 op_sel_hi:[1,0]\n"
  "ds_read_b128 v[220:223], v0 offset:23552\n"
  "ds_read_u16_d16_hi v245, v245\n"
  "v_pk_add_f16 v215, v215, 1.0 op_sel_hi:[1,0]\n"
  "v_mfma_f32_32x32x16_bf16 v[152:167], v[16:19], v[224:227], v[152:167]\n"
  "s_waitcnt lgkmcnt(6)\n"
  "v_rcp_f16 v208, v212\n"
  "v_rcp_f16 v209, v213\n"
  "v_rcp_f16 v210, v214\n"
  "v_rcp_f16 v211, v215\n"
  "ds_read_b128 v[224:227], v0 offset:24576\n"
  "ds_read_u16 v246, v246\n"
  "v_rcp_f16_sdwa v208, v212 dst_sel:WORD_1 dst_unused:UNUSED_PRESERVE src0_sel:WORD_1\n"
  "v_mfma_f32_32x32x16_bf16 v[152:167], v[20:23], v[228:231], v[152:167]\n"
  "s_waitcnt lgkmcnt(5)\n"
  "v_rcp_f16_sdwa v209, v213 dst_sel:WORD_1 dst_unused:UNUSED_PRESERVE src0_sel:WORD_1\n"
  "v_rcp_f16_sdwa v210, v214 dst_sel:WORD_1 dst_unused:UNUSED_PRESERVE src0_sel:WORD_1\n"
  "v_rcp_f16_sdwa v211, v215 dst_sel:WORD_1 dst_unused:UNUSED_PRESERVE src0_sel:WORD_1\n"
  "v_pk_mul_f16 v184, v184, v208\n"
  "v_pk_mul_f16 v185, v185, v209\n"
  "v_pk_mul_f16 v186, v186, v210\n"
  "v_pk_mul_f16 v187, v187, v211\n"
  "ds_read_b128 v[228:231], v0 offset:25600\n"
  "ds_read_u16_d16_hi v247, v247\n"
  "v_mfma_f32_32x32x16_bf16 v[152:167], v[24:27], v[216:219], v[152:167]\n"
  "s_waitcnt lgkmcnt(5)\n"
  "v_lshlrev_b32_sdwa v248, 1, v176 dst_sel:DWORD dst_unused:UNUSED_PAD src0_sel:DWORD src1_sel:WORD_1\n"
  "v_cvt_pk_f16_f32 v188, v168, v169\n"
  "v_lshlrev_b32_sdwa v249, 1, v177 dst_sel:DWORD dst_unused:UNUSED_PAD src0_sel:DWORD src1_sel:WORD_1\n"
  "v_cvt_pk_f16_f32 v189, v170, v171\n"
  "v_lshlrev_b32_sdwa v250, 1, v178 dst_sel:DWORD dst_unused:UNUSED_PAD src0_sel:DWORD src1_sel:WORD_1\n"
  "v_cvt_pk_f16_f32 v190, v172, v173\n"
  "v_lshlrev_b32_sdwa v251, 1, v179 dst_sel:DWORD dst_unused:UNUSED_PAD src0_sel:DWORD src1_sel:WORD_1\n"
  "v_cvt_pk_f16_f32 v191, v174, v175\n"
  "v_lshlrev_b32_sdwa v252, 1, v180 dst_sel:DWORD dst_unused:UNUSED_PAD src0_sel:DWORD src1_sel:WORD_1\n"
  "v_mfma_f32_4x4x4_16b_f16 v[104:107], v[184:185], v[184:185], v[104:107]\n"
  "v_lshlrev_b32_sdwa v253, 1, v181 dst_sel:DWORD dst_unused:UNUSED_PAD src0_sel:DWORD src1_sel:WORD_1\n"
  "v_lshlrev_b32_sdwa v254, 1, v182 dst_sel:DWORD dst_unused:UNUSED_PAD src0_sel:DWORD src1_sel:WORD_1\n"
  "v_mfma_f32_4x4x4_16b_f16 v[104:107], v[186:187], v[186:187], v[104:107]\n"
  "ds_read_b128 v[216:219], v0 offset:26624\n"
  "ds_read_u16 v248, v248\n"
  "ds_read_u16_d16_hi v249, v249\n"
  "v_lshlrev_b32_sdwa v255, 1, v183 dst_sel:DWORD dst_unused:UNUSED_PAD src0_sel:DWORD src1_sel:WORD_1\n"
  "v_mfma_f32_32x32x16_bf16 v[152:167], v[28:31], v[220:223], v[152:167]\n"
  "s_waitcnt lgkmcnt(6)\n"
  "v_pk_fma_f16 v208, v188, v188, s56\n"
  "v_pk_fma_f16 v209, v189, v189, s56\n"
  "v_pk_fma_f16 v210, v190, v190, s56\n"
  "v_pk_fma_f16 v211, v191, v191, s56\n"
  "v_pk_mul_f16 v208, v188, v208\n"
  "v_pk_mul_f16 v209, v189, v209\n"
  "v_pk_mul_f16 v210, v190, v210\n"
  "v_pk_mul_f16 v211, v191, v211\n"
  "v_mfma_f32_4x4x4_16b_f16 v[104:107], v[240:241], v[240:241], v[104:107]\n"
  "v_exp_f16 v212, v208\n"
  "v_exp_f16 v213, v209\n"
  "v_mfma_f32_4x4x4_16b_f16 v[104:107], v[242:243], v[242:243], v[104:107]\n"
  "ds_read_b128 v[220:223], v0 offset:27648\n"
  "ds_read_u16 v250, v250\n"
  "ds_read_u16_d16_hi v251, v251\n"
  "v_exp_f16 v214, v210\n"
  "v_mfma_f32_32x32x16_bf16 v[168:183], v[8:11], v[224:227], 0\n"
  "s_waitcnt lgkmcnt(6)\n"
  "v_exp_f16 v215, v211\n"
  "v_exp_f16_sdwa v212, v208 dst_sel:WORD_1 dst_unused:UNUSED_PRESERVE src0_sel:WORD_1\n"
  "v_exp_f16_sdwa v213, v209 dst_sel:WORD_1 dst_unused:UNUSED_PRESERVE src0_sel:WORD_1\n"
  "v_mfma_f32_4x4x4_16b_f16 v[104:107], v[244:245], v[244:245], v[104:107]\n"
  "v_exp_f16_sdwa v214, v210 dst_sel:WORD_1 dst_unused:UNUSED_PRESERVE src0_sel:WORD_1\n"
  "v_exp_f16_sdwa v215, v211 dst_sel:WORD_1 dst_unused:UNUSED_PRESERVE src0_sel:WORD_1\n"
  "v_mfma_f32_4x4x4_16b_f16 v[104:107], v[246:247], v[246:247], v[104:107]\n"
  "ds_read_b128 v[224:227], v0 offset:28672\n"
  "ds_read_u16 v252, v252\n"
  "v_pk_add_f16 v212, v212, 1.0 op_sel_hi:[1,0]\n"
  "v_mfma_f32_32x32x16_bf16 v[168:183], v[12:15], v[228:231], v[168:183]\n"
  "s_waitcnt lgkmcnt(7)\n"
  "v_pk_add_f16 v213, v213, 1.0 op_sel_hi:[1,0]\n"
  "v_pk_add_f16 v214, v214, 1.0 op_sel_hi:[1,0]\n"
  "v_cndmask_b32 v104, v104, v105, s[86:87]\n"
  "v_pk_add_f16 v215, v215, 1.0 op_sel_hi:[1,0]\n"
  "v_cndmask_b32 v104, v104, v106, s[88:89]\n"
  "v_rcp_f16 v208, v212\n"
  "v_cndmask_b32 v104, v104, v107, s[90:91]\n"
  "v_rcp_f16 v209, v213\n"
  "v_mul_f32 v104, s97, v104\n"
  "ds_read_b128 v[228:231], v0 offset:29696\n"
  "ds_read_u16_d16_hi v253, v253\n"
  "v_rcp_f16 v210, v214\n"
  "v_mfma_f32_32x32x16_bf16 v[168:183], v[16:19], v[216:219], v[168:183]\n"
  "s_waitcnt lgkmcnt(6)\n"
  "v_rcp_f16 v211, v215\n"
  "v_min_f32 v104, 0x54ffffff, v104\n"
  "s_setprio 1\n"
  "s_bitcmp1_b32 s72, 2\n"
  "s_cbranch_scc0 .Lpr19%=\n"
  "s_setprio 1\n"
  ".Lpr19%=:\n"
  "v_rcp_f16_sdwa v208, v212 dst_sel:WORD_1 dst_unused:UNUSED_PRESERVE src0_sel:WORD_1\n"
  "v_mul_f32 v105, s84, v104\n"
  "v_rcp_f16_sdwa v209, v213 dst_sel:WORD_1 dst_unused:UNUSED_PRESERVE src0_sel:WORD_1\n"
  "v_floor_f32 v105, v105\n"
  "v_rcp_f16_sdwa v210, v214 dst_sel:WORD_1 dst_unused:UNUSED_PRESERVE src0_sel:WORD_1\n"
  "ds_read_b128 v[216:219], v0 offset:30720\n"
  "ds_read_u16 v254, v254\n"
  "v_cvt_u32_f32 v107, v105\n"
  "v_mfma_f32_32x32x16_bf16 v[168:183], v[20:23], v[220:223], v[168:183]\n"
  "s_waitcnt lgkmcnt(5)\n"
  "v_rcp_f16_sdwa v211, v215 dst_sel:WORD_1 dst_unused:UNUSED_PRESERVE src0_sel:WORD_1\n"
  "s_waitcnt vmcnt(0)\n"
  "v_readfirstlane_b32 s50, v5\n"
  "s_cbranch_vccz .Lsp2%=\n"
  "s_mov_b64 exec, 1\n"
  "v_mov_b32 v2, 160148\n"
  "v_mov_b32 v3, 1\n"
  "ds_add_u32 v2, v3\n"
  "s_mov_b64 exec, -1\n"
  ".Lsp2%=:\n"
  "v_fma_f32 v104, -v105, s85, v104\n"
  "v_pk_mul_f16 v188, v188, v208\n"
  "v_cvt_u32_f32 v106, v104\n"
  "v_pk_mul_f16 v189, v189, v209\n"
  "v_pk_mul_f16 v190, v190, v210\n"
  "v_pk_mul_f16 v191, v191, v211\n"
  "ds_read_b128 v[220:223], v0 offset:31744\n"
  "ds_read_u16_d16_hi v255, v255\n"
  "ds_add_u64 v6, v[106:107] offset:0\n"
  "v_or_b32 v104, v240, v241\n"
  "v_or_b32 v105, v242, v243\n"
  "v_or_b32 v106, v244, v245\n"
  "v_or_b32 v107, v246, v247\n"
  "v_mfma_f32_32x32x16_bf16 v[168:183], v[24:27], v[224:227], v[168:183]\n"
  "s_waitcnt lgkmcnt(6)\n"
  "v_lshlrev_b32_sdwa v240, 1, v160 dst_sel:DWORD dst_unused:UNUSED_PAD src0_sel:DWORD src1_sel:WORD_1\n"
  "v_cvt_pk_f16_f32 v192, v152, v153\n"
  "v_lshlrev_b32_sdwa v241, 1, v161 dst_sel:DWORD dst_unused:UNUSED_PAD src0_sel:DWORD src1_sel:WORD_1\n"
  "v_cvt_pk_f16_f32 v193, v154, v155\n"
  "v_lshlrev_b32_sdwa v242, 1, v162 dst_sel:DWORD dst_unused:UNUSED_PAD src0_sel:DWORD src1_sel:WORD_1\n"
  "v_cvt_pk_f16_f32 v194, v156, v157\n"
  "v_lshlrev_b32_sdwa v243, 1, v163 dst_sel:DWORD dst_unused:UNUSED_PAD src0_sel:DWORD src1_sel:WORD_1\n"
  "v_cvt_pk_f16_f32 v195, v158, v159\n"
  "v_lshlrev_b32_sdwa v244, 1, v164 dst_sel:DWORD dst_unused:UNUSED_PAD src0_sel:DWORD src1_sel:WORD_1\n"
  "v_mfma_f32_4x4x4_16b_f16 v[108:111], v[188:189], v[188:189], v[108:111]\n"
  "v_lshlrev_b32_sdwa v245, 1, v165 dst_sel:DWORD dst_unused:UNUSED_PAD src0_sel:DWORD src1_sel:WORD_1\n"
  "v_lshlrev_b32_sdwa v246, 1, v166 dst_sel:DWORD dst_unused:UNUSED_PAD src0_sel:DWORD src1_sel:WORD_1\n"
  "v_mfma_f32_4x4x4_16b_f16 v[108:111], v[190:191], v[190:191], v[108:111]\n"
  "ds_read_b128 v[224:227], v0 offset:32768\n"
  "ds_read_u16 v240, v240\n"
  "ds_read_u16_d16_hi v241, v241\n"
  "v_lshlrev_b32_sdwa v247, 1, v167 dst_sel:DWORD dst_unused:UNUSED_PAD src0_sel:DWORD src1_sel:WORD_1\n"
  "v_mfma_f32_32x32x16_bf16 v[168:183], v[28:31], v[228:231], v[168:183]\n"
  "s_waitcnt lgkmcnt(7)\n"
  "v_pk_fma_f16 v208, v192, v192, s56\n"
  "v_pk_fma_f16 v209, v193, v193, s56\n"
  "v_pk_fma_f16 v210, v194, v194, s56\n"
  "v_pk_fma_f16 v211, v195, v195, s56\n"
  "v_pk_mul_f16 v208, v192, v208\n"
  "v_pk_mul_f16 v209, v193, v209\n"
  "v_pk_mul_f16 v210, v194, v210\n"
  "v_pk_mul_f16 v211, v195, v211\n"
  "v_mfma_f32_4x4x4_16b_f16 v[108:111], v[248:249], v[248:249], v[108:111]\n"
  "v_exp_f16 v212, v208\n"
  "v_exp_f16 v213, v209\n"
  "v_mfma_f32_4x4x4_16b_f16 v[108:111], v[250:251], v[250:251], v[108:111]\n"
  "ds_read_b128 v[228:231], v0 offset:33792\n"
  "ds_read_u16 v242, v242\n"
  "ds_read_u16_d16_hi v243, v243\n"
  "v_exp_f16 v214, v210\n"
  "v_mfma_f32_32x32x16_bf16 v[152:167], v[8:11], v[216:219], 0\n"
  "s_waitcnt lgkmcnt(7)\n"
  "v_exp_f16 v215, v211\n"
  "v_exp_f16_sdwa v212, v208 dst_sel:WORD_1 dst_unused:UNUSED_PRESERVE src0_sel:WORD_1\n"
  "v_exp_f16_sdwa v213, v209 dst_sel:WORD_1 dst_unused:UNUSED_PRESERVE src0_sel:WORD_1\n"
  "v_mfma_f32_4x4x4_16b_f16 v[108:111], v[252:253], v[252:253], v[108:111]\n"
  "v_exp_f16_sdwa v214, v210 dst_sel:WORD_1 dst_unused:UNUSED_PRESERVE src0_sel:WORD_1\n"
  "v_exp_f16_sdwa v215, v211 dst_sel:WORD_1 dst_unused:UNUSED_PRESERVE src0_sel:WORD_1\n"
  "v_mfma_f32_4x4x4_16b_f16 v[108:111], v[254:255], v[254:255], v[108:111]\n"
  "ds_read_b128 v[216:219], v0 offset:34816\n"
  "ds_read_u16 v244, v244\n"
  "v_pk_add_f16 v212, v212, 1.0 op_sel_hi:[1,0]\n"
  "v_mfma_f32_32x32x16_bf16 v[152:167], v[12:15], v[220:223], v[152:167]\n"
  "s_waitcnt lgkmcnt(7)\n"
  "v_pk_add_f16 v213, v213, 1.0 op_sel_hi:[1,0]\n"
  "v_pk_add_f16 v214, v214, 1.0 op_sel_hi:[1,0]\n"
  "v_cndmask_b32 v108, v108, v109, s[86:87]\n"
  "v_pk_add_f16 v215, v215, 1.0 op_sel_hi:[1,0]\n"
  "v_cndmask_b32 v108, v108, v110, s[88:89]\n"
  "v_rcp_f16 v208, v212\n"
  "v_cndmask_b32 v108, v108, v111, s[90:91]\n"
  "v_rcp_f16 v209, v213\n"
  "v_mul_f32 v108, s97, v108\n"
  "ds_read_b128 v[220:223], v0 offset:35840\n"
  "ds_read_u16_d16_hi v245, v245\n"
  "v_rcp_f16 v210, v214\n"
  "v_mfma_f32_32x32x16_bf16 v[152:167], v[16:19], v[224:227], v[152:167]\n"
  "s_waitcnt lgkmcnt(6)\n"
  "v_rcp_f16 v211, v215\n"
  "v_min_f32 v108, 0x54ffffff, v108\n"
  "v_rcp_f16_sdwa v208, v212 dst_sel:WORD_1 dst_unused:UNUSED_PRESERVE src0_sel:WORD_1\n"
  "v_mul_f32 v109, s84, v108\n"
  "v_rcp_f16_sdwa v209, v213 dst_sel:WORD_1 dst_unused:UNUSED_PRESERVE src0_sel:WORD_1\n"
  "v_floor_f32 v109, v109\n"
  "v_rcp_f16_sdwa v210, v214 dst_sel:WORD_1 dst_unused:UNUSED_PRESERVE src0_sel:WORD_1\n"
  "ds_read_b128 v[224:227], v0 offset:36864\n"
  "ds_read_u16 v246, v246\n"
  "v_cvt_u32_f32 v111, v109\n"
  "v_mfma_f32_32x32x16_bf16 v[152:167], v[20:23], v[228:231], v[152:167]\n"
  "s_waitcnt lgkmcnt(5)\n"
  "v_rcp_f16_sdwa v211, v215 dst_sel:WORD_1 dst_unused:UNUSED_PRESERVE src0_sel:WORD_1\n"
  "v_fma_f32 v108, -v109, s85, v108\n"
  "v_pk_mul_f16 v192, v192, v208\n"
  "v_cvt_u32_f32 v110, v108\n"
  "v_pk_mul_f16 v193, v193, v209\n"
  "v_pk_mul_f16 v194, v194, v210\n"
  "v_pk_mul_f16 v195, v195, v211\n"
  "ds_read_b128 v[228:231], v0 offset:37888\n"
  "ds_read_u16_d16_hi v247, v247\n"
  "ds_add_u64 v6, v[110:111] offset:256\n"
  "v_or_b32 v108, v248, v249\n"
  "v_or_b32 v109, v250, v251\n"
  "v_or_b32 v110, v252, v253\n"
  "v_or_b32 v111, v254, v255\n"
  "v_mfma_f32_32x32x16_bf16 v[152:167], v[24:27], v[216:219], v[152:167]\n"
  "s_waitcnt lgkmcnt(6)\n"
  "v_lshlrev_b32_sdwa v248, 1, v176 dst_sel:DWORD dst_unused:UNUSED_PAD src0_sel:DWORD src1_sel:WORD_1\n"
  "v_cvt_pk_f16_f32 v196, v168, v169\n"
  "v_lshlrev_b32_sdwa v249, 1, v177 dst_sel:DWORD dst_unused:UNUSED_PAD src0_sel:DWORD src1_sel:WORD_1\n"
  "v_cvt_pk_f16_f32 v197, v170, v171\n"
  "v_lshlrev_b32_sdwa v250, 1, v178 dst_sel:DWORD dst_unused:UNUSED_PAD src0_sel:DWORD src1_sel:WORD_1\n"
  "v_cvt_pk_f16_f32 v198, v172, v173\n"
  "v_lshlrev_b32_sdwa v251, 1, v179 dst_sel:DWORD dst_unused:UNUSED_PAD src0_sel:DWORD src1_sel:WORD_1\n"
  "v_cvt_pk_f16_f32 v199, v174, v175\n"
  "v_lshlrev_b32_sdwa v252, 1, v180 dst_sel:DWORD dst_unused:UNUSED_PAD src0_sel:DWORD src1_sel:WORD_1\n"
  "v_mfma_f32_4x4x4_16b_f16 v[112:115], v[192:193], v[192:193], v[112:115]\n"
  "v_lshlrev_b32_sdwa v253, 1, v181 dst_sel:DWORD dst_unused:UNUSED_PAD src0_sel:DWORD src1_sel:WORD_1\n"
  "v_lshlrev_b32_sdwa v254, 1, v182 dst_sel:DWORD dst_unused:UNUSED_PAD src0_sel:DWORD src1_sel:WORD_1\n"
  "v_mfma_f32_4x4x4_16b_f16 v[112:115], v[194:195], v[194:195], v[112:115]\n"
  "ds_read_b128 v[216:219], v0 offset:38912\n"
  "ds_read_u16 v248, v248\n"
  "ds_read_u16_d16_hi v249, v249\n"
  "v_lshlrev_b32_sdwa v255, 1, v183 dst_sel:DWORD dst_unused:UNUSED_PAD src0_sel:DWORD src1_sel:WORD_1\n"
  "v_mfma_f32_32x32x16_bf16 v[152:167], v[28:31], v[220:223], v[152:167]\n"
  "s_waitcnt lgkmcnt(7)\n"
  "v_pk_fma_f16 v208, v196, v196, s56\n"
  "v_pk_fma_f16 v209, v197, v197, s56\n"
  "v_pk_fma_f16 v210, v198, v198, s56\n"
  "v_pk_fma_f16 v211, v199, v199, s56\n"
  "v_pk_mul_f16 v208, v196, v208\n"
  "s_setprio 0\n"
  "s_bitcmp1_b32 s72, 2\n"
  "s_cbranch_scc0 .Lpr20%=\n"
  "s_setprio 1\n"
  ".Lpr20%=:\n"
  "v_pk_mul_f16 v209, v197, v209\n"
  "v_pk_mul_f16 v210, v198, v210\n"
  "v_pk_mul_f16 v211, v199, v211\n"
  "v_mfma_f32_4x4x4_16b_f16 v[112:115], v[240:241], v[240:241], v[112:115]\n"
  "v_exp_f16 v212, v208\n"
  "v_exp_f16 v213, v209\n"
  "v_mfma_f32_4x4x4_16b_f16 v[112:115], v[242:243], v[242:243], v[112:115]\n"
  "ds_read_b128 v[220:223], v0 offset:39936\n"
  "ds_read_u16 v250, v250\n"
  "ds_read_u16_d16_hi v251, v251\n"
  "v_exp_f16 v214, v210\n"
  "v_mfma_f32_32x32x16_bf16 v[168:183], v[8:11], v[224:227], 0\n"
  "s_waitcnt lgkmcnt(7)\n"
  "v_exp_f16 v215, v211\n"
  "v_exp_f16_sdwa v212, v208 dst_sel:WORD_1 dst_unused:UNUSED_PRESERVE src0_sel:WORD_1\n"
  "v_exp_f16_sdwa v213, v209 dst_sel:WORD_1 dst_unused:UNUSED_PRESERVE src0_sel:WORD_1\n"
  "v_mfma_f32_4x4x4_16b_f16 v[112:115], v[244:245], v[244:245], v[112:115]\n"
  "v_exp_f16_sdwa v214, v210 dst_sel:WORD_1 dst_unused:UNUSED_PRESERVE src0_sel:WORD_1\n"
  "v_exp_f16_sdwa v215, v211 dst_sel:WORD_1 dst_unused:UNUSED_PRESERVE src0_sel:WORD_1\n"
  "v_mfma_f32_4x4x4_16b_f16 v[112:115], v[246:247], v[246:247], v[112:115]\n"
  "ds_read_b128 v[224:227], v0 offset:40960\n"
  "ds_read_u16 v252, v252\n"
  "v_pk_add_f16 v212, v212, 1.0 op_sel_hi:[1,0]\n"
  "v_mfma_f32_32x32x16_bf16 v[168:183], v[12:15], v[228:231], v[168:183]\n"
  "s_waitcnt lgkmcnt(7)\n"
  "v_pk_add_f16 v213, v213, 1.0 op_sel_hi:[1,0]\n"
  "v_pk_add_f16 v214, v214, 1.0 op_sel_hi:[1,0]\n"
  "v_cndmask_b32 v112, v112, v113, s[86:87]\n"
  "v_pk_add_f16 v215, v215, 1.0 op_sel_hi:[1,0]\n"
  "v_cndmask_b32 v112, v112, v114, s[88:89]\n"
  "v_rcp_f16 v208, v212\n"
  "v_cndmask_b32 v112, v112, v115, s[90:91]\n"
  "v_rcp_f16 v209, v213\n"
  "v_mul_f32 v112, s97, v112\n"
  "ds_read_b128 v[228:231], v0 offset:41984\n"
  "ds_read_u16_d16_hi v253, v253\n"
  "v_rcp_f16 v210, v214\n"
  "v_mfma_f32_32x32x16_bf16 v[168:183], v[16:19], v[216:219], v[168:183]\n"
  "s_waitcnt lgkmcnt(6)\n"
  "v_rcp_f16 v211, v215\n"
  "v_min_f32 v112, 0x54ffffff, v112\n"
  "v_rcp_f16_sdwa v208, v212 dst_sel:WORD_1 dst_unused:UNUSED_PRESERVE src0_sel:WORD_1\n"
  "v_mul_f32 v113, s84, v112\n"
  "v_rcp_f16_sdwa v209, v213 dst_sel:WORD_1 dst_unused:UNUSED_PRESERVE src0_sel:WORD_1\n"
  "v_floor_f32 v113, v113\n"
  "v_rcp_f16_sdwa v210, v214 dst_sel:WORD_1 dst_unused:UNUSED_PRESERVE src0_sel:WORD_1\n"
  "ds_read_b128 v[216:219], v0 offset:43008\n"
  "ds_read_u16 v254, v254\n"
  "v_cvt_u32_f32 v115, v113\n"
  "v_mfma_f32_32x32x16_bf16 v[168:183], v[20:23], v[220:223], v[168:183]\n"
  "s_waitcnt lgkmcnt(5)\n"
  "v_rcp_f16_sdwa v211, v215 dst_sel:WORD_1 dst_unused:UNUSED_PRESERVE src0_sel:WORD_1\n"
  "v_fma_f32 v112, -v113, s85, v112\n"
  "v_pk_mul_f16 v196, v196, v208\n"
  "v_cvt_u32_f32 v114, v112\n"
  "v_pk_mul_f16 v197, v197, v209\n"
  "v_pk_mul_f16 v198, v198, v210\n"
  "v_pk_mul_f16 v199, v199, v211\n"
  "ds_read_b128 v[220:223], v0 offset:44032\n"
  "ds_read_u16_d16_hi v255, v255\n"
  "ds_add_u64 v6, v[114:115] offset:512\n"
  "v_or_b32 v112, v240, v241\n"
  "v_or_b32 v113, v242, v243\n"
  "v_or_b32 v114, v244, v245\n"
  "v_or_b32 v115, v246, v247\n"
  "v_mfma_f32_32x32x16_bf16 v[168:183], v[24:27], v[224:227], v[168:183]\n"
  "s_waitcnt lgkmcnt(6)\n"
  "v_lshlrev_b32_sdwa v240, 1, v160 dst_sel:DWORD dst_unused:UNUSED_PAD src0_sel:DWORD src1_sel:WORD_1\n"
  "v_cvt_pk_f16_f32 v200, v152, v153\n"
  "v_lshlrev_b32_sdwa v241, 1, v161 dst_sel:DWORD dst_unused:UNUSED_PAD src0_sel:DWORD src1_sel:WORD_1\n"
  "v_cvt_pk_f16_f32 v201, v154, v155\n"
  "v_lshlrev_b32_sdwa v242, 1, v162 dst_sel:DWORD dst_unused:UNUSED_PAD src0_sel:DWORD src1_sel:WORD_1\n"
  "v_cvt_pk_f16_f32 v202, v156, v157\n"
  "v_lshlrev_b32_sdwa v243, 1, v163 dst_sel:DWORD dst_unused:UNUSED_PAD src0_sel:DWORD src1_sel:WORD_1\n"
  "v_cvt_pk_f16_f32 v203, v158, v159\n"
  "v_lshlrev_b32_sdwa v244, 1, v164 dst_sel:DWORD dst_unused:UNUSED_PAD src0_sel:DWORD src1_sel:WORD_1\n"
  "v_mfma_f32_4x4x4_16b_f16 v[116:119], v[196:197], v[196:197], v[116:119]\n"
  "v_lshlrev_b32_sdwa v245, 1, v165 dst_sel:DWORD dst_unused:UNUSED_PAD src0_sel:DWORD src1_sel:WORD_1\n"
  "v_lshlrev_b32_sdwa v246, 1, v166 dst_sel:DWORD dst_unused:UNUSED_PAD src0_sel:DWORD src1_sel:WORD_1\n"
  "v_mfma_f32_4x4x4_16b_f16 v[116:119], v[198:199], v[198:199], v[116:119]\n"
  "ds_read_b128 v[224:227], v0 offset:45056\n"
  "ds_read_u16 v240, v240\n"
  "ds_read_u16_d16_hi v241, v241\n"
  "v_lshlrev_b32_sdwa v247, 1, v167 dst_sel:DWORD dst_unused:UNUSED_PAD src0_sel:DWORD src1_sel:WORD_1\n"
  "v_mfma_f32_32x32x16_bf16 v[168:183], v[28:31], v[228:231], v[168:183]\n"
  "s_waitcnt lgkmcnt(7)\n"
  "v_pk_fma_f16 v208, v200, v200, s56\n"
  "v_pk_fma_f16 v209, v201, v201, s56\n"
  "v_pk_fma_f16 v210, v202, v202, s56\n"
  "v_pk_fma_f16 v211, v203, v203, s56\n"
  "v_pk_mul_f16 v208, v200, v208\n"
  "v_pk_mul_f16 v209, v201, v209\n"
  "v_pk_mul_f16 v210, v202, v210\n"
  "v_pk_mul_f16 v211, v203, v211\n"
  "v_mfma_f32_4x4x4_16b_f16 v[116:119], v[248:249], v[248:249], v[116:119]\n"
  "v_exp_f16 v212, v208\n"
  "v_exp_f16 v213, v209\n"
  "v_mfma_f32_4x4x4_16b_f16 v[116:119], v[250:251], v[250:251], v[116:119]\n"
  "ds_read_b128 v[228:231], v0 offset:46080\n"
  "ds_read_u16 v242, v242\n"
  "ds_read_u16_d16_hi v243, v243\n"
  "v_exp_f16 v214, v210\n"
  "v_mfma_f32_32x32x16_bf16 v[152:167], v[8:11], v[216:219], 0\n"
  "s_waitcnt lgkmcnt(7)\n"
  "v_exp_f16 v215, v211\n"
  "v_exp_f16_sdwa v212, v208 dst_sel:WORD_1 dst_unused:UNUSED_PRESERVE src0_sel:WORD_1\n"
  "v_exp_f16_sdwa v213, v209 dst_sel:WORD_1 dst_unused:UNUSED_PRESERVE src0_sel:WORD_1\n"
  "v_mfma_f32_4x4x4_16b_f16 v[116:119], v[252:253], v[252:253], v[116:119]\n"
  "v_exp_f16_sdwa v214, v210 dst_sel:WORD_1 dst_unused:UNUSED_PRESERVE src0_sel:WORD_1\n"
  "v_exp_f16_sdwa v215, v211 dst_sel:WORD_1 dst_unused:UNUSED_PRESERVE src0_sel:WORD_1\n"
  "v_mfma_f32_4x4x4_16b_f16 v[116:119], v[254:255], v[254:255], v[116:119]\n"
  "ds_read_b128 v[216:219], v0 offset:47104\n"
  "ds_read_u16 v244, v244\n"
  "v_pk_add_f16 v212, v212, 1.0 op_sel_hi:[1,0]\n"
  "v_mfma_f32_32x32x16_bf16 v[152:167], v[12:15], v[220:223], v[152:167]\n"
  "s_waitcnt lgkmcnt(7)\n"
  "v_pk_add_f16 v213, v213, 1.0 op_sel_hi:[1,0]\n"
  "v_pk_add_f16 v214, v214, 1.0 op_sel_hi:[1,0]\n"
  "v_cndmask_b32 v116, v116, v117, s[86:87]\n"
  "v_pk_add_f16 v215, v215, 1.0 op_sel_hi:[1,0]\n"
  "v_cndmask_b32 v116, v116, v118, s[88:89]\n"
  "v_rcp_f16 v208, v212\n"
  "v_cndmask_b32 v116, v116, v119, s[90:91]\n"
  "v_rcp_f16 v209, v213\n"
  "v_mul_f32 v116, s97, v116\n"
  "ds_read_b128 v[220:223], v0 offset:48128\n"
  "ds_read_u16_d16_hi v245, v245\n"
  "v_rcp_f16 v210, v214\n"
  "v_mfma_f32_32x32x16_bf16 v[152:167], v[16:19], v[224:227], v[152:167]\n"
  "s_waitcnt lgkmcnt(6)\n"
  "v_rcp_f16 v211, v215\n"
  "v_min_f32 v116, 0x54ffffff, v116\n"
  "v_rcp_f16_sdwa v208, v212 dst_sel:WORD_1 dst_unused:UNUSED_PRESERVE src0_sel:WORD_1\n"
  "v_mul_f32 v117, s84, v116\n"
  "v_rcp_f16_sdwa v209, v213 dst_sel:WORD_1 dst_unused:UNUSED_PRESERVE src0_sel:WORD_1\n"
  "v_floor_f32 v117, v117\n"
  "v_rcp_f16_sdwa v210, v214 dst_sel:WORD_1 dst_unused:UNUSED_PRESERVE src0_sel:WORD_1\n"
  "ds_read_b128 v[224:227], v0 offset:49152\n"
  "ds_read_u16 v246, v246\n"
  "v_cvt_u32_f32 v119, v117\n"
  "v_mfma_f32_32x32x16_bf16 v[152:167], v[20:23], v[228:231], v[152:167]\n"
  "s_waitcnt lgkmcnt(5)\n"
  "v_rcp_f16_sdwa v211, v215 dst_sel:WORD_1 dst_unused:UNUSED_PRESERVE src0_sel:WORD_1\n"
  "v_fma_f32 v116, -v117, s85, v116\n"
  "v_pk_mul_f16 v200, v200, v208\n"
  "v_cvt_u32_f32 v118, v116\n"
  "v_pk_mul_f16 v201, v201, v209\n"
  "v_pk_mul_f16 v202, v202, v210\n"
  "v_pk_mul_f16 v203, v203, v211\n"
  "ds_read_b128 v[228:231], v0 offset:50176\n"
  "ds_read_u16_d16_hi v247, v247\n"
  "ds_add_u64 v6, v[118:119] offset:768\n"
  "v_or_b32 v116, v248, v249\n"
  "v_or_b32 v117, v250, v251\n"
  "v_or_b32 v118, v252, v253\n"
  "v_or_b32 v119, v254, v255\n"
  "v_mfma_f32_32x32x16_bf16 v[152:167], v[24:27], v[216:219], v[152:167]\n"
  "s_waitcnt lgkmcnt(6)\n"
  "v_lshlrev_b32_sdwa v248, 1, v176 dst_sel:DWORD dst_unused:UNUSED_PAD src0_sel:DWORD src1_sel:WORD_1\n"
  "v_cvt_pk_f16_f32 v204, v168, v169\n"
  "v_lshlrev_b32_sdwa v249, 1, v177 dst_sel:DWORD dst_unused:UNUSED_PAD src0_sel:DWORD src1_sel:WORD_1\n"
  "v_cvt_pk_f16_f32 v205, v170, v171\n"
  "v_lshlrev_b32_sdwa v250, 1, v178 dst_sel:DWORD dst_unused:UNUSED_PAD src0_sel:DWORD src1_sel:WORD_1\n"
  "v_cvt_pk_f16_f32 v206, v172, v173\n"
  "v_lshlrev_b32_sdwa v251, 1, v179 dst_sel:DWORD dst_unused:UNUSED_PAD src0_sel:DWORD src1_sel:WORD_1\n"
  "s_setprio 2\n"
  "s_bitcmp1_b32 s72, 2\n"
  "s_cbranch_scc0 .Lpr21%=\n"
  "s_setprio 3\n"
  ".Lpr21%=:\n"
  "v_cvt_pk_f16_f32 v207, v174, v175\n"
  "v_lshlrev_b32_sdwa v252, 1, v180 dst_sel:DWORD dst_unused:UNUSED_PAD src0_sel:DWORD src1_sel:WORD_1\n"
  "v_mfma_f32_4x4x4_16b_f16 v[120:123], v[200:201], v[200:201], v[120:123]\n"
  "v_lshlrev_b32_sdwa v253, 1, v181 dst_sel:DWORD dst_unused:UNUSED_PAD src0_sel:DWORD src1_sel:WORD_1\n"
  "v_lshlrev_b32_sdwa v254, 1, v182 dst_sel:DWORD dst_unused:UNUSED_PAD src0_sel:DWORD src1_sel:WORD_1\n"
  "v_mfma_f32_4x4x4_16b_f16 v[120:123], v[202:203], v[202:203], v[120:123]\n"
  "ds_read_b128 v[216:219], v0 offset:51200\n"
  "ds_read_u16 v248, v248\n"
  "ds_read_u16_d16_hi v249, v249\n"
  "v_lshlrev_b32_sdwa v255, 1, v183 dst_sel:DWORD dst_unused:UNUSED_PAD src0_sel:DWORD src1_sel:WORD_1\n"
  "v_mfma_f32_32x32x16_bf16 v[152:167], v[28:31], v[220:223], v[152:167]\n"
  "s_waitcnt lgkmcnt(7)\n"
  "v_pk_fma_f16 v208, v204, v204, s56\n"
  "v_pk_fma_f16 v209, v205, v205, s56\n"
  "v_pk_fma_f16 v210, v206, v206, s56\n"
  "v_pk_fma_f16 v211, v207, v207, s56\n"
  "v_pk_mul_f16 v208, v204, v208\n"
  "v_pk_mul_f16 v209, v205, v209\n"
  "v_pk_mul_f16 v210, v206, v210\n"
  "v_pk_mul_f16 v211, v207, v211\n"
  "v_mfma_f32_4x4x4_16b_f16 v[120:123], v[240:241], v[240:241], v[120:123]\n"
  "v_exp_f16 v212, v208\n"
  "v_exp_f16 v213, v209\n"
  "v_mfma_f32_4x4x4_16b_f16 v[120:123], v[242:243], v[242:243], v[120:123]\n"
  "ds_read_b128 v[220:223], v0 offset:52224\n"
  "ds_read_u16 v250, v250\n"
  "ds_read_u16_d16_hi v251, v251\n"
  "v_exp_f16 v214, v210\n"
  "v_mfma_f32_32x32x16_bf16 v[168:183], v[8:11], v[224:227], 0\n"
  "s_waitcnt lgkmcnt(7)\n"
  "v_exp_f16 v215, v211\n"
  "v_exp_f16_sdwa v212, v208 dst_sel:WORD_1 dst_unused:UNUSED_PRESERVE src0_sel:WORD_1\n"
  "v_exp_f16_sdwa v213, v209 dst_sel:WORD_1 dst_unused:UNUSED_PRESERVE src0_sel:WORD_1\n"
  "v_mfma_f32_4x4x4_16b_f16 v[120:123], v[244:245], v[244:245], v[120:123]\n"
  "v_exp_f16_sdwa v214, v210 dst_sel:WORD_1 dst_unused:UNUSED_PRESERVE src0_sel:WORD_1\n"
  "v_exp_f16_sdwa v215, v211 dst_sel:WORD_1 dst_unused:UNUSED_PRESERVE src0_sel:WORD_1\n"
  "v_mfma_f32_4x4x4_16b_f16 v[120:123], v[246:247], v[246:247], v[120:123]\n"
  "ds_read_b128 v[224:227], v0 offset:53248\n"
  "ds_read_u16 v252, v252\n"
  "v_pk_add_f16 v212, v212, 1.0 op_sel_hi:[1,0]\n"
  "v_mfma_f32_32x32x16_bf16 v[168:183], v[12:15], v[228:231], v[168:183]\n"
  "s_waitcnt lgkmcnt(7)\n"
  "v_pk_add_f16 v213, v213, 1.0 op_sel_hi:[1,0]\n"
  "v_pk_add_f16 v214, v214, 1.0 op_sel_hi:[1,0]\n"
  "v_cndmask_b32 v120, v120, v121, s[86:87]\n"
  "v_pk_add_f16 v215, v215, 1.0 op_sel_hi:[1,0]\n"
  "v_cndmask_b32 v120, v120, v122, s[88:89]\n"
  "v_rcp_f16 v208, v212\n"
  "v_cndmask_b32 v120, v120, v123, s[90:91]\n"
  "v_rcp_f16 v209, v213\n"
  "v_mul_f32 v120, s97, v120\n"
  "ds_read_b128 v[228:231], v0 offset:54272\n"
  "ds_read_u16_d16_hi v253, v253\n"
  "v_rcp_f16 v210, v214\n"
  "v_mfma_f32_32x32x16_bf16 v[168:183], v[16:19], v[216:219], v[168:183]\n"
  "s_waitcnt lgkmcnt(6)\n"
  "v_rcp_f16 v211, v215\n"
  "v_min_f32 v120, 0x54ffffff, v120\n"
  "v_rcp_f16_sdwa v208, v212 dst_sel:WORD_1 dst_unused:UNUSED_PRESERVE src0_sel:WORD_1\n"
  "v_mul_f32 v121, s84, v120\n"
  "v_rcp_f16_sdwa v209, v213 dst_sel:WORD_1 dst_unused:UNUSED_PRESERVE src0_sel:WORD_1\n"
  "v_floor_f32 v121, v121\n"
  "v_rcp_f16_sdwa v210, v214 dst_sel:WORD_1 dst_unused:UNUSED_PRESERVE src0_sel:WORD_1\n"
  "ds_read_b128 v[216:219], v0 offset:55296\n"
  "ds_read_u16 v254, v254\n"
  "v_cvt_u32_f32 v123, v121\n"
  "v_mfma_f32_32x32x16_bf16 v[168:183], v[20:23], v[220:223], v[168:183]\n"
  "s_waitcnt lgkmcnt(5)\n"
  "v_rcp_f16_sdwa v211, v215 dst_sel:WORD_1 dst_unused:UNUSED_PRESERVE src0_sel:WORD_1\n"
  "v_fma_f32 v120, -v121, s85, v120\n"
  "v_pk_mul_f16 v204, v204, v208\n"
  "v_cvt_u32_f32 v122, v120\n"
  "v_pk_mul_f16 v205, v205, v209\n"
  "v_pk_mul_f16 v206, v206, v210\n"
  "v_pk_mul_f16 v207, v207, v211\n"
  "ds_read_b128 v[220:223], v0 offset:56320\n"
  "ds_read_u16_d16_hi v255, v255\n"
  "ds_add_u64 v6, v[122:123] offset:1024\n"
  "v_or_b32 v120, v240, v241\n"
  "v_or_b32 v121, v242, v243\n"
  "v_or_b32 v122, v244, v245\n"
  "v_or_b32 v123, v246, v247\n"
  "v_mfma_f32_32x32x16_bf16 v[168:183], v[24:27], v[224:227], v[168:183]\n"
  "s_waitcnt lgkmcnt(6)\n"
  "v_lshlrev_b32_sdwa v240, 1, v160 dst_sel:DWORD dst_unused:UNUSED_PAD src0_sel:DWORD src1_sel:WORD_1\n"
  "v_cvt_pk_f16_f32 v232, v152, v153\n"
  "v_lshlrev_b32_sdwa v241, 1, v161 dst_sel:DWORD dst_unused:UNUSED_PAD src0_sel:DWORD src1_sel:WORD_1\n"
  "v_cvt_pk_f16_f32 v233, v154, v155\n"
  "v_lshlrev_b32_sdwa v242, 1, v162 dst_sel:DWORD dst_unused:UNUSED_PAD src0_sel:DWORD src1_sel:WORD_1\n"
  "v_cvt_pk_f16_f32 v234, v156, v157\n"
  "v_lshlrev_b32_sdwa v243, 1, v163 dst_sel:DWORD dst_unused:UNUSED_PAD src0_sel:DWORD src1_sel:WORD_1\n"
  "v_cvt_pk_f16_f32 v235, v158, v159\n"
  "v_lshlrev_b32_sdwa v244, 1, v164 dst_sel:DWORD dst_unused:UNUSED_PAD src0_sel:DWORD src1_sel:WORD_1\n"
  "v_mfma_f32_4x4x4_16b_f16 v[124:127], v[204:205], v[204:205], v[124:127]\n"
  "v_lshlrev_b32_sdwa v245, 1, v165 dst_sel:DWORD dst_unused:UNUSED_PAD src0_sel:DWORD src1_sel:WORD_1\n"
  "v_lshlrev_b32_sdwa v246, 1, v166 dst_sel:DWORD dst_unused:UNUSED_PAD src0_sel:DWORD src1_sel:WORD_1\n"
  "v_mfma_f32_4x4x4_16b_f16 v[124:127], v[206:207], v[206:207], v[124:127]\n"
  "ds_read_b128 v[224:227], v0 offset:57344\n"
  "ds_read_u16 v240, v240\n"
  "ds_read_u16_d16_hi v241, v241\n"
  "v_lshlrev_b32_sdwa v247, 1, v167 dst_sel:DWORD dst_unused:UNUSED_PAD src0_sel:DWORD src1_sel:WORD_1\n"
  "v_mfma_f32_32x32x16_bf16 v[168:183], v[28:31], v[228:231], v[168:183]\n"
  "s_waitcnt lgkmcnt(7)\n"
  "v_pk_fma_f16 v208, v232, v232, s56\n"
  "v_pk_fma_f16 v209, v233, v233, s56\n"
  "v_pk_fma_f16 v210, v234, v234, s56\n"
  "v_pk_fma_f16 v211, v235, v235, s56\n"
  "v_pk_mul_f16 v208, v232, v208\n"
  "v_pk_mul_f16 v209, v233, v209\n"
  "v_pk_mul_f16 v210, v234, v210\n"
  "v_pk_mul_f16 v211, v235, v211\n"
  "v_mfma_f32_4x4x4_16b_f16 v[124:127], v[248:249], v[248:249], v[124:127]\n"
  "v_exp_f16 v212, v208\n"
  "v_exp_f16 v213, v209\n"
  "v_mfma_f32_4x4x4_16b_f16 v[124:127], v[250:251], v[250:251], v[124:127]\n"
  "ds_read_b128 v[228:231], v0 offset:58368\n"
  "ds_read_u16 v242, v242\n"
  "ds_read_u16_d16_hi v243, v243\n"
  "v_exp_f16 v214, v210\n"
  "v_mfma_f32_32x32x16_bf16 v[152:167], v[8:11], v[216:219], 0\n"
  "s_waitcnt lgkmcnt(7)\n"
  "v_exp_f16 v215, v211\n"
  "v_exp_f16_sdwa v212, v208 dst_sel:WORD_1 dst_unused:UNUSED_PRESERVE src0_sel:WORD_1\n"
  "v_exp_f16_sdwa v213, v209 dst_sel:WORD_1 dst_unused:UNUSED_PRESERVE src0_sel:WORD_1\n"
  "v_mfma_f32_4x4x4_16b_f16 v[124:127], v[252:253], v[252:253], v[124:127]\n"
  "v_exp_f16_sdwa v214, v210 dst_sel:WORD_1 dst_unused:UNUSED_PRESERVE src0_sel:WORD_1\n"
  "v_exp_f16_sdwa v215, v211 dst_sel:WORD_1 dst_unused:UNUSED_PRESERVE src0_sel:WORD_1\n"
  "v_mfma_f32_4x4x4_16b_f16 v[124:127], v[254:255], v[254:255], v[124:127]\n"
  "ds_read_b128 v[216:219], v0 offset:59392\n"
  "ds_read_u16 v244, v244\n"
  "v_pk_add_f16 v212, v212, 1.0 op_sel_hi:[1,0]\n"
  "v_mfma_f32_32x32x16_bf16 v[152:167], v[12:15], v[220:223], v[152:167]\n"
  "s_waitcnt lgkmcnt(7)\n"
  "s_setprio 1\n"
  "s_bitcmp1_b32 s72, 2\n"
  "s_cbranch_scc0 .Lpr22%=\n"
  "s_setprio 2\n"
  ".Lpr22%=:\n"
  "v_pk_add_f16 v213, v213, 1.0 op_sel_hi:[1,0]\n"
  "v_pk_add_f16 v214, v214, 1.0 op_sel_hi:[1,0]\n"
  "v_cndmask_b32 v124, v124, v125, s[86:87]\n"
  "v_pk_add_f16 v215, v215, 1.0 op_sel_hi:[1,0]\n"
  "v_cndmask_b32 v124, v124, v126, s[88:89]\n"
  "v_rcp_f16 v208, v212\n"
  "v_cndmask_b32 v124, v124, v127, s[90:91]\n"
  "v_rcp_f16 v209, v213\n"
  "v_mul_f32 v124, s97, v124\n"
  "ds_read_b128 v[220:223], v0 offset:60416\n"
  "ds_read_u16_d16_hi v245, v245\n"
  "v_rcp_f16 v210, v214\n"
  "v_mfma_f32_32x32x16_bf16 v[152:167], v[16:19], v[224:227], v[152:167]\n"
  "s_waitcnt lgkmcnt(6)\n"
  "v_rcp_f16 v211, v215\n"
  "v_min_f32 v124, 0x54ffffff, v124\n"
  "v_rcp_f16_sdwa v208, v212 dst_sel:WORD_1 dst_unused:UNUSED_PRESERVE src0_sel:WORD_1\n"
  "v_mul_f32 v125, s84, v124\n"
  "v_rcp_f16_sdwa v209, v213 dst_sel:WORD_1 dst_unused:UNUSED_PRESERVE src0_sel:WORD_1\n"
  "v_floor_f32 v125, v125\n"
  "v_rcp_f16_sdwa v210, v214 dst_sel:WORD_1 dst_unused:UNUSED_PRESERVE src0_sel:WORD_1\n"
  "ds_read_b128 v[224:227], v0 offset:61440\n"
  "ds_read_u16 v246, v246\n"
  "v_cvt_u32_f32 v127, v125\n"
  "v_mfma_f32_32x32x16_bf16 v[152:167], v[20:23], v[228:231], v[152:167]\n"
  "s_waitcnt lgkmcnt(5)\n"
  "v_rcp_f16_sdwa v211, v215 dst_sel:WORD_1 dst_unused:UNUSED_PRESERVE src0_sel:WORD_1\n"
  "v_fma_f32 v124, -v125, s85, v124\n"
  "v_pk_mul_f16 v232, v232, v208\n"
  "v_cvt_u32_f32 v126, v124\n"
  "v_pk_mul_f16 v233, v233, v209\n"
  "v_pk_mul_f16 v234, v234, v210\n"
  "v_pk_mul_f16 v235, v235, v211\n"
  "ds_read_b128 v[228:231], v0 offset:62464\n"
  "ds_read_u16_d16_hi v247, v247\n"
  "ds_add_u64 v6, v[126:127] offset:1280\n"
  "v_or_b32 v124, v248, v249\n"
  "v_or_b32 v125, v250, v251\n"
  "v_or_b32 v126, v252, v253\n"
  "v_or_b32 v127, v254, v255\n"
  "v_mfma_f32_32x32x16_bf16 v[152:167], v[24:27], v[216:219], v[152:167]\n"
  "s_waitcnt lgkmcnt(6)\n"
  "v_lshlrev_b32_sdwa v248, 1, v176 dst_sel:DWORD dst_unused:UNUSED_PAD src0_sel:DWORD src1_sel:WORD_1\n"
  "v_cvt_pk_f16_f32 v236, v168, v169\n"
  "v_lshlrev_b32_sdwa v249, 1, v177 dst_sel:DWORD dst_unused:UNUSED_PAD src0_sel:DWORD src1_sel:WORD_1\n"
  "v_cvt_pk_f16_f32 v237, v170, v171\n"
  "v_lshlrev_b32_sdwa v250, 1, v178 dst_sel:DWORD dst_unused:UNUSED_PAD src0_sel:DWORD src1_sel:WORD_1\n"
  "v_cvt_pk_f16_f32 v238, v172, v173\n"
  "v_lshlrev_b32_sdwa v251, 1, v179 dst_sel:DWORD dst_unused:UNUSED_PAD src0_sel:DWORD src1_sel:WORD_1\n"
  "v_cvt_pk_f16_f32 v239, v174, v175\n"
  "v_lshlrev_b32_sdwa v252, 1, v180 dst_sel:DWORD dst_unused:UNUSED_PAD src0_sel:DWORD src1_sel:WORD_1\n"
  "v_mfma_f32_4x4x4_16b_f16 v[128:131], v[232:233], v[232:233], v[128:131]\n"
  "v_lshlrev_b32_sdwa v253, 1, v181 dst_sel:DWORD dst_unused:UNUSED_PAD src0_sel:DWORD src1_sel:WORD_1\n"
  "v_lshlrev_b32_sdwa v254, 1, v182 dst_sel:DWORD dst_unused:UNUSED_PAD src0_sel:DWORD src1_sel:WORD_1\n"
  "v_mfma_f32_4x4x4_16b_f16 v[128:131], v[234:235], v[234:235], v[128:131]\n"
  "ds_read_b128 v[216:219], v0 offset:63488\n"
  "ds_read_u16 v248, v248\n"
  "ds_read_u16_d16_hi v249, v249\n"
  "v_lshlrev_b32_sdwa v255, 1, v183 dst_sel:DWORD dst_unused:UNUSED_PAD src0_sel:DWORD src1_sel:WORD_1\n"
  "v_mfma_f32_32x32x16_bf16 v[152:167], v[28:31], v[220:223], v[152:167]\n"
  "s_waitcnt lgkmcnt(7)\n"
  "v_pk_fma_f16 v208, v236, v236, s56\n"
  "v_pk_fma_f16 v209, v237, v237, s56\n"
  "v_pk_fma_f16 v210, v238, v238, s56\n"
  "v_pk_fma_f16 v211, v239, v239, s56\n"
  "v_pk_mul_f16 v208, v236, v208\n"
  "v_pk_mul_f16 v209, v237, v209\n"
  "v_pk_mul_f16 v210, v238, v210\n"
  "v_pk_mul_f16 v211, v239, v211\n"
  "v_mfma_f32_4x4x4_16b_f16 v[128:131], v[240:241], v[240:241], v[128:131]\n"
  "v_exp_f16 v212, v208\n"
  "v_exp_f16 v213, v209\n"
  "v_mfma_f32_4x4x4_16b_f16 v[128:131], v[242:243], v[242:243], v[128:131]\n"
  "ds_read_b128 v[220:223], v0 offset:64512\n"
  "ds_read_u16 v250, v250\n"
  "ds_read_u16_d16_hi v251, v251\n"
  "v_exp_f16 v214, v210\n"
  "v_mfma_f32_32x32x16_bf16 v[168:183], v[8:11], v[224:227], 0\n"
  "s_waitcnt lgkmcnt(7)\n"
  "v_exp_f16 v215, v211\n"
  "v_exp_f16_sdwa v212, v208 dst_sel:WORD_1 dst_unused:UNUSED_PRESERVE src0_sel:WORD_1\n"
  "v_exp_f16_sdwa v213, v209 dst_sel:WORD_1 dst_unused:UNUSED_PRESERVE src0_sel:WORD_1\n"
  "v_mfma_f32_4x4x4_16b_f16 v[128:131], v[244:245], v[244:245], v[128:131]\n"
  "v_exp_f16_sdwa v214, v210 dst_sel:WORD_1 dst_unused:UNUSED_PRESERVE src0_sel:WORD_1\n"
  "v_exp_f16_sdwa v215, v211 dst_sel:WORD_1 dst_unused:UNUSED_PRESERVE src0_sel:WORD_1\n"
  "v_mfma_f32_4x4x4_16b_f16 v[128:131], v[246:247], v[246:247], v[128:131]\n"
  "ds_read_b128 v[224:227], v1 offset:6144\n"
  "ds_read_u16 v252, v252\n"
  "v_pk_add_f16 v212, v212, 1.0 op_sel_hi:[1,0]\n"
  "v_mfma_f32_32x32x16_bf16 v[168:183], v[12:15], v[228:231], v[168:183]\n"
  "s_waitcnt lgkmcnt(7)\n"
  "v_pk_add_f16 v213, v213, 1.0 op_sel_hi:[1,0]\n"
  "v_pk_add_f16 v214, v214, 1.0 op_sel_hi:[1,0]\n"
  "v_cndmask_b32 v128, v128, v129, s[86:87]\n"
  "v_pk_add_f16 v215, v215, 1.0 op_sel_hi:[1,0]\n"
  "v_cndmask_b32 v128, v128, v130, s[88:89]\n"
  "v_rcp_f16 v208, v212\n"
  "v_cndmask_b32 v128, v128, v131, s[90:91]\n"
  "v_rcp_f16 v209, v213\n"
  "v_mul_f32 v128, s97, v128\n"
  "ds_read_b128 v[228:231], v1 offset:7168\n"
  "ds_read_u16_d16_hi v253, v253\n"
  "v_rcp_f16 v210, v214\n"
  "v_mfma_f32_32x32x16_bf16 v[168:183], v[16:19], v[216:219], v[168:183]\n"
  "s_waitcnt lgkmcnt(6)\n"
  "v_rcp_f16 v211, v215\n"
  "v_min_f32 v128, 0x54ffffff, v128\n"
  "v_rcp_f16_sdwa v208, v212 dst_sel:WORD_1 dst_unused:UNUSED_PRESERVE src0_sel:WORD_1\n"
  "v_mul_f32 v129, s84, v128\n"
  "v_rcp_f16_sdwa v209, v213 dst_sel:WORD_1 dst_unused:UNUSED_PRESERVE src0_sel:WORD_1\n"
  "v_floor_f32 v129, v129\n"
  "v_rcp_f16_sdwa v210, v214 dst_sel:WORD_1 dst_unused:UNUSED_PRESERVE src0_sel:WORD_1\n"
  "ds_read_b128 v[216:219], v1 offset:8192\n"
  "ds_read_u16 v254, v254\n"
  "v_cvt_u32_f32 v131, v129\n"
  "v_mfma_f32_32x32x16_bf16 v[168:183], v[20:23], v[220:223], v[168:183]\n"
  "s_waitcnt lgkmcnt(5)\n"
  "v_rcp_f16_sdwa v211, v215 dst_sel:WORD_1 dst_unused:UNUSED_PRESERVE src0_sel:WORD_1\n"
  "v_fma_f32 v128, -v129, s85, v128\n"
  "v_pk_mul_f16 v236, v236, v208\n"
  "v_cvt_u32_f32 v130, v128\n"
  "v_pk_mul_f16 v237, v237, v209\n"
  "v_pk_mul_f16 v238, v238, v210\n"
  "v_pk_mul_f16 v239, v239, v211\n"
  "ds_read_b128 v[220:223], v1 offset:9216\n"
  "ds_read_u16_d16_hi v255, v255\n"
  "ds_add_u64 v6, v[130:131] offset:1536\n"
  "v_or_b32 v128, v240, v241\n"
  "v_or_b32 v129, v242, v243\n"
  "v_or_b32 v130, v244, v245\n"
  "v_or_b32 v131, v246, v247\n"
  "v_mfma_f32_32x32x16_bf16 v[168:183], v[24:27], v[224:227], v[168:183]\n"
  "s_waitcnt lgkmcnt(6)\n"
  "s_setprio 1\n"
  "s_bitcmp1_b32 s72, 2\n"
  "s_cbranch_scc0 .Lpr23%=\n"
  "s_setprio 1\n"
  ".Lpr23%=:\n"
  "v_lshlrev_b32_sdwa v240, 1, v160 dst_sel:DWORD dst_unused:UNUSED_PAD src0_sel:DWORD src1_sel:WORD_1\n"
  "v_cvt_pk_f16_f32 v2, v152, v153\n"
  "v_lshlrev_b32_sdwa v241, 1, v161 dst_sel:DWORD dst_unused:UNUSED_PAD src0_sel:DWORD src1_sel:WORD_1\n"
  "v_cvt_pk_f16_f32 v3, v154, v155\n"
  "v_lshlrev_b32_sdwa v242, 1, v162 dst_sel:DWORD dst_unused:UNUSED_PAD src0_sel:DWORD src1_sel:WORD_1\n"
  "v_cvt_pk_f16_f32 v4, v156, v157\n"
  "v_lshlrev_b32_sdwa v243, 1, v163 dst_sel:DWORD dst_unused:UNUSED_PAD src0_sel:DWORD src1_sel:WORD_1\n"
  "v_cvt_pk_f16_f32 v5, v158, v159\n"
  "v_lshlrev_b32_sdwa v244, 1, v164 dst_sel:DWORD dst_unused:UNUSED_PAD src0_sel:DWORD src1_sel:WORD_1\n"
  "v_mfma_f32_4x4x4_16b_f16 v[132:135], v[236:237], v[236:237], v[132:135]\n"
  "v_lshlrev_b32_sdwa v245, 1, v165 dst_sel:DWORD dst_unused:UNUSED_PAD src0_sel:DWORD src1_sel:WORD_1\n"
  "v_lshlrev_b32_sdwa v246, 1, v166 dst_sel:DWORD dst_unused:UNUSED_PAD src0_sel:DWORD src1_sel:WORD_1\n"
  "v_mfma_f32_4x4x4_16b_f16 v[132:135], v[238:239], v[238:239], v[132:135]\n"
  "ds_read_b128 v[224:227], v1 offset:10240\n"
  "ds_read_u16 v240, v240\n"
  "ds_read_u16_d16_hi v241, v241\n"
  "v_lshlrev_b32_sdwa v247, 1, v167 dst_sel:DWORD dst_unused:UNUSED_PAD src0_sel:DWORD src1_sel:WORD_1\n"
  "v_mfma_f32_32x32x16_bf16 v[168:183], v[28:31], v[228:231], v[168:183]\n"
  "s_waitcnt lgkmcnt(7)\n"
  "v_pk_fma_f16 v208, v2, v2, s56\n"
  "v_pk_fma_f16 v209, v3, v3, s56\n"
  "v_pk_fma_f16 v210, v4, v4, s56\n"
  "v_pk_fma_f16 v211, v5, v5, s56\n"
  "v_pk_mul_f16 v208, v2, v208\n"
  "v_pk_mul_f16 v209, v3, v209\n"
  "v_pk_mul_f16 v210, v4, v210\n"
  "v_pk_mul_f16 v211, v5, v211\n"
  "v_mfma_f32_4x4x4_16b_f16 v[132:135], v[248:249], v[248:249], v[132:135]\n"
  "v_exp_f16 v212, v208\n"
  "v_exp_f16 v213, v209\n"
  "v_mfma_f32_4x4x4_16b_f16 v[132:135], v[250:251], v[250:251], v[132:135]\n"
  "ds_read_b128 v[228:231], v1 offset:11264\n"
  "ds_read_u16 v242, v242\n"
  "ds_read_u16_d16_hi v243, v243\n"
  "v_exp_f16 v214, v210\n"
  "v_mfma_f32_32x32x16_bf16 v[152:167], v[8:11], v[216:219], 0\n"
  "s_waitcnt lgkmcnt(7)\n"
  "v_exp_f16 v215, v211\n"
  "v_exp_f16_sdwa v212, v208 dst_sel:WORD_1 dst_unused:UNUSED_PRESERVE src0_sel:WORD_1\n"
  "v_exp_f16_sdwa v213, v209 dst_sel:WORD_1 dst_unused:UNUSED_PRESERVE src0_sel:WORD_1\n"
  "v_mfma_f32_4x4x4_16b_f16 v[132:135], v[252:253], v[252:253], v[132:135]\n"
  "v_exp_f16_sdwa v214, v210 dst_sel:WORD_1 dst_unused:UNUSED_PRESERVE src0_sel:WORD_1\n"
  "v_exp_f16_sdwa v215, v211 dst_sel:WORD_1 dst_unused:UNUSED_PRESERVE src0_sel:WORD_1\n"
  "v_mfma_f32_4x4x4_16b_f16 v[132:135], v[254:255], v[254:255], v[132:135]\n"
  "ds_read_b128 v[216:219], v1 offset:12288\n"
  "ds_read_u16 v244, v244\n"
  "v_pk_add_f16 v212, v212, 1.0 op_sel_hi:[1,0]\n"
  "v_mfma_f32_32x32x16_bf16 v[152:167], v[12:15], v[220:223], v[152:167]\n"
  "s_waitcnt lgkmcnt(7)\n"
  "v_pk_add_f16 v213, v213, 1.0 op_sel_hi:[1,0]\n"
  "v_pk_add_f16 v214, v214, 1.0 op_sel_hi:[1,0]\n"
  "v_cndmask_b32 v132, v132, v133, s[86:87]\n"
  "v_pk_add_f16 v215, v215, 1.0 op_sel_hi:[1,0]\n"
  "v_cndmask_b32 v132, v132, v134, s[88:89]\n"
  "v_rcp_f16 v208, v212\n"
  "v_cndmask_b32 v132, v132, v135, s[90:91]\n"
  "v_rcp_f16 v209, v213\n"
  "v_mul_f32 v132, s97, v132\n"
  "ds_read_b128 v[220:223], v1 offset:13312\n"
  "ds_read_u16_d16_hi v245, v245\n"
  "v_rcp_f16 v210, v214\n"
  "v_mfma_f32_32x32x16_bf16 v[152:167], v[16:19], v[224:227], v[152:167]\n"
  "s_waitcnt lgkmcnt(6)\n"
  "v_rcp_f16 v211, v215\n"
  "v_min_f32 v132, 0x54ffffff, v132\n"
  "v_rcp_f16_sdwa v208, v212 dst_sel:WORD_1 dst_unused:UNUSED_PRESERVE src0_sel:WORD_1\n"
  "v_mul_f32 v133, s84, v132\n"
  "v_rcp_f16_sdwa v209, v213 dst_sel:WORD_1 dst_unused:UNUSED_PRESERVE src0_sel:WORD_1\n"
  "v_floor_f32 v133, v133\n"
  "v_rcp_f16_sdwa v210, v214 dst_sel:WORD_1 dst_unused:UNUSED_PRESERVE src0_sel:WORD_1\n"
  "ds_read_b128 v[224:227], v1 offset:14336\n"
  "ds_read_u16 v246, v246\n"
  "v_cvt_u32_f32 v135, v133\n"
  "v_mfma_f32_32x32x16_bf16 v[152:167], v[20:23], v[228:231], v[152:167]\n"
  "s_waitcnt lgkmcnt(5)\n"
  "v_rcp_f16_sdwa v211, v215 dst_sel:WORD_1 dst_unused:UNUSED_PRESERVE src0_sel:WORD_1\n"
  "v_fma_f32 v132, -v133, s85, v132\n"
  "v_pk_mul_f16 v2, v2, v208\n"
  "v_cvt_u32_f32 v134, v132\n"
  "v_pk_mul_f16 v3, v3, v209\n"
  "v_pk_mul_f16 v4, v4, v210\n"
  "v_pk_mul_f16 v5, v5, v211\n"
  "ds_read_b128 v[228:231], v1 offset:15360\n"
  "ds_read_u16_d16_hi v247, v247\n"
  "ds_add_u64 v6, v[134:135] offset:1792\n"
  "v_or_b32 v132, v248, v249\n"
  "v_or_b32 v133, v250, v251\n"
  "v_or_b32 v134, v252, v253\n"
  "v_or_b32 v135, v254, v255\n"
  "v_mfma_f32_32x32x16_bf16 v[152:167], v[24:27], v[216:219], v[152:167]\n"
  "s_waitcnt lgkmcnt(6)\n"
  "v_lshlrev_b32_sdwa v176, 1, v176 dst_sel:DWORD dst_unused:UNUSED_PAD src0_sel:DWORD src1_sel:WORD_1\n"
  "v_cvt_pk_f16_f32 v168, v168, v169\n"
  "v_lshlrev_b32_sdwa v177, 1, v177 dst_sel:DWORD dst_unused:UNUSED_PAD src0_sel:DWORD src1_sel:WORD_1\n"
  "v_cvt_pk_f16_f32 v169, v170, v171\n"
  "v_lshlrev_b32_sdwa v178, 1, v178 dst_sel:DWORD dst_unused:UNUSED_PAD src0_sel:DWORD src1_sel:WORD_1\n"
  "v_cvt_pk_f16_f32 v170, v172, v173\n"
  "v_lshlrev_b32_sdwa v179, 1, v179 dst_sel:DWORD dst_unused:UNUSED_PAD src0_sel:DWORD src1_sel:WORD_1\n"
  "v_cvt_pk_f16_f32 v171, v174, v175\n"
  "v_lshlrev_b32_sdwa v180, 1, v180 dst_sel:DWORD dst_unused:UNUSED_PAD src0_sel:DWORD src1_sel:WORD_1\n"
  "v_mfma_f32_4x4x4_16b_f16 v[136:139], v[2:3], v[2:3], v[136:139]\n"
  "v_lshlrev_b32_sdwa v181, 1, v181 dst_sel:DWORD dst_unused:UNUSED_PAD src0_sel:DWORD src1_sel:WORD_1\n"
  "v_lshlrev_b32_sdwa v182, 1, v182 dst_sel:DWORD dst_unused:UNUSED_PAD src0_sel:DWORD src1_sel:WORD_1\n"
  "v_mfma_f32_4x4x4_16b_f16 v[136:139], v[4:5], v[4:5], v[136:139]\n"
  "ds_read_b128 v[216:219], v1 offset:16384\n"
  "ds_read_u16 v176, v176\n"
  "ds_read_u16_d16_hi v177, v177\n"
  "v_lshlrev_b32_sdwa v183, 1, v183 dst_sel:DWORD dst_unused:UNUSED_PAD src0_sel:DWORD src1_sel:WORD_1\n"
  "v_mfma_f32_32x32x16_bf16 v[152:167], v[28:31], v[220:223], v[152:167]\n"
  "s_waitcnt lgkmcnt(4)\n"
  "v_pk_fma_f16 v208, v168, v168, s56\n"
  "v_pk_fma_f16 v209, v169, v169, s56\n"
  "v_pk_fma_f16 v210, v170, v170, s56\n"
  "v_pk_fma_f16 v211, v171, v171, s56\n"
  "v_pk_mul_f16 v208, v168, v208\n"
  "v_pk_mul_f16 v209, v169, v209\n"
  "v_pk_mul_f16 v210, v170, v210\n"
  "v_pk_mul_f16 v211, v171, v211\n"
  "v_mfma_f32_4x4x4_16b_f16 v[136:139], v[240:241], v[240:241], v[136:139]\n"
  "v_exp_f16 v212, v208\n"
  "v_exp_f16 v213, v209\n"
  "v_mfma_f32_4x4x4_16b_f16 v[136:139], v[242:243], v[242:243], v[136:139]\n"
  "ds_read_b128 v[220:223], v1 offset:17408\n"
  "ds_read_u16 v178, v178\n"
  "ds_read_u16_d16_hi v179, v179\n"
  "v_exp_f16 v214, v210\n"
  "v_exp_f16 v215, v211\n"
  "v_exp_f16_sdwa v212, v208 dst_sel:WORD_1 dst_unused:UNUSED_PRESERVE src0_sel:WORD_1\n"
  "v_exp_f16_sdwa v213, v209 dst_sel:WORD_1 dst_unused:UNUSED_PRESERVE src0_sel:WORD_1\n"
  "v_mfma_f32_4x4x4_16b_f16 v[136:139], v[244:245], v[244:245], v[136:139]\n"
  "v_exp_f16_sdwa v214, v210 dst_sel:WORD_1 dst_unused:UNUSED_PRESERVE src0_sel:WORD_1\n"
  "v_exp_f16_sdwa v215, v211 dst_sel:WORD_1 dst_unused:UNUSED_PRESERVE src0_sel:WORD_1\n"
  "v_mfma_f32_4x4x4_16b_f16 v[136:139], v[246:247], v[246:247], v[136:139]\n"
  "ds_read_u16 v180, v180\n"
  "v_pk_add_f16 v212, v212, 1.0 op_sel_hi:[1,0]\n"
  "v_pk_add_f16 v213, v213, 1.0 op_sel_hi:[1,0]\n"
  "v_pk_add_f16 v214, v214, 1.0 op_sel_hi:[1,0]\n"
  "s_nop 0\n"
  "v_cndmask_b32 v136, v136, v137, s[86:87]\n"
  "v_pk_add_f16 v215, v215, 1.0 op_sel_hi:[1,0]\n"
  "v_cndmask_b32 v136, v136, v138, s[88:89]\n"
  "v_rcp_f16 v208, v212\n"
  "v_cndmask_b32 v136, v136, v139, s[90:91]\n"
  "v_rcp_f16 v209, v213\n"
  "v_mul_f32 v136, s97, v136\n"
  "ds_read_u16_d16_hi v181, v181\n"
  "v_rcp_f16 v210, v214\n"
  "v_rcp_f16 v211, v215\n"
  "v_min_f32 v136, 0x54ffffff, v136\n"
  "v_rcp_f16_sdwa v208, v212 dst_sel:WORD_1 dst_unused:UNUSED_PRESERVE src0_sel:WORD_1\n"
  "v_mul_f32 v137, s84, v136\n"
  "v_rcp_f16_sdwa v209, v213 dst_sel:WORD_1 dst_unused:UNUSED_PRESERVE src0_sel:WORD_1\n"
  "v_floor_f32 v137, v137\n"
  "v_rcp_f16_sdwa v210, v214 dst_sel:WORD_1 dst_unused:UNUSED_PRESERVE src0_sel:WORD_1\n"
  "ds_read_u16 v182, v182\n"
  "v_cvt_u32_f32 v139, v137\n"
  "v_rcp_f16_sdwa v211, v215 dst_sel:WORD_1 dst_unused:UNUSED_PRESERVE src0_sel:WORD_1\n"
  "v_fma_f32 v136, -v137, s85, v136\n"
  "v_pk_mul_f16 v168, v168, v208\n"
  "v_cvt_u32_f32 v138, v136\n"
  "v_pk_mul_f16 v169, v169, v209\n"
  "v_pk_mul_f16 v170, v170, v210\n"
  "v_pk_mul_f16 v171, v171, v211\n"
  "ds_read_u16_d16_hi v183, v183\n"
  "ds_add_u64 v6, v[138:139] offset:2048\n"
  "v_or_b32 v136, v240, v241\n"
  "v_or_b32 v137, v242, v243\n"
  "v_or_b32 v138, v244, v245\n"
  "v_or_b32 v139, v246, v247\n"
  "v_mfma_f32_32x32x16_bf16 v[240:255], v[8:11], v[224:227], 0\n"
  "v_lshlrev_b32_sdwa v160, 1, v160 dst_sel:DWORD dst_unused:UNUSED_PAD src0_sel:DWORD src1_sel:WORD_1\n"
  "v_cvt_pk_f16_f32 v152, v152, v153\n"
  "v_lshlrev_b32_sdwa v161, 1, v161 dst_sel:DWORD dst_unused:UNUSED_PAD src0_sel:DWORD src1_sel:WORD_1\n"
  "v_cvt_pk_f16_f32 v153, v154, v155\n"
  "v_lshlrev_b32_sdwa v162, 1, v162 dst_sel:DWORD dst_unused:UNUSED_PAD src0_sel:DWORD src1_sel:WORD_1\n"
  "v_cvt_pk_f16_f32 v154, v156, v157\n"
  "v_lshlrev_b32_sdwa v163, 1, v163 dst_sel:DWORD dst_unused:UNUSED_PAD src0_sel:DWORD src1_sel:WORD_1\n"
  "v_cvt_pk_f16_f32 v155, v158, v159\n"
  "v_lshlrev_b32_sdwa v164, 1, v164 dst_sel:DWORD dst_unused:UNUSED_PAD src0_sel:DWORD src1_sel:WORD_1\n"
  "v_mfma_f32_4x4x4_16b_f16 v[140:143], v[168:169], v[168:169], v[140:143]\n"
  "v_lshlrev_b32_sdwa v165, 1, v165 dst_sel:DWORD dst_unused:UNUSED_PAD src0_sel:DWORD src1_sel:WORD_1\n"
  "v_lshlrev_b32_sdwa v166, 1, v166 dst_sel:DWORD dst_unused:UNUSED_PAD src0_sel:DWORD src1_sel:WORD_1\n"
  "v_mfma_f32_4x4x4_16b_f16 v[140:143], v[170:171], v[170:171], v[140:143]\n"
  "ds_read_b128 v[224:227], v1 offset:18432\n"
  "ds_read_u16 v160, v160\n"
  "ds_read_u16_d16_hi v161, v161\n"
  "v_lshlrev_b32_sdwa v167, 1, v167 dst_sel:DWORD dst_unused:UNUSED_PAD src0_sel:DWORD src1_sel:WORD_1\n"
  "v_mfma_f32_32x32x16_bf16 v[240:255], v[12:15], v[228:231], v[240:255]\n"
  "s_waitcnt lgkmcnt(8)\n"
  "v_pk_fma_f16 v208, v152, v152, s56\n"
  "v_pk_fma_f16 v209, v153, v153, s56\n"
  "v_pk_fma_f16 v210, v154, v154, s56\n"
  "v_pk_fma_f16 v211, v155, v155, s56\n"
  "v_pk_mul_f16 v208, v152, v208\n"
  "v_pk_mul_f16 v209, v153, v209\n"
  "v_pk_mul_f16 v210, v154, v210\n"
  "v_pk_mul_f16 v211, v155, v211\n"
  "v_mfma_f32_4x4x4_16b_f16 v[140:143], v[176:177], v[176:177], v[140:143]\n"
  "v_exp_f16 v212, v208\n"
  "v_exp_f16 v213, v209\n"
  "v_mfma_f32_4x4x4_16b_f16 v[140:143], v[178:179], v[178:179], v[140:143]\n"
  "ds_read_b128 v[228:231], v1 offset:19456\n"
  "ds_read_u16 v162, v162\n"
  "ds_read_u16_d16_hi v163, v163\n"
  "v_exp_f16 v214, v210\n"
  "v_mfma_f32_32x32x16_bf16 v[240:255], v[16:19], v[216:219], v[240:255]\n"
  "s_waitcnt lgkmcnt(7)\n"
  "v_exp_f16 v215, v211\n"
  "v_exp_f16_sdwa v212, v208 dst_sel:WORD_1 dst_unused:UNUSED_PRESERVE src0_sel:WORD_1\n"
  "v_exp_f16_sdwa v213, v209 dst_sel:WORD_1 dst_unused:UNUSED_PRESERVE src0_sel:WORD_1\n"
  "v_mfma_f32_4x4x4_16b_f16 v[140:143], v[180:181], v[180:181], v[140:143]\n"
  "v_exp_f16_sdwa v214, v210 dst_sel:WORD_1 dst_unused:UNUSED_PRESERVE src0_sel:WORD_1\n"
  "v_exp_f16_sdwa v215, v211 dst_sel:WORD_1 dst_unused:UNUSED_PRESERVE src0_sel:WORD_1\n"
  "v_mfma_f32_4x4x4_16b_f16 v[140:143], v[182:183], v[182:183], v[140:143]\n"
  "ds_read_u16 v164, v164\n"
  "v_pk_add_f16 v212, v212, 1.0 op_sel_hi:[1,0]\n"
  "v_mfma_f32_32x32x16_bf16 v[240:255], v[20:23], v[220:223], v[240:255]\n"
  "s_waitcnt lgkmcnt(6)\n"
  "v_pk_add_f16 v213, v213, 1.0 op_sel_hi:[1,0]\n"
  "v_pk_add_f16 v214, v214, 1.0 op_sel_hi:[1,0]\n"
  "v_cndmask_b32 v140, v140, v141, s[86:87]\n"
  "v_pk_add_f16 v215, v215, 1.0 op_sel_hi:[1,0]\n"
  "v_cndmask_b32 v140, v140, v142, s[88:89]\n"
  "v_rcp_f16 v208, v212\n"
  "v_cndmask_b32 v140, v140, v143, s[90:91]\n"
  "v_rcp_f16 v209, v213\n"
  "v_mul_f32 v140, s97, v140\n"
  "ds_read_u16_d16_hi v165, v165\n"
  "v_rcp_f16 v210, v214\n"
  "v_mfma_f32_32x32x16_bf16 v[240:255], v[24:27], v[224:227], v[240:255]\n"
  "s_waitcnt lgkmcnt(4)\n"
  "v_rcp_f16 v211, v215\n"
  "v_min_f32 v140, 0x54ffffff, v140\n"
  "v_rcp_f16_sdwa v208, v212 dst_sel:WORD_1 dst_unused:UNUSED_PRESERVE src0_sel:WORD_1\n"
  "v_mul_f32 v141, s84, v140\n"
  "v_rcp_f16_sdwa v209, v213 dst_sel:WORD_1 dst_unused:UNUSED_PRESERVE src0_sel:WORD_1\n"
  "v_floor_f32 v141, v141\n"
  "v_rcp_f16_sdwa v210, v214 dst_sel:WORD_1 dst_unused:UNUSED_PRESERVE src0_sel:WORD_1\n"
  "ds_read_u16 v166, v166\n"
  "v_cvt_u32_f32 v143, v141\n"
  "v_mfma_f32_32x32x16_bf16 v[240:255], v[28:31], v[228:231], v[240:255]\n"
  "s_waitcnt lgkmcnt(0)\n"
  "v_rcp_f16_sdwa v211, v215 dst_sel:WORD_1 dst_unused:UNUSED_PRESERVE src0_sel:WORD_1\n"
  "v_fma_f32 v140, -v141, s85, v140\n"
  "v_pk_mul_f16 v152, v152, v208\n"
  "v_cvt_u32_f32 v142, v140\n"
  "v_pk_mul_f16 v153, v153, v209\n"
  "v_pk_mul_f16 v154, v154, v210\n"
  "v_pk_mul_f16 v155, v155, v211\n"
  "ds_read_u16_d16_hi v167, v167\n"
  "ds_add_u64 v6, v[142:143] offset:2304\n"
  "v_or_b32 v176, v176, v177\n"
  "v_or_b32 v177, v178, v179\n"
  "v_or_b32 v178, v180, v181\n"
  "v_or_b32 v179, v182, v183\n"
  "s_cmp_lg_u32 s95, 0\n"
  "s_cbranch_scc1 .Lexit%=\n"
  "s_cmp_ge_u32 s72, 4\n"
  "s_cbranch_scc1 .La1%=\n"
  "s_setprio 0\n"
  "s_mov_b32 s94, 28\n"
  "s_mov_b32 s96, 1048576\n"
  ".Lw24%=:\n"
  "v_mov_b32 v229, 160144\n"
  "ds_read_b32 v229, v229\n"
  "s_waitcnt lgkmcnt(0)\n"
  "v_readfirstlane_b32 s92, v229\n"
  "s_nop 1\n"
  "s_cmp_ge_u32 s92, s94\n"
  "s_cbranch_scc1 .Lwd24%=\n"
  "s_sleep 1\n"
  "s_sub_u32 s96, s96, 1\n"
  "s_cmp_lg_u32 s96, 0\n"
  "s_cbranch_scc1 .Lw24%=\n"
  ".Lwd24%=:\n"
  "s_mov_b64 s[76:77], s[14:15]\n"
  "s_mov_b32 s78, 73728\n"
  "s_mov_b32 s79, 0x20000\n"
  "s_mul_i32 s62, s73, 18432\n"
  "s_add_u32 s62, s62, 0\n"
  "s_add_u32 m0, s62, 86016\n"
  "s_nop 0\n"
  "buffer_load_dwordx4 v0, s[76:79], s62 offen offset:0 lds\n"
  "buffer_load_dwordx4 v0, s[76:79], s62 offen offset:1024 lds\n"
  "buffer_load_dwordx4 v0, s[76:79], s62 offen offset:2048 lds\n"
  "buffer_load_dwordx4 v0, s[76:79], s62 offen offset:3072 lds\n"
  "s_add_u32 s62, s62, 4096\n"
  "s_add_u32 m0, s62, 86016\n"
  "s_nop 0\n"
  "buffer_load_dwordx4 v0, s[76:79], s62 offen offset:0 lds\n"
  "buffer_load_dwordx4 v0, s[76:79], s62 offen offset:1024 lds\n"
  "buffer_load_dwordx4 v0, s[76:79], s62 offen offset:2048 lds\n"
  "buffer_load_dwordx4 v0, s[76:79], s62 offen offset:3072 lds\n"
  "s_add_u32 s62, s62, 4096\n"
  "s_add_u32 m0, s62, 86016\n"
  "s_nop 0\n"
  "buffer_load_dwordx4 v0, s[76:79], s62 offen offset:0 lds\n"
  "buffer_load_dwordx4 v0, s[76:79], s62 offen offset:1024 lds\n"
  "buffer_load_dwordx4 v0, s[76:79], s62 offen offset:2048 lds\n"
  "buffer_load_dwordx4 v0, s[76:79], s62 offen offset:3072 lds\n"
  "s_add_u32 s62, s62, 4096\n"
  "s_add_u32 m0, s62, 86016\n"
  "s_nop 0\n"
  "buffer_load_dwordx4 v0, s[76:79], s62 offen offset:0 lds\n"
  "buffer_load_dwordx4 v0, s[76:79], s62 offen offset:1024 lds\n"
  "buffer_load_dwordx4 v0, s[76:79], s62 offen offset:2048 lds\n"
  "buffer_load_dwordx4 v0, s[76:79], s62 offen offset:3072 lds\n"
  "s_add_u32 s62, s62, 4096\n"
  "s_add_u32 m0, s62, 86016\n"
  "s_nop 0\n"
  "buffer_load_dwordx4 v0, s[76:79], s62 offen offset:0 lds\n"
  "buffer_load_dwordx4 v0, s[76:79], s62 offen offset:1024 lds\n"
  "s_add_u32 s62, s62, 4096\n"
  "s_mov_b64 s[76:77], s[18:19]\n"
  "s_mov_b32 s78, 768\n"
  "s_mov_b32 s79, 0x20000\n"
  "v_lshrrev_b32 v228, 8, v0\n"
  "v_lshlrev_b32 v228, 3, v228\n"
  "s_mul_i32 s94, s73, 192\n"
  "buffer_load_dwordx2 v[216:217], v228, s[76:79], s94 offen offset:0\n"
  "buffer_load_dwordx2 v[218:219], v228, s[76:79], s94 offen offset:32\n"
  "buffer_load_dwordx2 v[220:221], v228, s[76:79], s94 offen offset:64\n"
  "buffer_load_dwordx2 v[222:223], v228, s[76:79], s94 offen offset:96\n"
  "buffer_load_dwordx2 v[224:225], v228, s[76:79], s94 offen offset:128\n"
  "buffer_load_dwordx2 v[226:227], v228, s[76:79], s94 offen offset:160\n"
  "s_branch .La2%=\n"
  ".La1%=:\n"
  "s_setprio 3\n"
  "s_cmp_lg_u32 s72, 4\n"
  "s_cbranch_scc1 .La2%=\n"
  "s_mov_b64 s[76:77], s[16:17]\n"
  "s_mov_b32 s78, 768\n"
  "s_mov_b32 s79, 0x20000\n"
  "v_lshrrev_b32 v229, 2, v0\n"
  "buffer_load_dword v220, v229, s[76:79], 0 offen offset:0\n"
  "buffer_load_dword v221, v229, s[76:79], 0 offen offset:256\n"
  "buffer_load_dword v222, v229, s[76:79], 0 offen offset:512\n"
  ".La2%=:\n"
  "v_lshlrev_b32_sdwa v248, 1, v248 dst_sel:DWORD dst_unused:UNUSED_PAD src0_sel:DWORD src1_sel:WORD_1\n"
  "v_cvt_pk_f16_f32 v240, v240, v241\n"
  "v_lshlrev_b32_sdwa v249, 1, v249 dst_sel:DWORD dst_unused:UNUSED_PAD src0_sel:DWORD src1_sel:WORD_1\n"
  "v_cvt_pk_f16_f32 v241, v242, v243\n"
  "v_lshlrev_b32_sdwa v250, 1, v250 dst_sel:DWORD dst_unused:UNUSED_PAD src0_sel:DWORD src1_sel:WORD_1\n"
  "v_cvt_pk_f16_f32 v242, v244, v245\n"
  "v_lshlrev_b32_sdwa v251, 1, v251 dst_sel:DWORD dst_unused:UNUSED_PAD src0_sel:DWORD src1_sel:WORD_1\n"
  "v_cvt_pk_f16_f32 v243, v246, v247\n"
  "v_lshlrev_b32_sdwa v252, 1, v252 dst_sel:DWORD dst_unused:UNUSED_PAD src0_sel:DWORD src1_sel:WORD_1\n"
  "v_mfma_f32_4x4x4_16b_f16 v[144:147], v[152:153], v[152:153], v[144:147]\n"
  "v_lshlrev_b32_sdwa v253, 1, v253 dst_sel:DWORD dst_unused:UNUSED_PAD src0_sel:DWORD src1_sel:WORD_1\n"
  "v_lshlrev_b32_sdwa v254, 1, v254 dst_sel:DWORD dst_unused:UNUSED_PAD src0_sel:DWORD src1_sel:WORD_1\n"
  "v_mfma_f32_4x4x4_16b_f16 v[144:147], v[154:155], v[154:155], v[144:147]\n"
  "ds_read_u16 v248, v248\n"
  "ds_read_u16_d16_hi v249, v249\n"
  "v_lshlrev_b32_sdwa v255, 1, v255 dst_sel:DWORD dst_unused:UNUSED_PAD src0_sel:DWORD src1_sel:WORD_1\n"
  "v_pk_fma_f16 v208, v240, v240, s56\n"
  "v_pk_fma_f16 v209, v241, v241, s56\n"
  "v_pk_fma_f16 v210, v242, v242, s56\n"
  "v_pk_fma_f16 v211, v243, v243, s56\n"
  "v_pk_mul_f16 v208, v240, v208\n"
  "v_pk_mul_f16 v209, v241, v209\n"
  "v_pk_mul_f16 v210, v242, v210\n"
  "v_pk_mul_f16 v211, v243, v211\n"
  "v_mfma_f32_4x4x4_16b_f16 v[144:147], v[160:161], v[160:161], v[144:147]\n"
  "v_exp_f16 v212, v208\n"
  "v_exp_f16 v213, v209\n"
  "v_mfma_f32_4x4x4_16b_f16 v[144:147], v[162:163], v[162:163], v[144:147]\n"
  "ds_read_u16 v250, v250\n"
  "ds_read_u16_d16_hi v251, v251\n"
  "v_exp_f16 v214, v210\n"
  "v_exp_f16 v215, v211\n"
  "v_exp_f16_sdwa v212, v208 dst_sel:WORD_1 dst_unused:UNUSED_PRESERVE src0_sel:WORD_1\n"
  "v_exp_f16_sdwa v213, v209 dst_sel:WORD_1 dst_unused:UNUSED_PRESERVE src0_sel:WORD_1\n"
  "v_mfma_f32_4x4x4_16b_f16 v[144:147], v[164:165], v[164:165], v[144:147]\n"
  "v_exp_f16_sdwa v214, v210 dst_sel:WORD_1 dst_unused:UNUSED_PRESERVE src0_sel:WORD_1\n"
  "v_exp_f16_sdwa v215, v211 dst_sel:WORD_1 dst_unused:UNUSED_PRESERVE src0_sel:WORD_1\n"
  "s_waitcnt lgkmcnt(5)\n"
  "v_mfma_f32_4x4x4_16b_f16 v[144:147], v[166:167], v[166:167], v[144:147]\n"
  "ds_read_u16 v252, v252\n"
  "v_pk_add_f16 v212, v212, 1.0 op_sel_hi:[1,0]\n"
  "v_pk_add_f16 v213, v213, 1.0 op_sel_hi:[1,0]\n"
  "v_pk_add_f16 v214, v214, 1.0 op_sel_hi:[1,0]\n"
  "s_nop 0\n"
  "v_cndmask_b32 v144, v144, v145, s[86:87]\n"
  "v_pk_add_f16 v215, v215, 1.0 op_sel_hi:[1,0]\n"
  "v_cndmask_b32 v144, v144, v146, s[88:89]\n"
  "v_rcp_f16 v208, v212\n"
  "v_cndmask_b32 v144, v144, v147, s[90:91]\n"
  "v_rcp_f16 v209, v213\n"
  "v_mul_f32 v144, s97, v144\n"
  "ds_read_u16_d16_hi v253, v253\n"
  "v_rcp_f16 v210, v214\n"
  "v_rcp_f16 v211, v215\n"
  "v_min_f32 v144, 0x54ffffff, v144\n"
  "v_rcp_f16_sdwa v208, v212 dst_sel:WORD_1 dst_unused:UNUSED_PRESERVE src0_sel:WORD_1\n"
  "v_mul_f32 v145, s84, v144\n"
  "v_rcp_f16_sdwa v209, v213 dst_sel:WORD_1 dst_unused:UNUSED_PRESERVE src0_sel:WORD_1\n"
  "v_floor_f32 v145, v145\n"
  "v_rcp_f16_sdwa v210, v214 dst_sel:WORD_1 dst_unused:UNUSED_PRESERVE src0_sel:WORD_1\n"
  "ds_read_u16 v254, v254\n"
  "v_cvt_u32_f32 v147, v145\n"
  "v_rcp_f16_sdwa v211, v215 dst_sel:WORD_1 dst_unused:UNUSED_PRESERVE src0_sel:WORD_1\n"
  "v_fma_f32 v144, -v145, s85, v144\n"
  "v_pk_mul_f16 v240, v240, v208\n"
  "v_cvt_u32_f32 v146, v144\n"
  "v_pk_mul_f16 v241, v241, v209\n"
  "v_pk_mul_f16 v242, v242, v210\n"
  "v_pk_mul_f16 v243, v243, v211\n"
  "ds_read_u16_d16_hi v255, v255\n"
  "ds_add_u64 v6, v[146:147] offset:2560\n"
  "v_or_b32 v160, v160, v161\n"
  "v_or_b32 v161, v162, v163\n"
  "v_or_b32 v162, v164, v165\n"
  "v_or_b32 v163, v166, v167\n"
  "v_mfma_f32_4x4x4_16b_f16 v[148:151], v[240:241], v[240:241], v[148:151]\n"
  "s_nop 1\n"
  "v_mfma_f32_4x4x4_16b_f16 v[148:151], v[242:243], v[242:243], v[148:151]\n"
  "s_waitcnt lgkmcnt(7)\n"
  "s_nop 0\n"
  "v_mfma_f32_4x4x4_16b_f16 v[148:151], v[248:249], v[248:249], v[148:151]\n"
  "s_waitcnt lgkmcnt(5)\n"
  "s_nop 0\n"
  "v_mfma_f32_4x4x4_16b_f16 v[148:151], v[250:251], v[250:251], v[148:151]\n"
  "s_waitcnt lgkmcnt(3)\n"
  "s_nop 0\n"
  "v_mfma_f32_4x4x4_16b_f16 v[148:151], v[252:253], v[252:253], v[148:151]\n"
  "s_waitcnt lgkmcnt(1)\n"
  "s_nop 0\n"
  "v_mfma_f32_4x4x4_16b_f16 v[148:151], v[254:255], v[254:255], v[148:151]\n"
  "v_or_b32 v248, v248, v249\n"
  "v_or_b32 v249, v250, v251\n"
  "v_or_b32 v250, v252, v253\n"
  "v_or_b32 v251, v254, v255\n"
  "s_nop 0\n"
  "v_cndmask_b32 v148, v148, v149, s[86:87]\n"
  "v_cndmask_b32 v148, v148, v150, s[88:89]\n"
  "v_cndmask_b32 v148, v148, v151, s[90:91]\n"
  "v_mul_f32 v148, s97, v148\n"
  "v_min_f32 v148, 0x54ffffff, v148\n"
  "v_mul_f32 v149, s84, v148\n"
  "v_floor_f32 v149, v149\n"
  "v_cvt_u32_f32 v151, v149\n"
  "v_fma_f32 v148, -v149, s85, v148\n"
  "v_cvt_u32_f32 v150, v148\n"
  "ds_add_u64 v6, v[150:151] offset:2816\n"
  "v_mov_b32 v11, 163328\n"
  "v_mov_b32 v12, 1\n"
  "s_mov_b64 exec, 1\n"
  "ds_add_rtn_u32 v13, v11, v12\n"
  "s_mov_b64 exec, -1\n"
  "v_lshrrev_b32 v14, 1, v0\n"
  "v_add_u32 v10, 160256, v14\n"
  "v_mov_b32 v23, 0\n"
  "s_waitcnt lgkmcnt(0)\n"
  "v_readfirstlane_b32 s94, v13\n"
  "s_nop 1\n"
  "s_cmp_lg_u32 s94, 7\n"
  "s_cbranch_scc1 .Lpubd%=\n"
  "s_setprio 3\n"
  "ds_read_b64 v[140:141], v10 offset:0\n"
  "ds_read_b64 v[142:143], v10 offset:512\n"
  "ds_read_b64 v[144:145], v10 offset:1024\n"
  "ds_read_b64 v[146:147], v10 offset:1536\n"
  "ds_read_b64 v[148:149], v10 offset:2048\n"
  "ds_read_b64 v[150:151], v10 offset:2560\n"
  "s_cmp_eq_u32 s50, s74\n"
  "s_cbranch_scc1 .Lpfd%=\n"
  "s_mov_b32 s96, 131072\n"
  ".Lpf%=:\n"
  "buffer_load_dword v22, v23, s[80:83], 0 offen offset:256 sc1\n"
  "s_waitcnt vmcnt(0)\n"
  "v_readfirstlane_b32 s94, v22\n"
  "s_nop 1\n"
  "s_cmp_eq_u32 s94, s74\n"
  "s_cbranch_scc1 .Lpfd%=\n"
  "s_sleep 2\n"
  "s_sub_u32 s96, s96, 1\n"
  "s_cmp_lg_u32 s96, 0\n"
  "s_cbranch_scc1 .Lpf%=\n"
  ".Lpfd%=:\n"
  "s_bfe_u32 s94, s75, 0x20003\n"
  "s_mul_i32 s94, s94, 3072\n"
  "s_add_u32 s94, s94, 8192\n"
  "s_waitcnt lgkmcnt(5)\n"
  "v_add_u32 v141, 0x800000, v141\n"
  "buffer_atomic_add_x2 v[140:141], v14, s[80:83], s94 offen offset:0\n"
  "s_waitcnt lgkmcnt(4)\n"
  "v_add_u32 v143, 0x800000, v143\n"
  "buffer_atomic_add_x2 v[142:143], v14, s[80:83], s94 offen offset:512\n"
  "s_waitcnt lgkmcnt(3)\n"
  "v_add_u32 v145, 0x800000, v145\n"
  "buffer_atomic_add_x2 v[144:145], v14, s[80:83], s94 offen offset:1024\n"
  "s_waitcnt lgkmcnt(2)\n"
  "v_add_u32 v147, 0x800000, v147\n"
  "buffer_atomic_add_x2 v[146:147], v14, s[80:83], s94 offen offset:1536\n"
  "s_waitcnt lgkmcnt(1)\n"
  "v_add_u32 v149, 0x800000, v149\n"
  "buffer_atomic_add_x2 v[148:149], v14, s[80:83], s94 offen offset:2048\n"
  "s_waitcnt lgkmcnt(0)\n"
  "v_add_u32 v151, 0x800000, v151\n"
  "buffer_atomic_add_x2 v[150:151], v14, s[80:83], s94 offen offset:2560\n"
  "s_nop 1\n"
  "s_lshr_b32 s94, s75, 4\n"
  "s_lshl_b32 s94, s94, 8\n"
  "s_mov_b64 exec, 1\n"
  "buffer_atomic_add v12, v23, s[80:83], s94 offen offset:512\n"
  "s_mov_b64 exec, -1\n"
  ".Lpubd%=:\n"
  "s_setprio 0\n"
  "v_bfe_u32 v212, v0, 4, 1\n"
  "v_lshlrev_b32 v212, 4, v212\n"
  "v_mov_b32 v213, 0x3c00\n"
  "v_lshlrev_b32 v212, v212, v213\n"
  "s_mov_b32 s86, 0x3\n"
  "s_mov_b32 s87, 0x30\n"
  "v_cndmask_b32 v24, 0, v212, s[86:87]\n"
  "s_mov_b32 s88, 0xc\n"
  "s_mov_b32 s89, 0xc0\n"
  "v_cndmask_b32 v25, 0, v212, s[88:89]\n"
  "s_mov_b32 s86, 0x300\n"
  "s_mov_b32 s87, 0x3000\n"
  "v_cndmask_b32 v26, 0, v212, s[86:87]\n"
  "s_mov_b32 s88, 0xc00\n"
  "s_mov_b32 s89, 0xc000\n"
  "v_cndmask_b32 v27, 0, v212, s[88:89]\n"
  "s_mov_b32 s86, 0x30000\n"
  "s_mov_b32 s87, 0x300000\n"
  "v_cndmask_b32 v28, 0, v212, s[86:87]\n"
  "s_mov_b32 s88, 0xc0000\n"
  "s_mov_b32 s89, 0xc00000\n"
  "v_cndmask_b32 v29, 0, v212, s[88:89]\n"
  "s_mov_b32 s86, 0x3000000\n"
  "s_mov_b32 s87, 0x30000000\n"
  "v_cndmask_b32 v30, 0, v212, s[86:87]\n"
  "s_mov_b32 s88, 0xc000000\n"
  "s_mov_b32 s89, 0xc0000000\n"
  "v_cndmask_b32 v31, 0, v212, s[88:89]\n"
  "v_mfma_f32_16x16x32_f16 v[8:11], v[184:187], v[24:27], 0\n"
  "s_nop 0\n"
  "v_mfma_f32_16x16x32_f16 v[12:15], v[184:187], v[28:31], 0\n"
  "v_mfma_f32_16x16x32_f16 v[16:19], v[104:107], v[24:27], 0\n"
  "v_mfma_f32_16x16x32_f16 v[20:23], v[104:107], v[28:31], 0\n"
  "v_mfma_f32_16x16x32_f16 v[140:143], v[188:191], v[24:27], 0\n"
  "s_nop 2\n"
  "v_cvt_pk_f16_f32 v184, v8, v9\n"
  "v_cvt_pk_f16_f32 v185, v10, v11\n"
  "v_mfma_f32_16x16x32_f16 v[144:147], v[188:191], v[28:31], 0\n"
  "v_cvt_pk_f16_f32 v186, v12, v13\n"
  "v_cvt_pk_f16_f32 v187, v14, v15\n"
  "v_mfma_f32_16x16x32_f16 v[148:151], v[108:111], v[24:27], 0\n"
  "v_cvt_pk_f16_f32 v104, v16, v17\n"
  "v_cvt_pk_f16_f32 v105, v18, v19\n"
  "v_mfma_f32_16x16x32_f16 v[208:211], v[108:111], v[28:31], 0\n"
  "v_cvt_pk_f16_f32 v106, v20, v21\n"
  "v_cvt_pk_f16_f32 v107, v22, v23\n"
  "v_mfma_f32_16x16x32_f16 v[8:11], v[192:195], v[24:27], 0\n"
  "v_cvt_pk_f16_f32 v188, v140, v141\n"
  "v_cvt_pk_f16_f32 v189, v142, v143\n"
  "v_mfma_f32_16x16x32_f16 v[12:15], v[192:195], v[28:31], 0\n"
  "v_cvt_pk_f16_f32 v190, v144, v145\n"
  "v_cvt_pk_f16_f32 v191, v146, v147\n"
  "v_mfma_f32_16x16x32_f16 v[16:19], v[112:115], v[24:27], 0\n"
  "v_cvt_pk_f16_f32 v108, v148, v149\n"
  "v_cvt_pk_f16_f32 v109, v150, v151\n"
  "v_mfma_f32_16x16x32_f16 v[20:23], v[112:115], v[28:31], 0\n"
  "v_cvt_pk_f16_f32 v110, v208, v209\n"
  "v_cvt_pk_f16_f32 v111, v210, v211\n"
  "v_mfma_f32_16x16x32_f16 v[140:143], v[196:199], v[24:27], 0\n"
  "v_cvt_pk_f16_f32 v192, v8, v9\n"
  "v_cvt_pk_f16_f32 v193, v10, v11\n"
  "v_mfma_f32_16x16x32_f16 v[144:147], v[196:199], v[28:31], 0\n"
  "v_cvt_pk_f16_f32 v194, v12, v13\n"
  "v_cvt_pk_f16_f32 v195, v14, v15\n"
  "v_mfma_f32_16x16x32_f16 v[148:151], v[116:119], v[24:27], 0\n"
  "v_cvt_pk_f16_f32 v112, v16, v17\n"
  "v_cvt_pk_f16_f32 v113, v18, v19\n"
  "v_mfma_f32_16x16x32_f16 v[208:211], v[116:119], v[28:31], 0\n"
  "v_cvt_pk_f16_f32 v114, v20, v21\n"
  "v_cvt_pk_f16_f32 v115, v22, v23\n"
  "v_mfma_f32_16x16x32_f16 v[8:11], v[200:203], v[24:27], 0\n"
  "v_cvt_pk_f16_f32 v196, v140, v141\n"
  "v_cvt_pk_f16_f32 v197, v142, v143\n"
  "v_mfma_f32_16x16x32_f16 v[12:15], v[200:203], v[28:31], 0\n"
  "v_cvt_pk_f16_f32 v198, v144, v145\n"
  "v_cvt_pk_f16_f32 v199, v146, v147\n"
  "v_mfma_f32_16x16x32_f16 v[16:19], v[120:123], v[24:27], 0\n"
  "v_cvt_pk_f16_f32 v116, v148, v149\n"
  "v_cvt_pk_f16_f32 v117, v150, v151\n"
  "v_mfma_f32_16x16x32_f16 v[20:23], v[120:123], v[28:31], 0\n"
  "v_cvt_pk_f16_f32 v118, v208, v209\n"
  "v_cvt_pk_f16_f32 v119, v210, v211\n"
  "v_mfma_f32_16x16x32_f16 v[140:143], v[204:207], v[24:27], 0\n"
  "v_cvt_pk_f16_f32 v200, v8, v9\n"
  "v_cvt_pk_f16_f32 v201, v10, v11\n"
  "v_mfma_f32_16x16x32_f16 v[144:147], v[204:207], v[28:31], 0\n"
  "v_cvt_pk_f16_f32 v202, v12, v13\n"
  "v_cvt_pk_f16_f32 v203, v14, v15\n"
  "v_mfma_f32_16x16x32_f16 v[148:151], v[124:127], v[24:27], 0\n"
  "v_cvt_pk_f16_f32 v120, v16, v17\n"
  "v_cvt_pk_f16_f32 v121, v18, v19\n"
  "v_mfma_f32_16x16x32_f16 v[208:211], v[124:127], v[28:31], 0\n"
  "v_cvt_pk_f16_f32 v122, v20, v21\n"
  "v_cvt_pk_f16_f32 v123, v22, v23\n"
  "v_mfma_f32_16x16x32_f16 v[8:11], v[232:235], v[24:27], 0\n"
  "v_cvt_pk_f16_f32 v204, v140, v141\n"
  "v_cvt_pk_f16_f32 v205, v142, v143\n"
  "v_mfma_f32_16x16x32_f16 v[12:15], v[232:235], v[28:31], 0\n"
  "v_cvt_pk_f16_f32 v206, v144, v145\n"
  "v_cvt_pk_f16_f32 v207, v146, v147\n"
  "v_mfma_f32_16x16x32_f16 v[16:19], v[128:131], v[24:27], 0\n"
  "v_cvt_pk_f16_f32 v124, v148, v149\n"
  "v_cvt_pk_f16_f32 v125, v150, v151\n"
  "v_mfma_f32_16x16x32_f16 v[20:23], v[128:131], v[28:31], 0\n"
  "v_cvt_pk_f16_f32 v126, v208, v209\n"
  "v_cvt_pk_f16_f32 v127, v210, v211\n"
  "v_mfma_f32_16x16x32_f16 v[140:143], v[236:239], v[24:27], 0\n"
  "v_cvt_pk_f16_f32 v232, v8, v9\n"
  "v_cvt_pk_f16_f32 v233, v10, v11\n"
  "v_mfma_f32_16x16x32_f16 v[144:147], v[236:239], v[28:31], 0\n"
  "v_cvt_pk_f16_f32 v234, v12, v13\n"
  "v_cvt_pk_f16_f32 v235, v14, v15\n"
  "v_mfma_f32_16x16x32_f16 v[148:151], v[132:135], v[24:27], 0\n"
  "v_cvt_pk_f16_f32 v164, v16, v17\n"
  "v_cvt_pk_f16_f32 v165, v18, v19\n"
  "v_mfma_f32_16x16x32_f16 v[208:211], v[132:135], v[28:31], 0\n"
  "v_cvt_pk_f16_f32 v166, v20, v21\n"
  "v_cvt_pk_f16_f32 v167, v22, v23\n"
  "v_mfma_f32_16x16x32_f16 v[8:11], v[2:5], v[24:27], 0\n"
  "v_cvt_pk_f16_f32 v236, v140, v141\n"
  "v_cvt_pk_f16_f32 v237, v142, v143\n"
  "v_mfma_f32_16x16x32_f16 v[12:15], v[2:5], v[28:31], 0\n"
  "v_cvt_pk_f16_f32 v238, v144, v145\n"
  "v_cvt_pk_f16_f32 v239, v146, v147\n"
  "v_mfma_f32_16x16x32_f16 v[16:19], v[136:139], v[24:27], 0\n"
  "v_cvt_pk_f16_f32 v172, v148, v149\n"
  "v_cvt_pk_f16_f32 v173, v150, v151\n"
  "v_mfma_f32_16x16x32_f16 v[20:23], v[136:139], v[28:31], 0\n"
  "v_cvt_pk_f16_f32 v174, v208, v209\n"
  "v_cvt_pk_f16_f32 v175, v210, v211\n"
  "v_mfma_f32_16x16x32_f16 v[140:143], v[168:171], v[24:27], 0\n"
  "v_cvt_pk_f16_f32 v156, v8, v9\n"
  "v_cvt_pk_f16_f32 v157, v10, v11\n"
  "v_mfma_f32_16x16x32_f16 v[144:147], v[168:171], v[28:31], 0\n"
  "v_cvt_pk_f16_f32 v158, v12, v13\n"
  "v_cvt_pk_f16_f32 v159, v14, v15\n"
  "v_mfma_f32_16x16x32_f16 v[148:151], v[176:179], v[24:27], 0\n"
  "v_cvt_pk_f16_f32 v180, v16, v17\n"
  "v_cvt_pk_f16_f32 v181, v18, v19\n"
  "v_mfma_f32_16x16x32_f16 v[208:211], v[176:179], v[28:31], 0\n"
  "v_cvt_pk_f16_f32 v182, v20, v21\n"
  "v_cvt_pk_f16_f32 v183, v22, v23\n"
  "v_mfma_f32_16x16x32_f16 v[8:11], v[152:155], v[24:27], 0\n"
  "v_cvt_pk_f16_f32 v168, v140, v141\n"
  "v_cvt_pk_f16_f32 v169, v142, v143\n"
  "v_mfma_f32_16x16x32_f16 v[12:15], v[152:155], v[28:31], 0\n"
  "v_cvt_pk_f16_f32 v170, v144, v145\n"
  "v_cvt_pk_f16_f32 v171, v146, v147\n"
  "v_mfma_f32_16x16x32_f16 v[16:19], v[160:163], v[24:27], 0\n"
  "v_cvt_pk_f16_f32 v176, v148, v149\n"
  "v_cvt_pk_f16_f32 v177, v150, v151\n"
  "v_mfma_f32_16x16x32_f16 v[20:23], v[160:163], v[28:31], 0\n"
  "v_cvt_pk_f16_f32 v178, v208, v209\n"
  "v_cvt_pk_f16_f32 v179, v210, v211\n"
  "v_mfma_f32_16x16x32_f16 v[140:143], v[240:243], v[24:27], 0\n"
  "v_cvt_pk_f16_f32 v152, v8, v9\n"
  "v_cvt_pk_f16_f32 v153, v10, v11\n"
  "v_mfma_f32_16x16x32_f16 v[144:147], v[240:243], v[28:31], 0\n"
  "v_cvt_pk_f16_f32 v154, v12, v13\n"
  "v_cvt_pk_f16_f32 v155, v14, v15\n"
  "v_mfma_f32_16x16x32_f16 v[148:151], v[248:251], v[24:27], 0\n"
  "v_cvt_pk_f16_f32 v160, v16, v17\n"
  "v_cvt_pk_f16_f32 v161, v18, v19\n"
  "v_mfma_f32_16x16x32_f16 v[208:211], v[248:251], v[28:31], 0\n"
  "v_cvt_pk_f16_f32 v162, v20, v21\n"
  "v_cvt_pk_f16_f32 v163, v22, v23\n"
  "v_cvt_pk_f16_f32 v240, v140, v141\n"
  "v_cvt_pk_f16_f32 v241, v142, v143\n"
  "v_cvt_pk_f16_f32 v242, v144, v145\n"
  "v_cvt_pk_f16_f32 v243, v146, v147\n"
  "v_cvt_pk_f16_f32 v248, v148, v149\n"
  "v_cvt_pk_f16_f32 v249, v150, v151\n"
  "v_cvt_pk_f16_f32 v250, v208, v209\n"
  "v_cvt_pk_f16_f32 v251, v210, v211\n"
  "v_lshrrev_b32 v252, 4, v0\n"
  "s_cmp_ge_u32 s72, 4\n"
  "s_cbranch_scc1 .Lnow2%=\n"
  "v_lshrrev_b32 v253, 2, v252\n"
  "v_mul_u32_u24 v253, 192, v253\n"
  "v_and_b32 v255, 3, v252\n"
  "v_lshl_add_u32 v253, v255, 4, v253\n"
  "s_mul_i32 s94, s73, 18432\n"
  "s_add_u32 s94, s94, 86016\n"
  "s_cmp_ge_u32 s72, 4\n"
  "s_cselect_b32 s92, 12288, 0\n"
  "s_add_u32 s94, s94, s92\n"
  "v_add_u32 v253, s94, v253\n"
  "v_add_u32 v254, s94, v0\n"
  "s_waitcnt vmcnt(0)\n"
  "ds_read_b64_tr_b16 v[8:9], v253 offset:0\n"
  "ds_read_b64_tr_b16 v[10:11], v253 offset:3072\n"
  "ds_read_b64_tr_b16 v[12:13], v253 offset:8\n"
  "ds_read_b64_tr_b16 v[14:15], v253 offset:3080\n"
  "ds_read_b64_tr_b16 v[16:17], v253 offset:64\n"
  "ds_read_b64_tr_b16 v[18:19], v253 offset:3136\n"
  "ds_read_b64_tr_b16 v[20:21], v253 offset:72\n"
  "ds_read_b64_tr_b16 v[22:23], v253 offset:3144\n"
  "ds_read_b64_tr_b16 v[24:25], v253 offset:128\n"
  "ds_read_b64_tr_b16 v[26:27], v253 offset:3200\n"
  "ds_read_b64_tr_b16 v[28:29], v253 offset:136\n"
  "ds_read_b64_tr_b16 v[30:31], v253 offset:3208\n"
  ".Lnow2%=:\n"
  "v_and_b32 v244, 15, v252\n"
  "v_lshrrev_b32 v245, 4, v252\n"
  "v_and_b32 v246, 1, v245\n"
  "v_lshrrev_b32 v247, 5, v252\n"
  "v_lshlrev_b32 v2, 4, v252\n"
  "v_add_u32 v3, 0x20000, v2\n"
  "v_add_u32 v2, 0x10000, v2\n"
  "v_lshlrev_b32 v0, 4, v244\n"
  "v_lshl_add_u32 v0, v246, 10, v0\n"
  "v_lshl_add_u32 v0, v247, 9, v0\n"
  "v_add_u32 v1, 0x10000, v0\n"
  "v_lshlrev_b32 v4, 5, v245\n"
  "v_add_u32 v4, 159744, v4\n"
  "v_mul_u32_u24 v5, 192, v244\n"
  "v_lshl_add_u32 v5, v245, 4, v5\n"
  "s_mov_b64 s[44:45], s[20:21]\n"
  "s_mov_b32 s46, 0x3000000\n"
  "s_mov_b32 s47, 0x20000\n"
  "s_mov_b32 s42, s69\n"
  "s_lshr_b32 s94, s72, 1\n"
  "s_lshl_b32 s94, s94, 1\n"
  "s_add_u32 s94, s94, %6\n"
  "s_add_u32 s94, s94, 24\n"
  "s_mul_i32 s52, s94, 0x18000\n"
  "s_and_b32 s94, s72, 1\n"
  "s_lshl_b32 s94, s94, 4\n"
  "s_add_u32 s94, s94, %5\n"
  "s_add_u32 s94, s94, 1\n"
  "s_mul_i32 s94, s94, 192\n"
  "s_add_u32 s52, s52, s94\n"
  "s_sub_u32 s53, s52, 0xC0000\n"
  "s_mov_b32 s55, 4\n"
  "s_nop 1\n"
  "s_add_u32 s60, s52, 0x18000\n"
  "s_add_u32 s61, s53, 0x18000\n"
  "s_sub_u32 s57, s53, 0xC0000\n"
  "s_add_u32 s58, s57, 0x18000\n"
  "s_sub_u32 s59, s57, 0xC0000\n"
  "s_add_u32 s63, s59, 0x18000\n"
  "s_mov_b32 s84, 0x7800000\n"
  "s_mov_b32 s85, s84\n"
  "s_mov_b32 s51, s84\n"
  "s_cmp_ge_u32 s72, 4\n"
  "s_cbranch_scc1 .Lnow3%=\n"
  "s_setprio 0\n"
  "s_mov_b32 s92, 0xffff0000\n"
  "s_waitcnt lgkmcnt(0)\n"
  "v_mfma_f32_16x16x32_bf16 v[128:131], v[216:219], v[8:11], 0\n"
  "v_lshlrev_b32 v208, 16, v8\n"
  "v_lshlrev_b32 v210, 16, v9\n"
  "v_lshlrev_b32 v212, 16, v10\n"
  "v_lshlrev_b32 v214, 16, v11\n"
  "v_and_b32 v209, s92, v8\n"
  "v_and_b32 v211, s92, v9\n"
  "v_and_b32 v213, s92, v10\n"
  "v_and_b32 v215, s92, v11\n"
  "v_cvt_pk_f16_f32 v8, v208, v209\n"
  "v_cvt_pk_f16_f32 v9, v210, v211\n"
  "v_cvt_pk_f16_f32 v10, v212, v213\n"
  "v_cvt_pk_f16_f32 v11, v214, v215\n"
  "v_pk_maximum3_f16 v228, v8, v8, v8 neg_lo:[0,0,1] neg_hi:[0,0,1]\n"
  "v_pk_maximum3_f16 v229, v9, v9, v9 neg_lo:[0,0,1] neg_hi:[0,0,1]\n"
  "v_pk_maximum3_f16 v230, v10, v10, v10 neg_lo:[0,0,1] neg_hi:[0,0,1]\n"
  "v_pk_maximum3_f16 v231, v11, v11, v11 neg_lo:[0,0,1] neg_hi:[0,0,1]\n"
  "ds_write_b128 v254, v[8:11] offset:0\n"
  "ds_read_b64_tr_b16 v[8:9], v253 offset:6144\n"
  "ds_read_b64_tr_b16 v[10:11], v253 offset:9216\n"
  "v_mfma_f32_16x16x32_bf16 v[132:135], v[216:219], v[12:15], 0\n"
  "v_lshlrev_b32 v208, 16, v12\n"
  "v_lshlrev_b32 v210, 16, v13\n"
  "v_lshlrev_b32 v212, 16, v14\n"
  "v_lshlrev_b32 v214, 16, v15\n"
  "v_and_b32 v209, s92, v12\n"
  "v_and_b32 v211, s92, v13\n"
  "v_and_b32 v213, s92, v14\n"
  "v_and_b32 v215, s92, v15\n"
  "v_cvt_pk_f16_f32 v12, v208, v209\n"
  "v_cvt_pk_f16_f32 v13, v210, v211\n"
  "v_cvt_pk_f16_f32 v14, v212, v213\n"
  "v_cvt_pk_f16_f32 v15, v214, v215\n"
  "v_pk_maximum3_f16 v228, v228, v12, v12 neg_lo:[0,0,1] neg_hi:[0,0,1]\n"
  "v_pk_maximum3_f16 v229, v229, v13, v13 neg_lo:[0,0,1] neg_hi:[0,0,1]\n"
  "v_pk_maximum3_f16 v230, v230, v14, v14 neg_lo:[0,0,1] neg_hi:[0,0,1]\n"
  "v_pk_maximum3_f16 v231, v231, v15, v15 neg_lo:[0,0,1] neg_hi:[0,0,1]\n"
  "ds_write_b128 v254, v[12:15] offset:1024\n"
  "ds_read_b64_tr_b16 v[12:13], v253 offset:6152\n"
  "ds_read_b64_tr_b16 v[14:15], v253 offset:9224\n"
  "v_mfma_f32_16x16x32_bf16 v[136:139], v[216:219], v[16:19], 0\n"
  "v_lshlrev_b32 v208, 16, v16\n"
  "v_lshlrev_b32 v210, 16, v17\n"
  "v_lshlrev_b32 v212, 16, v18\n"
  "v_lshlrev_b32 v214, 16, v19\n"
  "v_and_b32 v209, s92, v16\n"
  "v_and_b32 v211, s92, v17\n"
  "v_and_b32 v213, s92, v18\n"
  "v_and_b32 v215, s92, v19\n"
  "v_cvt_pk_f16_f32 v16, v208, v209\n"
  "v_cvt_pk_f16_f32 v17, v210, v211\n"
  "v_cvt_pk_f16_f32 v18, v212, v213\n"
  "v_cvt_pk_f16_f32 v19, v214, v215\n"
  "v_pk_maximum3_f16 v228, v228, v16, v16 neg_lo:[0,0,1] neg_hi:[0,0,1]\n"
  "v_pk_maximum3_f16 v229, v229, v17, v17 neg_lo:[0,0,1] neg_hi:[0,0,1]\n"
  "v_pk_maximum3_f16 v230, v230, v18, v18 neg_lo:[0,0,1] neg_hi:[0,0,1]\n"
  "v_pk_maximum3_f16 v231, v231, v19, v19 neg_lo:[0,0,1] neg_hi:[0,0,1]\n"
  "ds_write_b128 v254, v[16:19] offset:2048\n"
  "ds_read_b64_tr_b16 v[16:17], v253 offset:6208\n"
  "ds_read_b64_tr_b16 v[18:19], v253 offset:9280\n"
  "v_mfma_f32_16x16x32_bf16 v[140:143], v[216:219], v[20:23], 0\n"
  "v_lshlrev_b32 v208, 16, v20\n"
  "v_lshlrev_b32 v210, 16, v21\n"
  "v_lshlrev_b32 v212, 16, v22\n"
  "v_lshlrev_b32 v214, 16, v23\n"
  "v_and_b32 v209, s92, v20\n"
  "v_and_b32 v211, s92, v21\n"
  "v_and_b32 v213, s92, v22\n"
  "v_and_b32 v215, s92, v23\n"
  "v_cvt_pk_f16_f32 v20, v208, v209\n"
  "v_cvt_pk_f16_f32 v21, v210, v211\n"
  "v_cvt_pk_f16_f32 v22, v212, v213\n"
  "v_cvt_pk_f16_f32 v23, v214, v215\n"
  "v_pk_maximum3_f16 v228, v228, v20, v20 neg_lo:[0,0,1] neg_hi:[0,0,1]\n"
  "v_pk_maximum3_f16 v229, v229, v21, v21 neg_lo:[0,0,1] neg_hi:[0,0,1]\n"
  "v_pk_maximum3_f16 v230, v230, v22, v22 neg_lo:[0,0,1] neg_hi:[0,0,1]\n"
  "v_pk_maximum3_f16 v231, v231, v23, v23 neg_lo:[0,0,1] neg_hi:[0,0,1]\n"
  "ds_write_b128 v254, v[20:23] offset:3072\n"
  "ds_read_b64_tr_b16 v[20:21], v253 offset:6216\n"
  "ds_read_b64_tr_b16 v[22:23], v253 offset:9288\n"
  "v_mfma_f32_16x16x32_bf16 v[144:147], v[216:219], v[24:27], 0\n"
  "v_lshlrev_b32 v208, 16, v24\n"
  "v_lshlrev_b32 v210, 16, v25\n"
  "v_lshlrev_b32 v212, 16, v26\n"
  "v_lshlrev_b32 v214, 16, v27\n"
  "v_and_b32 v209, s92, v24\n"
  "v_and_b32 v211, s92, v25\n"
  "v_and_b32 v213, s92, v26\n"
  "v_and_b32 v215, s92, v27\n"
  "v_cvt_pk_f16_f32 v24, v208, v209\n"
  "v_cvt_pk_f16_f32 v25, v210, v211\n"
  "v_cvt_pk_f16_f32 v26, v212, v213\n"
  "v_cvt_pk_f16_f32 v27, v214, v215\n"
  "v_pk_maximum3_f16 v228, v228, v24, v24 neg_lo:[0,0,1] neg_hi:[0,0,1]\n"
  "v_pk_maximum3_f16 v229, v229, v25, v25 neg_lo:[0,0,1] neg_hi:[0,0,1]\n"
  "v_pk_maximum3_f16 v230, v230, v26, v26 neg_lo:[0,0,1] neg_hi:[0,0,1]\n"
  "v_pk_maximum3_f16 v231, v231, v27, v27 neg_lo:[0,0,1] neg_hi:[0,0,1]\n"
  "ds_write_b128 v254, v[24:27] offset:4096\n"
  "ds_read_b64_tr_b16 v[24:25], v253 offset:6272\n"
  "ds_read_b64_tr_b16 v[26:27], v253 offset:9344\n"
  "v_mfma_f32_16x16x32_bf16 v[148:151], v[216:219], v[28:31], 0\n"
  "v_lshlrev_b32 v208, 16, v28\n"
  "v_lshlrev_b32 v210, 16, v29\n"
  "v_lshlrev_b32 v212, 16, v30\n"
  "v_lshlrev_b32 v214, 16, v31\n"
  "v_and_b32 v209, s92, v28\n"
  "v_and_b32 v211, s92, v29\n"
  "v_and_b32 v213, s92, v30\n"
  "v_and_b32 v215, s92, v31\n"
  "v_cvt_pk_f16_f32 v28, v208, v209\n"
  "v_cvt_pk_f16_f32 v29, v210, v211\n"
  "v_cvt_pk_f16_f32 v30, v212, v213\n"
  "v_cvt_pk_f16_f32 v31, v214, v215\n"
  "v_pk_maximum3_f16 v228, v228, v28, v28 neg_lo:[0,0,1] neg_hi:[0,0,1]\n"
  "v_pk_maximum3_f16 v229, v229, v29, v29 neg_lo:[0,0,1] neg_hi:[0,0,1]\n"
  "v_pk_maximum3_f16 v230, v230, v30, v30 neg_lo:[0,0,1] neg_hi:[0,0,1]\n"
  "v_pk_maximum3_f16 v231, v231, v31, v31 neg_lo:[0,0,1] neg_hi:[0,0,1]\n"
  "ds_write_b128 v254, v[28:31] offset:5120\n"
  "ds_read_b64_tr_b16 v[28:29], v253 offset:6280\n"
  "ds_read_b64_tr_b16 v[30:31], v253 offset:9352\n"
  "s_waitcnt lgkmcnt(15)\n"
  "v_mfma_f32_16x16x32_bf16 v[128:131], v[220:223], v[8:11], v[128:131]\n"
  "v_lshlrev_b32 v208, 16, v8\n"
  "v_lshlrev_b32 v210, 16, v9\n"
  "v_lshlrev_b32 v212, 16, v10\n"
  "v_lshlrev_b32 v214, 16, v11\n"
  "v_and_b32 v209, s92, v8\n"
  "v_and_b32 v211, s92, v9\n"
  "v_and_b32 v213, s92, v10\n"
  "v_and_b32 v215, s92, v11\n"
  "v_cvt_pk_f16_f32 v8, v208, v209\n"
  "v_cvt_pk_f16_f32 v9, v210, v211\n"
  "v_cvt_pk_f16_f32 v10, v212, v213\n"
  "v_cvt_pk_f16_f32 v11, v214, v215\n"
  "v_pk_maximum3_f16 v228, v228, v8, v8 neg_lo:[0,0,1] neg_hi:[0,0,1]\n"
  "v_pk_maximum3_f16 v229, v229, v9, v9 neg_lo:[0,0,1] neg_hi:[0,0,1]\n"
  "v_pk_maximum3_f16 v230, v230, v10, v10 neg_lo:[0,0,1] neg_hi:[0,0,1]\n"
  "v_pk_maximum3_f16 v231, v231, v11, v11 neg_lo:[0,0,1] neg_hi:[0,0,1]\n"
  "ds_write_b128 v254, v[8:11] offset:6144\n"
  "ds_read_b64_tr_b16 v[8:9], v253 offset:12288\n"
  "ds_read_b64_tr_b16 v[10:11], v253 offset:15360\n"
  "s_waitcnt lgkmcnt(15)\n"
  "v_mfma_f32_16x16x32_bf16 v[132:135], v[220:223], v[12:15], v[132:135]\n"
  "v_lshlrev_b32 v208, 16, v12\n"
  "v_lshlrev_b32 v210, 16, v13\n"
  "v_lshlrev_b32 v212, 16, v14\n"
  "v_lshlrev_b32 v214, 16, v15\n"
  "v_and_b32 v209, s92, v12\n"
  "v_and_b32 v211, s92, v13\n"
  "v_and_b32 v213, s92, v14\n"
  "v_and_b32 v215, s92, v15\n"
  "v_cvt_pk_f16_f32 v12, v208, v209\n"
  "v_cvt_pk_f16_f32 v13, v210, v211\n"
  "v_cvt_pk_f16_f32 v14, v212, v213\n"
  "v_cvt_pk_f16_f32 v15, v214, v215\n"
  "v_pk_maximum3_f16 v228, v228, v12, v12 neg_lo:[0,0,1] neg_hi:[0,0,1]\n"
  "v_pk_maximum3_f16 v229, v229, v13, v13 neg_lo:[0,0,1] neg_hi:[0,0,1]\n"
  "v_pk_maximum3_f16 v230, v230, v14, v14 neg_lo:[0,0,1] neg_hi:[0,0,1]\n"
  "v_pk_maximum3_f16 v231, v231, v15, v15 neg_lo:[0,0,1] neg_hi:[0,0,1]\n"
  "ds_write_b128 v254, v[12:15] offset:7168\n"
  "ds_read_b64_tr_b16 v[12:13], v253 offset:12296\n"
  "ds_read_b64_tr_b16 v[14:15], v253 offset:15368\n"
  "s_waitcnt lgkmcnt(15)\n"
  "v_mfma_f32_16x16x32_bf16 v[136:139], v[220:223], v[16:19], v[136:139]\n"
  "v_lshlrev_b32 v208, 16, v16\n"
  "v_lshlrev_b32 v210, 16, v17\n"
  "v_lshlrev_b32 v212, 16, v18\n"
  "v_lshlrev_b32 v214, 16, v19\n"
  "v_and_b32 v209, s92, v16\n"
  "v_and_b32 v211, s92, v17\n"
  "v_and_b32 v213, s92, v18\n"
  "v_and_b32 v215, s92, v19\n"
  "v_cvt_pk_f16_f32 v16, v208, v209\n"
  "v_cvt_pk_f16_f32 v17, v210, v211\n"
  "v_cvt_pk_f16_f32 v18, v212, v213\n"
  "v_cvt_pk_f16_f32 v19, v214, v215\n"
  "v_pk_maximum3_f16 v228, v228, v16, v16 neg_lo:[0,0,1] neg_hi:[0,0,1]\n"
  "v_pk_maximum3_f16 v229, v229, v17, v17 neg_lo:[0,0,1] neg_hi:[0,0,1]\n"
  "v_pk_maximum3_f16 v230, v230, v18, v18 neg_lo:[0,0,1] neg_hi:[0,0,1]\n"
  "v_pk_maximum3_f16 v231, v231, v19, v19 neg_lo:[0,0,1] neg_hi:[0,0,1]\n"
  "ds_write_b128 v254, v[16:19] offset:8192\n"
  "ds_read_b64_tr_b16 v[16:17], v253 offset:12352\n"
  "ds_read_b64_tr_b16 v[18:19], v253 offset:15424\n"
  "s_waitcnt lgkmcnt(15)\n"
  "v_mfma_f32_16x16x32_bf16 v[140:143], v[220:223], v[20:23], v[140:143]\n"
  "v_lshlrev_b32 v208, 16, v20\n"
  "v_lshlrev_b32 v210, 16, v21\n"
  "v_lshlrev_b32 v212, 16, v22\n"
  "v_lshlrev_b32 v214, 16, v23\n"
  "v_and_b32 v209, s92, v20\n"
  "v_and_b32 v211, s92, v21\n"
  "v_and_b32 v213, s92, v22\n"
  "v_and_b32 v215, s92, v23\n"
  "v_cvt_pk_f16_f32 v20, v208, v209\n"
  "v_cvt_pk_f16_f32 v21, v210, v211\n"
  "v_cvt_pk_f16_f32 v22, v212, v213\n"
  "v_cvt_pk_f16_f32 v23, v214, v215\n"
  "v_pk_maximum3_f16 v228, v228, v20, v20 neg_lo:[0,0,1] neg_hi:[0,0,1]\n"
  "v_pk_maximum3_f16 v229, v229, v21, v21 neg_lo:[0,0,1] neg_hi:[0,0,1]\n"
  "v_pk_maximum3_f16 v230, v230, v22, v22 neg_lo:[0,0,1] neg_hi:[0,0,1]\n"
  "v_pk_maximum3_f16 v231, v231, v23, v23 neg_lo:[0,0,1] neg_hi:[0,0,1]\n"
  "ds_write_b128 v254, v[20:23] offset:9216\n"
  "ds_read_b64_tr_b16 v[20:21], v253 offset:12360\n"
  "ds_read_b64_tr_b16 v[22:23], v253 offset:15432\n"
  "s_waitcnt lgkmcnt(15)\n"
  "v_mfma_f32_16x16x32_bf16 v[144:147], v[220:223], v[24:27], v[144:147]\n"
  "v_lshlrev_b32 v208, 16, v24\n"
  "v_lshlrev_b32 v210, 16, v25\n"
  "v_lshlrev_b32 v212, 16, v26\n"
  "v_lshlrev_b32 v214, 16, v27\n"
  "v_and_b32 v209, s92, v24\n"
  "v_and_b32 v211, s92, v25\n"
  "v_and_b32 v213, s92, v26\n"
  "v_and_b32 v215, s92, v27\n"
  "v_cvt_pk_f16_f32 v24, v208, v209\n"
  "v_cvt_pk_f16_f32 v25, v210, v211\n"
  "v_cvt_pk_f16_f32 v26, v212, v213\n"
  "v_cvt_pk_f16_f32 v27, v214, v215\n"
  "v_pk_maximum3_f16 v228, v228, v24, v24 neg_lo:[0,0,1] neg_hi:[0,0,1]\n"
  "v_pk_maximum3_f16 v229, v229, v25, v25 neg_lo:[0,0,1] neg_hi:[0,0,1]\n"
  "v_pk_maximum3_f16 v230, v230, v26, v26 neg_lo:[0,0,1] neg_hi:[0,0,1]\n"
  "v_pk_maximum3_f16 v231, v231, v27, v27 neg_lo:[0,0,1] neg_hi:[0,0,1]\n"
  "ds_write_b128 v254, v[24:27] offset:10240\n"
  "ds_read_b64_tr_b16 v[24:25], v253 offset:12416\n"
  "ds_read_b64_tr_b16 v[26:27], v253 offset:15488\n"
  "s_waitcnt lgkmcnt(15)\n"
  "v_mfma_f32_16x16x32_bf16 v[148:151], v[220:223], v[28:31], v[148:151]\n"
  "v_lshlrev_b32 v208, 16, v28\n"
  "v_lshlrev_b32 v210, 16, v29\n"
  "v_lshlrev_b32 v212, 16, v30\n"
  "v_lshlrev_b32 v214, 16, v31\n"
  "v_and_b32 v209, s92, v28\n"
  "v_and_b32 v211, s92, v29\n"
  "v_and_b32 v213, s92, v30\n"
  "v_and_b32 v215, s92, v31\n"
  "v_cvt_pk_f16_f32 v28, v208, v209\n"
  "v_cvt_pk_f16_f32 v29, v210, v211\n"
  "v_cvt_pk_f16_f32 v30, v212, v213\n"
  "v_cvt_pk_f16_f32 v31, v214, v215\n"
  "v_pk_maximum3_f16 v228, v228, v28, v28 neg_lo:[0,0,1] neg_hi:[0,0,1]\n"
  "v_pk_maximum3_f16 v229, v229, v29, v29 neg_lo:[0,0,1] neg_hi:[0,0,1]\n"
  "v_pk_maximum3_f16 v230, v230, v30, v30 neg_lo:[0,0,1] neg_hi:[0,0,1]\n"
  "v_pk_maximum3_f16 v231, v231, v31, v31 neg_lo:[0,0,1] neg_hi:[0,0,1]\n"
  "ds_write_b128 v254, v[28:31] offset:11264\n"
  "ds_read_b64_tr_b16 v[28:29], v253 offset:12424\n"
  "ds_read_b64_tr_b16 v[30:31], v253 offset:15496\n"
  "s_waitcnt lgkmcnt(15)\n"
  "v_mfma_f32_16x16x32_bf16 v[128:131], v[224:227], v[8:11], v[128:131]\n"
  "v_lshlrev_b32 v208, 16, v8\n"
  "v_lshlrev_b32 v210, 16, v9\n"
  "v_lshlrev_b32 v212, 16, v10\n"
  "v_lshlrev_b32 v214, 16, v11\n"
  "v_and_b32 v209, s92, v8\n"
  "v_and_b32 v211, s92, v9\n"
  "v_and_b32 v213, s92, v10\n"
  "v_and_b32 v215, s92, v11\n"
  "v_cvt_pk_f16_f32 v8, v208, v209\n"
  "v_cvt_pk_f16_f32 v9, v210, v211\n"
  "v_cvt_pk_f16_f32 v10, v212, v213\n"
  "v_cvt_pk_f16_f32 v11, v214, v215\n"
  "v_pk_maximum3_f16 v228, v228, v8, v8 neg_lo:[0,0,1] neg_hi:[0,0,1]\n"
  "v_pk_maximum3_f16 v229, v229, v9, v9 neg_lo:[0,0,1] neg_hi:[0,0,1]\n"
  "v_pk_maximum3_f16 v230, v230, v10, v10 neg_lo:[0,0,1] neg_hi:[0,0,1]\n"
  "v_pk_maximum3_f16 v231, v231, v11, v11 neg_lo:[0,0,1] neg_hi:[0,0,1]\n"
  "ds_write_b128 v254, v[8:11] offset:12288\n"
  "s_waitcnt lgkmcnt(13)\n"
  "v_mfma_f32_16x16x32_bf16 v[132:135], v[224:227], v[12:15], v[132:135]\n"
  "v_lshlrev_b32 v208, 16, v12\n"
  "v_lshlrev_b32 v210, 16, v13\n"
  "v_lshlrev_b32 v212, 16, v14\n"
  "v_lshlrev_b32 v214, 16, v15\n"
  "v_and_b32 v209, s92, v12\n"
  "v_and_b32 v211, s92, v13\n"
  "v_and_b32 v213, s92, v14\n"
  "v_and_b32 v215, s92, v15\n"
  "v_cvt_pk_f16_f32 v12, v208, v209\n"
  "v_cvt_pk_f16_f32 v13, v210, v211\n"
  "v_cvt_pk_f16_f32 v14, v212, v213\n"
  "v_cvt_pk_f16_f32 v15, v214, v215\n"
  "v_pk_maximum3_f16 v228, v228, v12, v12 neg_lo:[0,0,1] neg_hi:[0,0,1]\n"
  "v_pk_maximum3_f16 v229, v229, v13, v13 neg_lo:[0,0,1] neg_hi:[0,0,1]\n"
  "v_pk_maximum3_f16 v230, v230, v14, v14 neg_lo:[0,0,1] neg_hi:[0,0,1]\n"
  "v_pk_maximum3_f16 v231, v231, v15, v15 neg_lo:[0,0,1] neg_hi:[0,0,1]\n"
  "ds_write_b128 v254, v[12:15] offset:13312\n"
  "s_waitcnt lgkmcnt(11)\n"
  "v_mfma_f32_16x16x32_bf16 v[136:139], v[224:227], v[16:19], v[136:139]\n"
  "v_lshlrev_b32 v208, 16, v16\n"
  "v_lshlrev_b32 v210, 16, v17\n"
  "v_lshlrev_b32 v212, 16, v18\n"
  "v_lshlrev_b32 v214, 16, v19\n"
  "v_and_b32 v209, s92, v16\n"
  "v_and_b32 v211, s92, v17\n"
  "v_and_b32 v213, s92, v18\n"
  "v_and_b32 v215, s92, v19\n"
  "v_cvt_pk_f16_f32 v16, v208, v209\n"
  "v_cvt_pk_f16_f32 v17, v210, v211\n"
  "v_cvt_pk_f16_f32 v18, v212, v213\n"
  "v_cvt_pk_f16_f32 v19, v214, v215\n"
  "v_pk_maximum3_f16 v228, v228, v16, v16 neg_lo:[0,0,1] neg_hi:[0,0,1]\n"
  "v_pk_maximum3_f16 v229, v229, v17, v17 neg_lo:[0,0,1] neg_hi:[0,0,1]\n"
  "v_pk_maximum3_f16 v230, v230, v18, v18 neg_lo:[0,0,1] neg_hi:[0,0,1]\n"
  "v_pk_maximum3_f16 v231, v231, v19, v19 neg_lo:[0,0,1] neg_hi:[0,0,1]\n"
  "ds_write_b128 v254, v[16:19] offset:14336\n"
  "s_waitcnt lgkmcnt(9)\n"
  "v_mfma_f32_16x16x32_bf16 v[140:143], v[224:227], v[20:23], v[140:143]\n"
  "v_lshlrev_b32 v208, 16, v20\n"
  "v_lshlrev_b32 v210, 16, v21\n"
  "v_lshlrev_b32 v212, 16, v22\n"
  "v_lshlrev_b32 v214, 16, v23\n"
  "v_and_b32 v209, s92, v20\n"
  "v_and_b32 v211, s92, v21\n"
  "v_and_b32 v213, s92, v22\n"
  "v_and_b32 v215, s92, v23\n"
  "v_cvt_pk_f16_f32 v20, v208, v209\n"
  "v_cvt_pk_f16_f32 v21, v210, v211\n"
  "v_cvt_pk_f16_f32 v22, v212, v213\n"
  "v_cvt_pk_f16_f32 v23, v214, v215\n"
  "v_pk_maximum3_f16 v228, v228, v20, v20 neg_lo:[0,0,1] neg_hi:[0,0,1]\n"
  "v_pk_maximum3_f16 v229, v229, v21, v21 neg_lo:[0,0,1] neg_hi:[0,0,1]\n"
  "v_pk_maximum3_f16 v230, v230, v22, v22 neg_lo:[0,0,1] neg_hi:[0,0,1]\n"
  "v_pk_maximum3_f16 v231, v231, v23, v23 neg_lo:[0,0,1] neg_hi:[0,0,1]\n"
  "ds_write_b128 v254, v[20:23] offset:15360\n"
  "s_waitcnt lgkmcnt(7)\n"
  "v_mfma_f32_16x16x32_bf16 v[144:147], v[224:227], v[24:27], v[144:147]\n"
  "v_lshlrev_b32 v208, 16, v24\n"
  "v_lshlrev_b32 v210, 16, v25\n"
  "v_lshlrev_b32 v212, 16, v26\n"
  "v_lshlrev_b32 v214, 16, v27\n"
  "v_and_b32 v209, s92, v24\n"
  "v_and_b32 v211, s92, v25\n"
  "v_and_b32 v213, s92, v26\n"
  "v_and_b32 v215, s92, v27\n"
  "v_cvt_pk_f16_f32 v24, v208, v209\n"
  "v_cvt_pk_f16_f32 v25, v210, v211\n"
  "v_cvt_pk_f16_f32 v26, v212, v213\n"
  "v_cvt_pk_f16_f32 v27, v214, v215\n"
  "v_pk_maximum3_f16 v228, v228, v24, v24 neg_lo:[0,0,1] neg_hi:[0,0,1]\n"
  "v_pk_maximum3_f16 v229, v229, v25, v25 neg_lo:[0,0,1] neg_hi:[0,0,1]\n"
  "v_pk_maximum3_f16 v230, v230, v26, v26 neg_lo:[0,0,1] neg_hi:[0,0,1]\n"
  "v_pk_maximum3_f16 v231, v231, v27, v27 neg_lo:[0,0,1] neg_hi:[0,0,1]\n"
  "ds_write_b128 v254, v[24:27] offset:16384\n"
  "s_waitcnt lgkmcnt(5)\n"
  "v_mfma_f32_16x16x32_bf16 v[148:151], v[224:227], v[28:31], v[148:151]\n"
  "v_lshlrev_b32 v208, 16, v28\n"
  "v_lshlrev_b32 v210, 16, v29\n"
  "v_lshlrev_b32 v212, 16, v30\n"
  "v_lshlrev_b32 v214, 16, v31\n"
  "v_and_b32 v209, s92, v28\n"
  "v_and_b32 v211, s92, v29\n"
  "v_and_b32 v213, s92, v30\n"
  "v_and_b32 v215, s92, v31\n"
  "v_cvt_pk_f16_f32 v28, v208, v209\n"
  "v_cvt_pk_f16_f32 v29, v210, v211\n"
  "v_cvt_pk_f16_f32 v30, v212, v213\n"
  "v_cvt_pk_f16_f32 v31, v214, v215\n"
  "v_pk_maximum3_f16 v228, v228, v28, v28 neg_lo:[0,0,1] neg_hi:[0,0,1]\n"
  "v_pk_maximum3_f16 v229, v229, v29, v29 neg_lo:[0,0,1] neg_hi:[0,0,1]\n"
  "v_pk_maximum3_f16 v230, v230, v30, v30 neg_lo:[0,0,1] neg_hi:[0,0,1]\n"
  "v_pk_maximum3_f16 v231, v231, v31, v31 neg_lo:[0,0,1] neg_hi:[0,0,1]\n"
  "ds_write_b128 v254, v[28:31] offset:17408\n"
  "v_pk_maximum3_f16 v228, v228, v229, v230\n"
  "v_pk_maximum3_f16 v228, v228, v231, v231\n"
  "v_bfe_u32 v229, v228, 16, 15\n"
  "v_and_b32 v228, 0x7fff, v228\n"
  "v_max_u32 v228, v228, v229\n"
  "s_nop 1\n"
  "v_max_u32_dpp v228, v228, v228 row_ror:8 row_mask:0xf bank_mask:0xf\n"
  "s_nop 1\n"
  "v_max_u32_dpp v228, v228, v228 row_ror:4 row_mask:0xf bank_mask:0xf\n"
  "s_nop 1\n"
  "v_max_u32_dpp v228, v228, v228 row_ror:2 row_mask:0xf bank_mask:0xf\n"
  "s_nop 1\n"
  "v_max_u32_dpp v228, v228, v228 row_ror:1 row_mask:0xf bank_mask:0xf\n"
  "v_mov_b32 v229, 163332\n"
  "v_bfe_u32 v230, v252, 2, 2\n"
  "v_lshlrev_b32 v230, 5, v230\n"
  "v_lshl_add_u32 v230, v255, 2, v230\n"
  "s_cmp_ge_u32 s73, 2\n"
  "s_cselect_b32 s94, 3648, 0\n"
  "s_cselect_b32 s92, 0x10000, 0\n"
  "s_add_u32 s94, s94, 159744\n"
  "s_add_u32 s92, s92, 6144\n"
  "s_cmp_ge_u32 s72, 4\n"
  "s_cselect_b32 s94, s92, s94\n"
  "v_add_u32 v230, s94, v230\n"
  "s_mov_b32 exec_lo, 0x00010001\n"
  "s_mov_b32 exec_hi, 0x00010001\n"
  "ds_max_u32 v229, v228\n"
  "s_mov_b64 exec, 0xffff\n"
  "ds_add_f32 v230, v128 offset:0\n"
  "ds_add_f32 v230, v132 offset:16\n"
  "ds_add_f32 v230, v136 offset:128\n"
  "ds_add_f32 v230, v140 offset:144\n"
  "ds_add_f32 v230, v144 offset:256\n"
  "ds_add_f32 v230, v148 offset:272\n"
  "s_mov_b64 exec, -1\n"
  ".Lnow3%=:\n"
  "s_waitcnt vmcnt(0) lgkmcnt(0)\n"
  "s_nop 7\n"
  "s_cmp_lg_u32 s72, 4\n"
  "s_cbranch_scc1 .Lnopoll%=\n"
  "s_setprio 3\n"
  "v_mbcnt_lo_u32_b32 v216, -1, 0\n"
  "v_mbcnt_hi_u32_b32 v216, -1, v216\n"
  "v_lshlrev_b32 v217, 2, v216\n"
  "v_lshlrev_b32 v218, 3, v216\n"
  "v_and_b32 v219, 0xc4, v217\n"
  "v_and_b32 v224, 8, v216\n"
  "v_and_b32 v225, 0x30, v218\n"
  "v_add3_u32 v219, v219, v224, v225\n"
  "v_add_u32 v219, 161792, v219\n"
  "v_and_b32 v227, 15, v216\n"
  "v_lshlrev_b32 v227, 8, v227\n"
  "s_mov_b32 s90, s50\n"
  "s_mov_b32 s91, 0\n"
  "v_lshlrev_b32 v231, 4, v216\n"
  "s_mov_b32 s96, 131072\n"
  "s_cmp_eq_u32 s91, 1\n"
  "s_cbranch_scc1 .Lsv%=\n"
  "s_cmp_eq_u32 s90, s74\n"
  "s_cbranch_scc1 .Lpe%=\n"
  "v_mov_b32 v228, 0\n"
  "v_mov_b32 v225, 0\n"
  "s_mov_b32 s96, 131072\n"
  ".Lpg%=:\n"
  "buffer_load_dword v224, v225, s[80:83], 0 offen offset:256 sc1\n"
  "s_waitcnt vmcnt(0)\n"
  "v_readfirstlane_b32 s94, v224\n"
  "s_nop 1\n"
  "s_cmp_eq_u32 s94, s74\n"
  "s_cbranch_scc1 .Lpgd%=\n"
  "s_sleep 2\n"
  "s_sub_u32 s96, s96, 1\n"
  "s_cmp_lg_u32 s96, 0\n"
  "s_cbranch_scc1 .Lpg%=\n"
  ".Lpgd%=:\n"
  ".Lpe%=:\n"
  "v_mov_b32 v225, v227\n"
  "s_mov_b64 exec, 0xffff\n"
  "s_mov_b32 s96, 131072\n"
  ".Lpc%=:\n"
  "buffer_load_dword v224, v225, s[80:83], 0 offen offset:512 sc1\n"
  "s_waitcnt vmcnt(0)\n"
  "v_cmp_gt_u32 vcc, 15, v224\n"
  "s_nop 1\n"
  "s_cmp_eq_u64 vcc, 0\n"
  "s_cbranch_scc1 .Lpcd%=\n"
  "s_sleep 3\n"
  "s_sub_u32 s96, s96, 1\n"
  "s_cmp_lg_u32 s96, 0\n"
  "s_cbranch_scc1 .Lpc%=\n"
  ".Lpcd%=:\n"
  "s_mov_b64 exec, -1\n"
  "s_mov_b32 s96, 131072\n"
  ".Lsl%=:\n"
  "s_mov_b32 s94, 8192\n"
  "buffer_load_dwordx4 v[8:11], v231, s[80:83], s94 offen offset:0 sc1\n"
  "buffer_load_dwordx4 v[12:15], v231, s[80:83], s94 offen offset:1024 sc1\n"
  "buffer_load_dwordx4 v[16:19], v231, s[80:83], s94 offen offset:2048 sc1\n"
  "s_mov_b32 s94, 11264\n"
  "buffer_load_dwordx4 v[20:23], v231, s[80:83], s94 offen offset:0 sc1\n"
  "buffer_load_dwordx4 v[24:27], v231, s[80:83], s94 offen offset:1024 sc1\n"
  "buffer_load_dwordx4 v[28:31], v231, s[80:83], s94 offen offset:2048 sc1\n"
  "s_mov_b32 s94, 14336\n"
  "buffer_load_dwordx4 v[128:131], v231, s[80:83], s94 offen offset:0 sc1\n"
  "buffer_load_dwordx4 v[132:135], v231, s[80:83], s94 offen offset:1024 sc1\n"
  "buffer_load_dwordx4 v[136:139], v231, s[80:83], s94 offen offset:2048 sc1\n"
  "s_mov_b32 s94, 17408\n"
  "buffer_load_dwordx4 v[140:143], v231, s[80:83], s94 offen offset:0 sc1\n"
  "buffer_load_dwordx4 v[144:147], v231, s[80:83], s94 offen offset:1024 sc1\n"
  "buffer_load_dwordx4 v[148:151], v231, s[80:83], s94 offen offset:2048 sc1\n"
  ".Lsv%=:\n"
  "s_waitcnt vmcnt(8)\n"
  "v_lshl_add_u64 v[8:9], v[8:9], 0, v[20:21]\n"
  "v_lshl_add_u64 v[10:11], v[10:11], 0, v[22:23]\n"
  "s_waitcnt vmcnt(7)\n"
  "v_lshl_add_u64 v[12:13], v[12:13], 0, v[24:25]\n"
  "v_lshl_add_u64 v[14:15], v[14:15], 0, v[26:27]\n"
  "s_waitcnt vmcnt(6)\n"
  "v_lshl_add_u64 v[16:17], v[16:17], 0, v[28:29]\n"
  "v_lshl_add_u64 v[18:19], v[18:19], 0, v[30:31]\n"
  "s_waitcnt vmcnt(2)\n"
  "v_lshl_add_u64 v[128:129], v[128:129], 0, v[140:141]\n"
  "v_lshl_add_u64 v[130:131], v[130:131], 0, v[142:143]\n"
  "s_waitcnt vmcnt(1)\n"
  "v_lshl_add_u64 v[132:133], v[132:133], 0, v[144:145]\n"
  "v_lshl_add_u64 v[134:135], v[134:135], 0, v[146:147]\n"
  "s_waitcnt vmcnt(0)\n"
  "v_lshl_add_u64 v[136:137], v[136:137], 0, v[148:149]\n"
  "v_lshl_add_u64 v[138:139], v[138:139], 0, v[150:151]\n"
  "v_lshl_add_u64 v[8:9], v[8:9], 0, v[128:129]\n"
  "v_lshl_add_u64 v[10:11], v[10:11], 0, v[130:131]\n"
  "v_lshl_add_u64 v[12:13], v[12:13], 0, v[132:133]\n"
  "v_lshl_add_u64 v[14:15], v[14:15], 0, v[134:135]\n"
  "v_lshl_add_u64 v[16:17], v[16:17], 0, v[136:137]\n"
  "v_lshl_add_u64 v[18:19], v[18:19], 0, v[138:139]\n"
  "v_and_b32 v224, v9, v11\n"
  "v_and_b32 v226, v13, v15\n"
  "v_and_b32 v227, v17, v19\n"
  "v_and_b32 v224, v224, v226\n"
  "v_and_b32 v224, v224, v227\n"
  "v_cmp_le_i32 vcc, 0, v224\n"
  "s_nop 1\n"
  "s_cmp_eq_u64 vcc, 0\n"
  "s_cbranch_scc1 .Lsd%=\n"
  "s_sub_u32 s96, s96, 1\n"
  "s_cmp_lg_u32 s96, 0\n"
  "s_cbranch_scc1 .Lsl%=\n"
  ".Lsd%=:\n"
  "s_cmp_lg_u32 s75, 0\n"
  "s_cbranch_scc1 .Lnoep%=\n"
  "v_mov_b32 v224, s74\n"
  "v_mov_b32 v225, 0\n"
  "s_mov_b64 exec, 1\n"
  "buffer_store_dword v224, v225, s[80:83], 0 offen offset:0 sc1\n"
  "s_mov_b64 exec, -1\n"
  ".Lnoep%=:\n"
  "s_mov_b32 s93, 0x4f800000\n"
  "v_and_b32 v9, 0x7fffff, v9\n"
  "v_and_b32 v11, 0x7fffff, v11\n"
  "v_and_b32 v13, 0x7fffff, v13\n"
  "v_and_b32 v15, 0x7fffff, v15\n"
  "v_and_b32 v17, 0x7fffff, v17\n"
  "v_and_b32 v19, 0x7fffff, v19\n"
  "v_cvt_f32_u32 v20, v8\n"
  "v_cvt_f32_u32 v21, v9\n"
  "v_cvt_f32_u32 v22, v10\n"
  "v_cvt_f32_u32 v23, v11\n"
  "v_cvt_f32_u32 v24, v12\n"
  "v_cvt_f32_u32 v25, v13\n"
  "v_cvt_f32_u32 v26, v14\n"
  "v_cvt_f32_u32 v27, v15\n"
  "v_cvt_f32_u32 v28, v16\n"
  "v_cvt_f32_u32 v29, v17\n"
  "v_cvt_f32_u32 v30, v18\n"
  "v_cvt_f32_u32 v31, v19\n"
  "v_fma_f32 v8, v21, s93, v20\n"
  "v_fma_f32 v9, v23, s93, v22\n"
  "v_fma_f32 v10, v25, s93, v24\n"
  "v_fma_f32 v11, v27, s93, v26\n"
  "v_fma_f32 v12, v29, s93, v28\n"
  "v_fma_f32 v13, v31, s93, v30\n"
  "s_sub_u32 s92, 0x8011cb7a, s97\n"
  "v_mov_b32 v226, 0x358637bd\n"
  "v_fma_f32 v8, v8, s92, v226\n"
  "v_fma_f32 v9, v9, s92, v226\n"
  "v_fma_f32 v10, v10, s92, v226\n"
  "v_fma_f32 v11, v11, s92, v226\n"
  "v_fma_f32 v12, v12, s92, v226\n"
  "v_fma_f32 v13, v13, s92, v226\n"
  "v_sqrt_f32 v8, v8\n"
  "v_sqrt_f32 v9, v9\n"
  "v_sqrt_f32 v10, v10\n"
  "v_sqrt_f32 v11, v11\n"
  "v_sqrt_f32 v12, v12\n"
  "v_sqrt_f32 v13, v13\n"
  "v_add_f32 v227, v8, v9\n"
  "v_add_f32 v228, v10, v11\n"
  "v_add_f32 v229, v12, v13\n"
  "v_add_f32 v227, v227, v228\n"
  "v_add_f32 v227, v227, v229\n"
  "s_nop 1\n"
  "v_add_f32_dpp v227, v227, v227 row_ror:8 row_mask:0xf bank_mask:0xf\n"
  "s_nop 1\n"
  "v_add_f32_dpp v227, v227, v227 row_ror:4 row_mask:0xf bank_mask:0xf\n"
  "s_nop 1\n"
  "v_add_f32_dpp v227, v227, v227 row_ror:2 row_mask:0xf bank_mask:0xf\n"
  "s_nop 1\n"
  "v_add_f32_dpp v227, v227, v227 row_ror:1 row_mask:0xf bank_mask:0xf\n"
  "v_mov_b32 v228, v227\n"
  "s_nop 1\n"
  "v_permlane16_swap_b32 v227, v228\n"
  "v_add_f32 v227, v227, v228\n"
  "v_mov_b32 v228, v227\n"
  "s_nop 1\n"
  "v_permlane32_swap_b32 v227, v228\n"
  "v_add_f32 v227, v227, v228\n"
  "s_mov_b32 s92, 0x3b2aaaab\n"
  "v_fma_f32 v227, v227, s92, v226\n"
  "v_rcp_f32 v227, v227\n"
  "s_mov_b32 s92, 0xc0089ba4\n"
  "v_mov_b32 v230, 0\n"
  "v_lshlrev_b32 v228, 16, v220\n"
  "v_mul_f32 v8, v8, v227\n"
  "v_fma_f32 v8, v8, v228, 1.0\n"
  "v_mul_f32 v8, s92, v8\n"
  "v_and_b32 v228, 0xffff0000, v220\n"
  "v_mul_f32 v9, v9, v227\n"
  "v_fma_f32 v9, v9, v228, 1.0\n"
  "v_mul_f32 v9, s92, v9\n"
  "v_lshlrev_b32 v228, 16, v221\n"
  "v_mul_f32 v10, v10, v227\n"
  "v_fma_f32 v10, v10, v228, 1.0\n"
  "v_mul_f32 v10, s92, v10\n"
  "v_and_b32 v228, 0xffff0000, v221\n"
  "v_mul_f32 v11, v11, v227\n"
  "v_fma_f32 v11, v11, v228, 1.0\n"
  "v_mul_f32 v11, s92, v11\n"
  "v_lshlrev_b32 v228, 16, v222\n"
  "v_mul_f32 v12, v12, v227\n"
  "v_fma_f32 v12, v12, v228, 1.0\n"
  "v_mul_f32 v12, s92, v12\n"
  "v_and_b32 v228, 0xffff0000, v222\n"
  "v_mul_f32 v13, v13, v227\n"
  "v_fma_f32 v13, v13, v228, 1.0\n"
  "v_mul_f32 v13, s92, v13\n"
  "v_cvt_pk_f16_f32 v16, v8, v9\n"
  "v_cvt_pk_f16_f32 v17, v10, v11\n"
  "v_cvt_pk_f16_f32 v18, v12, v13\n"
  "ds_write_b32 v219, v16 offset:0\n"
  "ds_write_b32 v219, v17 offset:256\n"
  "ds_write_b32 v219, v18 offset:512\n"
  "v_max3_f32 v230, |v8|, |v9|, |v10|\n"
  "v_max3_f32 v229, |v11|, |v12|, |v13|\n"
  "v_max_u32 v230, v230, v229\n"
  "v_and_b32 v229, 0x7fffffff, v227\n"
  "v_cmp_lt_u32 vcc, 0x7f7fffff, v229\n"
  "s_nop 1\n"
  "v_cndmask_b32 v230, v230, v229, vcc\n"
  "v_mov_b32 v228, 163336\n"
  "ds_max_u32 v228, v230\n"
  "v_add_u32 v218, 160256, v218\n"
  "ds_write_b64 v218, v[8:9] offset:0\n"
  "ds_write_b64 v218, v[10:11] offset:512\n"
  "ds_write_b64 v218, v[12:13] offset:1024\n"
  ".Lnopoll%=:\n"
  "s_waitcnt lgkmcnt(0)\n"
  "v_mbcnt_lo_u32_b32 v216, -1, 0\n"
  "v_mbcnt_hi_u32_b32 v216, -1, v216\n"
  "s_lshr_b32 s92, s72, 1\n"
  "s_mul_i32 s93, s72, 3072\n"
  "s_add_u32 s93, s93, 86016\n"
  "s_lshl_b32 s92, s92, 6\n"
  "s_add_u32 s92, s92, 161792\n"
  "v_and_b32 v218, 0x30, v216\n"
  "v_lshlrev_b32 v219, 4, v216\n"
  "v_add_u32 v218, s92, v218\n"
  "v_add_u32 v219, s93, v219\n"
  "s_mov_b32 s92, 0x00040004\n"
  "s_mov_b32 s93, 0xfff8fff8\n"
  "s_waitcnt lgkmcnt(0)\n"
  "s_barrier\n"
  "ds_read_b128 v[140:143], v218 offset:0\n"
  "ds_read_b128 v[8:11], v219 offset:0\n"
  "ds_read_b128 v[12:15], v219 offset:1024\n"
  "ds_read_b128 v[16:19], v219 offset:2048\n"
  "ds_read_b128 v[144:147], v218 offset:256\n"
  "ds_read_b128 v[20:23], v219 offset:24576\n"
  "ds_read_b128 v[24:27], v219 offset:25600\n"
  "ds_read_b128 v[28:31], v219 offset:26624\n"
  "ds_read_b128 v[148:151], v218 offset:512\n"
  "ds_read_b128 v[128:131], v219 offset:49152\n"
  "ds_read_b128 v[132:135], v219 offset:50176\n"
  "ds_read_b128 v[136:139], v219 offset:51200\n"
  "s_cmp_lg_u32 s72, 3\n"
  "s_cbranch_scc1 .Lnb1%=\n"
  "s_mov_b64 exec, 0xffffff\n"
  "ds_read_b128 v[208:211], v3 offset:28672\n"
  "ds_read_b128 v[212:215], v3 offset:32320\n"
  "s_mov_b64 exec, -1\n"
  ".Lnb1%=:\n"
  "s_waitcnt lgkmcnt(10)\n"
  "v_pk_mul_f16 v8, v8, v140\n"
  "v_pk_mul_f16 v9, v9, v141\n"
  "v_pk_mul_f16 v10, v10, v142\n"
  "v_pk_mul_f16 v11, v11, v143\n"
  "v_add_u32 v8, s92, v8\n"
  "v_add_u32 v9, s92, v9\n"
  "v_add_u32 v10, s92, v10\n"
  "v_add_u32 v11, s92, v11\n"
  "v_and_b32 v8, s93, v8\n"
  "v_and_b32 v9, s93, v9\n"
  "v_and_b32 v10, s93, v10\n"
  "v_and_b32 v11, s93, v11\n"
  "ds_write_b128 v219, v[8:11] offset:0\n"
  "s_waitcnt lgkmcnt(10)\n"
  "v_pk_mul_f16 v12, v12, v140\n"
  "v_pk_mul_f16 v13, v13, v141\n"
  "v_pk_mul_f16 v14, v14, v142\n"
  "v_pk_mul_f16 v15, v15, v143\n"
  "v_add_u32 v12, s92, v12\n"
  "v_add_u32 v13, s92, v13\n"
  "v_add_u32 v14, s92, v14\n"
  "v_add_u32 v15, s92, v15\n"
  "v_and_b32 v12, s93, v12\n"
  "v_and_b32 v13, s93, v13\n"
  "v_and_b32 v14, s93, v14\n"
  "v_and_b32 v15, s93, v15\n"
  "ds_write_b128 v219, v[12:15] offset:1024\n"
  "s_waitcnt lgkmcnt(10)\n"
  "v_pk_mul_f16 v16, v16, v140\n"
  "v_pk_mul_f16 v17, v17, v141\n"
  "v_pk_mul_f16 v18, v18, v142\n"
  "v_pk_mul_f16 v19, v19, v143\n"
  "v_add_u32 v16, s92, v16\n"
  "v_add_u32 v17, s92, v17\n"
  "v_add_u32 v18, s92, v18\n"
  "v_add_u32 v19, s92, v19\n"
  "v_and_b32 v16, s93, v16\n"
  "v_and_b32 v17, s93, v17\n"
  "v_and_b32 v18, s93, v18\n"
  "v_and_b32 v19, s93, v19\n"
  "ds_write_b128 v219, v[16:19] offset:2048\n"
  "s_waitcnt lgkmcnt(9)\n"
  "v_pk_mul_f16 v20, v20, v144\n"
  "v_pk_mul_f16 v21, v21, v145\n"
  "v_pk_mul_f16 v22, v22, v146\n"
  "v_pk_mul_f16 v23, v23, v147\n"
  "v_add_u32 v20, s92, v20\n"
  "v_add_u32 v21, s92, v21\n"
  "v_add_u32 v22, s92, v22\n"
  "v_add_u32 v23, s92, v23\n"
  "v_and_b32 v20, s93, v20\n"
  "v_and_b32 v21, s93, v21\n"
  "v_and_b32 v22, s93, v22\n"
  "v_and_b32 v23, s93, v23\n"
  "ds_write_b128 v219, v[20:23] offset:24576\n"
  "s_waitcnt lgkmcnt(9)\n"
  "v_pk_mul_f16 v24, v24, v144\n"
  "v_pk_mul_f16 v25, v25, v145\n"
  "v_pk_mul_f16 v26, v26, v146\n"
  "v_pk_mul_f16 v27, v27, v147\n"
  "v_add_u32 v24, s92, v24\n"
  "v_add_u32 v25, s92, v25\n"
  "v_add_u32 v26, s92, v26\n"
  "v_add_u32 v27, s92, v27\n"
  "v_and_b32 v24, s93, v24\n"
  "v_and_b32 v25, s93, v25\n"
  "v_and_b32 v26, s93, v26\n"
  "v_and_b32 v27, s93, v27\n"
  "ds_write_b128 v219, v[24:27] offset:25600\n"
  "s_waitcnt lgkmcnt(9)\n"
  "v_pk_mul_f16 v28, v28, v144\n"
  "v_pk_mul_f16 v29, v29, v145\n"
  "v_pk_mul_f16 v30, v30, v146\n"
  "v_pk_mul_f16 v31, v31, v147\n"
  "v_add_u32 v28, s92, v28\n"
  "v_add_u32 v29, s92, v29\n"
  "v_add_u32 v30, s92, v30\n"
  "v_add_u32 v31, s92, v31\n"
  "v_and_b32 v28, s93, v28\n"
  "v_and_b32 v29, s93, v29\n"
  "v_and_b32 v30, s93, v30\n"
  "v_and_b32 v31, s93, v31\n"
  "ds_write_b128 v219, v[28:31] offset:26624\n"
  "s_waitcnt lgkmcnt(8)\n"
  "v_pk_mul_f16 v128, v128, v148\n"
  "v_pk_mul_f16 v129, v129, v149\n"
  "v_pk_mul_f16 v130, v130, v150\n"
  "v_pk_mul_f16 v131, v131, v151\n"
  "v_add_u32 v128, s92, v128\n"
  "v_add_u32 v129, s92, v129\n"
  "v_add_u32 v130, s92, v130\n"
  "v_add_u32 v131, s92, v131\n"
  "v_and_b32 v128, s93, v128\n"
  "v_and_b32 v129, s93, v129\n"
  "v_and_b32 v130, s93, v130\n"
  "v_and_b32 v131, s93, v131\n"
  "ds_write_b128 v219, v[128:131] offset:49152\n"
  "s_waitcnt lgkmcnt(8)\n"
  "v_pk_mul_f16 v132, v132, v148\n"
  "v_pk_mul_f16 v133, v133, v149\n"
  "v_pk_mul_f16 v134, v134, v150\n"
  "v_pk_mul_f16 v135, v135, v151\n"
  "v_add_u32 v132, s92, v132\n"
  "v_add_u32 v133, s92, v133\n"
  "v_add_u32 v134, s92, v134\n"
  "v_add_u32 v135, s92, v135\n"
  "v_and_b32 v132, s93, v132\n"
  "v_and_b32 v133, s93, v133\n"
  "v_and_b32 v134, s93, v134\n"
  "v_and_b32 v135, s93, v135\n"
  "ds_write_b128 v219, v[132:135] offset:50176\n"
  "s_waitcnt lgkmcnt(8)\n"
  "v_pk_mul_f16 v136, v136, v148\n"
  "v_pk_mul_f16 v137, v137, v149\n"
  "v_pk_mul_f16 v138, v138, v150\n"
  "v_pk_mul_f16 v139, v139, v151\n"
  "v_add_u32 v136, s92, v136\n"
  "v_add_u32 v137, s92, v137\n"
  "v_add_u32 v138, s92, v138\n"
  "v_add_u32 v139, s92, v139\n"
  "v_and_b32 v136, s93, v136\n"
  "v_and_b32 v137, s93, v137\n"
  "v_and_b32 v138, s93, v138\n"
  "v_and_b32 v139, s93, v139\n"
  "ds_write_b128 v219, v[136:139] offset:51200\n"
  "s_cmp_lg_u32 s72, 3\n"
  "s_cbranch_scc1 .Lnb2%=\n"
  "s_mov_b64 exec, 0xffffff\n"
  "s_waitcnt lgkmcnt(9)\n"
  "v_add_f32 v208, v208, v212\n"
  "v_add_f32 v209, v209, v213\n"
  "v_add_f32 v210, v210, v214\n"
  "v_add_f32 v211, v211, v215\n"
  "ds_write_b128 v3, v[208:211] offset:28672\n"
  "s_mov_b64 exec, -1\n"
  ".Lnb2%=:\n"
  "s_waitcnt lgkmcnt(0)\n"
  "s_barrier\n"
  "ds_read_b128 v[216:219], v3 offset:22528\n"
  "ds_read_b128 v[220:223], v3 offset:23552\n"
  "ds_read_b128 v[224:227], v3 offset:24576\n"
  "ds_read_b128 v[228:231], v3 offset:25600\n"
  "ds_read_b128 v[8:11], v4 offset:0\n"
  "ds_read_b128 v[12:15], v4 offset:16\n"
  "ds_read_b128 v[16:19], v4 offset:128\n"
  "ds_read_b128 v[20:23], v4 offset:144\n"
  "ds_read_b128 v[24:27], v4 offset:256\n"
  "ds_read_b128 v[28:31], v4 offset:272\n"
  "s_setprio 3\n"
  "s_waitcnt lgkmcnt(5)\n"
  "v_mfma_f32_16x16x32_f16 v[128:131], v[216:219], v[248:251], v[8:11]\n"
  "v_permlane16_swap_b32 v80, v84\n"
  "v_mfma_f32_16x16x32_f16 v[8:11], v[216:219], v[240:243], v[8:11]\n"
  "s_waitcnt lgkmcnt(4)\n"
  "ds_read_b128 v[216:219], v3 offset:26624\n"
  "v_permlane16_swap_b32 v81, v85\n"
  "v_mfma_f32_16x16x32_f16 v[132:135], v[220:223], v[248:251], v[12:15]\n"
  "v_permlane16_swap_b32 v82, v86\n"
  "v_mfma_f32_16x16x32_f16 v[12:15], v[220:223], v[240:243], v[12:15]\n"
  "s_waitcnt lgkmcnt(4)\n"
  "ds_read_b128 v[220:223], v3 offset:27648\n"
  "v_permlane16_swap_b32 v83, v87\n"
  "v_mfma_f32_16x16x32_f16 v[136:139], v[224:227], v[248:251], v[16:19]\n"
  "v_permlane16_swap_b32 v88, v92\n"
  "v_mfma_f32_16x16x32_f16 v[16:19], v[224:227], v[240:243], v[16:19]\n"
  "s_waitcnt lgkmcnt(4)\n"
  "ds_read_b128 v[224:227], v2 offset:45056\n"
  "v_permlane16_swap_b32 v89, v93\n"
  "v_mfma_f32_16x16x32_f16 v[140:143], v[228:231], v[248:251], v[20:23]\n"
  "v_permlane16_swap_b32 v90, v94\n"
  "v_mfma_f32_16x16x32_f16 v[20:23], v[228:231], v[240:243], v[20:23]\n"
  "s_waitcnt lgkmcnt(2)\n"
  "ds_read_b128 v[228:231], v2 offset:46080\n"
  "v_permlane16_swap_b32 v91, v95\n"
  "v_mfma_f32_16x16x32_f16 v[144:147], v[216:219], v[248:251], v[24:27]\n"
  "v_permlane16_swap_b32 v96, v100\n"
  "v_mfma_f32_16x16x32_f16 v[24:27], v[216:219], v[240:243], v[24:27]\n"
  "s_waitcnt lgkmcnt(2)\n"
  "ds_read_b128 v[216:219], v2 offset:47104\n"
  "v_permlane16_swap_b32 v97, v101\n"
  "v_mfma_f32_16x16x32_f16 v[148:151], v[220:223], v[248:251], v[28:31]\n"
  "v_permlane16_swap_b32 v98, v102\n"
  "v_mfma_f32_16x16x32_f16 v[28:31], v[220:223], v[240:243], v[28:31]\n"
  "s_waitcnt lgkmcnt(2)\n"
  "ds_read_b128 v[220:223], v2 offset:48128\n"
  "v_permlane16_swap_b32 v99, v103\n"
  "v_mfma_f32_16x16x32_f16 v[8:11], v[224:227], v[200:203], v[8:11]\n"
  "v_mbcnt_lo_u32_b32 v252, -1, 0\n"
  "v_mov_b32 v250, 0x3f80\n"
  "v_mfma_f32_16x16x32_f16 v[128:131], v[224:227], v[120:123], v[128:131]\n"
  "s_waitcnt lgkmcnt(2)\n"
  "ds_read_b128 v[224:227], v2 offset:49152\n"
  "v_mbcnt_hi_u32_b32 v252, -1, v252\n"
  "v_mfma_f32_16x16x32_f16 v[12:15], v[228:231], v[200:203], v[12:15]\n"
  "v_mov_b32 v251, 0\n"
  "v_lshrrev_b32 v253, 4, v252\n"
  "v_mfma_f32_16x16x32_f16 v[132:135], v[228:231], v[120:123], v[132:135]\n"
  "s_waitcnt lgkmcnt(2)\n"
  "ds_read_b128 v[228:231], v2 offset:50176\n"
  "v_bfe_u32 v254, v252, 2, 2\n"
  "v_and_b32 v255, 1, v252\n"
  "v_mfma_f32_16x16x32_f16 v[16:19], v[216:219], v[200:203], v[16:19]\n"
  "v_cmp_eq_u32 vcc, v253, v254\n"
  "v_mfma_f32_16x16x32_f16 v[136:139], v[216:219], v[120:123], v[136:139]\n"
  "s_waitcnt lgkmcnt(2)\n"
  "ds_read_b128 v[216:219], v2 offset:51200\n"
  "v_lshlrev_b32 v255, 4, v255\n"
  "v_bfe_u32 v254, v252, 1, 1\n"
  "v_mfma_f32_16x16x32_f16 v[20:23], v[220:223], v[200:203], v[20:23]\n"
  "v_lshlrev_b32 v255, v255, v250\n"
  "v_mov_b32 v252, 0\n"
  "v_mfma_f32_16x16x32_f16 v[140:143], v[220:223], v[120:123], v[140:143]\n"
  "s_waitcnt lgkmcnt(2)\n"
  "ds_read_b128 v[220:223], v2 offset:52224\n"
  "v_cndmask_b32 v255, 0, v255, vcc\n"
  "v_mfma_f32_16x16x32_f16 v[24:27], v[224:227], v[200:203], v[24:27]\n"
  "v_cmp_eq_u32 vcc, 0, v254\n"
  "v_mov_b32 v250, 0\n"
  "v_mfma_f32_16x16x32_f16 v[144:147], v[224:227], v[120:123], v[144:147]\n"
  "s_waitcnt lgkmcnt(2)\n"
  "ds_read_b128 v[224:227], v2 offset:53248\n"
  "v_mov_b32 v253, 0\n"
  "v_cndmask_b32 v248, 0, v255, vcc\n"
  "v_mfma_f32_16x16x32_f16 v[28:31], v[228:231], v[200:203], v[28:31]\n"
  "v_cndmask_b32 v254, 0, v255, vcc\n"
  "v_mfma_f32_16x16x32_f16 v[148:151], v[228:231], v[120:123], v[148:151]\n"
  "s_waitcnt lgkmcnt(2)\n"
  "ds_read_b128 v[228:231], v2 offset:54272\n"
  "v_sub_u32 v249, v255, v248\n"
  "v_sub_u32 v255, v255, v248\n"
  "v_mfma_f32_16x16x32_f16 v[8:11], v[216:219], v[204:207], v[8:11]\n"
  "v_permlane16_swap_b32 v56, v60\n"
  "v_mfma_f32_16x16x32_f16 v[128:131], v[216:219], v[124:127], v[128:131]\n"
  "s_waitcnt lgkmcnt(2)\n"
  "ds_read_b128 v[216:219], v2 offset:55296\n"
  "v_permlane16_swap_b32 v57, v61\n"
  "v_mfma_f32_16x16x32_f16 v[12:15], v[220:223], v[204:207], v[12:15]\n"
  "v_permlane16_swap_b32 v58, v62\n"
  "v_mfma_f32_16x16x32_f16 v[132:135], v[220:223], v[124:127], v[132:135]\n"
  "s_waitcnt lgkmcnt(2)\n"
  "ds_read_b128 v[220:223], v2 offset:56320\n"
  "v_permlane16_swap_b32 v59, v63\n"
  "v_mfma_f32_16x16x32_f16 v[16:19], v[224:227], v[204:207], v[16:19]\n"
  "v_permlane16_swap_b32 v64, v68\n"
  "v_mfma_f32_16x16x32_f16 v[136:139], v[224:227], v[124:127], v[136:139]\n"
  "s_waitcnt lgkmcnt(2)\n"
  "ds_read_b128 v[224:227], v2 offset:57344\n"
  "v_permlane16_swap_b32 v65, v69\n"
  "v_mfma_f32_16x16x32_f16 v[20:23], v[228:231], v[204:207], v[20:23]\n"
  "v_permlane16_swap_b32 v66, v70\n"
  "v_mfma_f32_16x16x32_f16 v[140:143], v[228:231], v[124:127], v[140:143]\n"
  "s_waitcnt lgkmcnt(2)\n"
  "ds_read_b128 v[228:231], v2 offset:58368\n"
  "v_permlane16_swap_b32 v67, v71\n"
  "v_mfma_f32_16x16x32_f16 v[24:27], v[216:219], v[204:207], v[24:27]\n"
  "v_permlane16_swap_b32 v72, v76\n"
  "v_mfma_f32_16x16x32_f16 v[144:147], v[216:219], v[124:127], v[144:147]\n"
  "s_waitcnt lgkmcnt(2)\n"
  "ds_read_b128 v[216:219], v2 offset:59392\n"
  "v_permlane16_swap_b32 v73, v77\n"
  "v_mfma_f32_16x16x32_f16 v[28:31], v[220:223], v[204:207], v[28:31]\n"
  "v_permlane16_swap_b32 v74, v78\n"
  "v_mfma_f32_16x16x32_f16 v[148:151], v[220:223], v[124:127], v[148:151]\n"
  "s_waitcnt lgkmcnt(2)\n"
  "ds_read_b128 v[220:223], v2 offset:60416\n"
  "v_permlane16_swap_b32 v75, v79\n"
  "v_mfma_f32_16x16x32_f16 v[8:11], v[224:227], v[232:235], v[8:11]\n"
  "v_permlane16_swap_b32 v32, v36\n"
  "v_mfma_f32_16x16x32_f16 v[128:131], v[224:227], v[164:167], v[128:131]\n"
  "s_waitcnt lgkmcnt(2)\n"
  "ds_read_b128 v[224:227], v2 offset:61440\n"
  "v_permlane16_swap_b32 v33, v37\n"
  "v_mfma_f32_16x16x32_f16 v[12:15], v[228:231], v[232:235], v[12:15]\n"
  "v_permlane16_swap_b32 v34, v38\n"
  "v_mfma_f32_16x16x32_f16 v[132:135], v[228:231], v[164:167], v[132:135]\n"
  "s_waitcnt lgkmcnt(2)\n"
  "ds_read_b128 v[228:231], v2 offset:62464\n"
  "v_permlane16_swap_b32 v35, v39\n"
  "v_mfma_f32_16x16x32_f16 v[16:19], v[216:219], v[232:235], v[16:19]\n"
  "v_permlane16_swap_b32 v40, v44\n"
  "v_mfma_f32_16x16x32_f16 v[136:139], v[216:219], v[164:167], v[136:139]\n"
  "s_waitcnt lgkmcnt(2)\n"
  "ds_read_b128 v[216:219], v2 offset:63488\n"
  "v_permlane16_swap_b32 v41, v45\n"
  "v_mfma_f32_16x16x32_f16 v[20:23], v[220:223], v[232:235], v[20:23]\n"
  "v_permlane16_swap_b32 v42, v46\n"
  "v_mfma_f32_16x16x32_f16 v[140:143], v[220:223], v[164:167], v[140:143]\n"
  "s_waitcnt lgkmcnt(2)\n"
  "ds_read_b128 v[220:223], v2 offset:64512\n"
  "v_permlane16_swap_b32 v43, v47\n"
  "v_mfma_f32_16x16x32_f16 v[24:27], v[224:227], v[232:235], v[24:27]\n"
  "v_permlane16_swap_b32 v48, v52\n"
  "v_mfma_f32_16x16x32_f16 v[144:147], v[224:227], v[164:167], v[144:147]\n"
  "s_waitcnt lgkmcnt(2)\n"
  "ds_read_b128 v[224:227], v3 offset:0\n"
  "v_permlane16_swap_b32 v49, v53\n"
  "v_mfma_f32_16x16x32_f16 v[28:31], v[228:231], v[232:235], v[28:31]\n"
  "v_permlane16_swap_b32 v50, v54\n"
  "v_mfma_f32_16x16x32_f16 v[148:151], v[228:231], v[164:167], v[148:151]\n"
  "s_waitcnt lgkmcnt(2)\n"
  "ds_read_b128 v[228:231], v3 offset:1024\n"
  "buffer_load_dwordx4 v[200:203], v5, s[40:43], s52 offen offset:0 \n"
  "buffer_load_dwordx4 v[204:207], v5, s[40:43], s52 offen offset:64 \n"
  "buffer_load_dwordx4 v[232:235], v5, s[40:43], s52 offen offset:128 \n"
  "v_permlane16_swap_b32 v51, v55\n"
  "v_mfma_f32_16x16x32_f16 v[8:11], v[216:219], v[236:239], v[8:11]\n"
  "v_lshlrev_b32 v208, 16, v56\n"
  "v_mfma_f32_16x16x32_f16 v[128:131], v[216:219], v[172:175], v[128:131]\n"
  "s_waitcnt lgkmcnt(2)\n"
  "ds_read_b128 v[216:219], v3 offset:2048\n"
  "v_lshlrev_b32 v210, 16, v57\n"
  "v_mfma_f32_16x16x32_f16 v[12:15], v[220:223], v[236:239], v[12:15]\n"
  "v_lshlrev_b32 v212, 16, v58\n"
  "v_mfma_f32_16x16x32_f16 v[132:135], v[220:223], v[172:175], v[132:135]\n"
  "s_waitcnt lgkmcnt(2)\n"
  "ds_read_b128 v[220:223], v3 offset:3072\n"
  "v_lshlrev_b32 v214, 16, v59\n"
  "v_mfma_f32_16x16x32_f16 v[16:19], v[224:227], v[236:239], v[16:19]\n"
  "v_and_b32 v209, 0xffff0000, v56\n"
  "v_mfma_f32_16x16x32_f16 v[136:139], v[224:227], v[172:175], v[136:139]\n"
  "s_waitcnt lgkmcnt(2)\n"
  "ds_read_b128 v[224:227], v3 offset:4096\n"
  "v_and_b32 v211, 0xffff0000, v57\n"
  "v_mfma_f32_16x16x32_f16 v[20:23], v[228:231], v[236:239], v[20:23]\n"
  "v_and_b32 v213, 0xffff0000, v58\n"
  "v_mfma_f32_16x16x32_f16 v[140:143], v[228:231], v[172:175], v[140:143]\n"
  "s_waitcnt lgkmcnt(2)\n"
  "ds_read_b128 v[228:231], v3 offset:5120\n"
  "v_and_b32 v215, 0xffff0000, v59\n"
  "v_mfma_f32_16x16x32_f16 v[24:27], v[216:219], v[236:239], v[24:27]\n"
  "v_mul_f32 v208, s51, v208\n"
  "v_mfma_f32_16x16x32_f16 v[144:147], v[216:219], v[172:175], v[144:147]\n"
  "s_waitcnt lgkmcnt(2)\n"
  "ds_read_b128 v[216:219], v3 offset:6144\n"
  "v_mul_f32 v209, s51, v209\n"
  "v_mfma_f32_16x16x32_f16 v[28:31], v[220:223], v[236:239], v[28:31]\n"
  "v_mul_f32 v210, s51, v210\n"
  "v_mfma_f32_16x16x32_f16 v[148:151], v[220:223], v[172:175], v[148:151]\n"
  "s_waitcnt lgkmcnt(2)\n"
  "ds_read_b128 v[220:223], v3 offset:7168\n"
  "buffer_load_dwordx4 v[236:239], v5, s[40:43], s60 offen offset:0 \n"
  "buffer_load_dwordx4 v[240:243], v5, s[40:43], s60 offen offset:64 \n"
  "buffer_load_dwordx4 v[244:247], v5, s[40:43], s60 offen offset:128 \n"
  "v_mul_f32 v211, s51, v211\n"
  "v_mfma_f32_16x16x32_f16 v[8:11], v[224:227], v[156:159], v[8:11]\n"
  "v_mul_f32 v212, s51, v212\n"
  "v_mfma_f32_16x16x32_f16 v[128:131], v[224:227], v[180:183], v[128:131]\n"
  "s_waitcnt lgkmcnt(2)\n"
  "ds_read_b128 v[224:227], v3 offset:8192\n"
  "v_mul_f32 v213, s51, v213\n"
  "v_mfma_f32_16x16x32_f16 v[12:15], v[228:231], v[156:159], v[12:15]\n"
  "v_mul_f32 v214, s51, v214\n"
  "v_mfma_f32_16x16x32_f16 v[132:135], v[228:231], v[180:183], v[132:135]\n"
  "s_waitcnt lgkmcnt(2)\n"
  "ds_read_b128 v[228:231], v3 offset:9216\n"
  "v_mul_f32 v215, s51, v215\n"
  "v_mfma_f32_16x16x32_f16 v[16:19], v[216:219], v[156:159], v[16:19]\n"
  "v_cvt_pk_bf16_f32 v56, v208, v209\n"
  "v_mfma_f32_16x16x32_f16 v[136:139], v[216:219], v[180:183], v[136:139]\n"
  "s_waitcnt lgkmcnt(2)\n"
  "ds_read_b128 v[216:219], v3 offset:16384\n"
  "v_cvt_pk_bf16_f32 v57, v210, v211\n"
  "v_mfma_f32_16x16x32_f16 v[20:23], v[220:223], v[156:159], v[20:23]\n"
  "v_cvt_pk_bf16_f32 v58, v212, v213\n"
  "v_mfma_f32_16x16x32_f16 v[140:143], v[220:223], v[180:183], v[140:143]\n"
  "s_waitcnt lgkmcnt(2)\n"
  "ds_read_b128 v[220:223], v3 offset:17408\n"
  "v_cvt_pk_bf16_f32 v59, v214, v215\n"
  "v_mfma_f32_16x16x32_f16 v[24:27], v[224:227], v[156:159], v[24:27]\n"
  "v_lshlrev_b32 v208, 16, v64\n"
  "v_mfma_f32_16x16x32_f16 v[144:147], v[224:227], v[180:183], v[144:147]\n"
  "s_waitcnt lgkmcnt(2)\n"
  "ds_read_b128 v[224:227], v3 offset:18432\n"
  "v_lshlrev_b32 v210, 16, v65\n"
  "v_mfma_f32_16x16x32_f16 v[28:31], v[228:231], v[156:159], v[28:31]\n"
  "v_lshlrev_b32 v212, 16, v66\n"
  "v_mfma_f32_16x16x32_f16 v[148:151], v[228:231], v[180:183], v[148:151]\n"
  "s_waitcnt lgkmcnt(2)\n"
  "ds_read_b128 v[228:231], v3 offset:19456\n"
  "v_lshlrev_b32 v214, 16, v67\n"
  "v_mfma_f32_16x16x32_f16 v[8:11], v[216:219], v[152:155], v[8:11]\n"
  "v_and_b32 v209, 0xffff0000, v64\n"
  "v_mfma_f32_16x16x32_f16 v[128:131], v[216:219], v[160:163], v[128:131]\n"
  "s_waitcnt lgkmcnt(2)\n"
  "ds_read_b128 v[216:219], v3 offset:20480\n"
  "v_and_b32 v211, 0xffff0000, v65\n"
  "v_mfma_f32_16x16x32_f16 v[12:15], v[220:223], v[152:155], v[12:15]\n"
  "v_and_b32 v213, 0xffff0000, v66\n"
  "v_mfma_f32_16x16x32_f16 v[132:135], v[220:223], v[160:163], v[132:135]\n"
  "s_waitcnt lgkmcnt(2)\n"
  "ds_read_b128 v[220:223], v3 offset:21504\n"
  "v_and_b32 v215, 0xffff0000, v67\n"
  "v_mfma_f32_16x16x32_f16 v[16:19], v[224:227], v[152:155], v[16:19]\n"
  "v_mul_f32 v208, s51, v208\n"
  "v_mfma_f32_16x16x32_f16 v[136:139], v[224:227], v[160:163], v[136:139]\n"
  "s_waitcnt lgkmcnt(2)\n"
  "ds_read_b128 v[224:227], v3 offset:10240\n"
  "v_mul_f32 v209, s51, v209\n"
  "v_mfma_f32_16x16x32_f16 v[20:23], v[228:231], v[152:155], v[20:23]\n"
  "v_mul_f32 v210, s51, v210\n"
  "v_mfma_f32_16x16x32_f16 v[140:143], v[228:231], v[160:163], v[140:143]\n"
  "s_waitcnt lgkmcnt(2)\n"
  "ds_read_b128 v[228:231], v3 offset:11264\n"
  "v_mul_f32 v211, s51, v211\n"
  "v_mfma_f32_16x16x32_f16 v[24:27], v[216:219], v[152:155], v[24:27]\n"
  "v_mul_f32 v212, s51, v212\n"
  "v_mfma_f32_16x16x32_f16 v[144:147], v[216:219], v[160:163], v[144:147]\n"
  "s_waitcnt lgkmcnt(2)\n"
  "ds_read_b128 v[216:219], v3 offset:12288\n"
  "v_mul_f32 v213, s51, v213\n"
  "v_mfma_f32_16x16x32_f16 v[28:31], v[220:223], v[152:155], v[28:31]\n"
  "v_mul_f32 v214, s51, v214\n"
  "v_mfma_f32_16x16x32_f16 v[148:151], v[220:223], v[160:163], v[148:151]\n"
  "s_waitcnt lgkmcnt(2)\n"
  "ds_read_b128 v[220:223], v3 offset:13312\n"
  "v_mul_f32 v215, s51, v215\n"
  "v_mfma_f32_16x16x32_f16 v[8:11], v[224:227], v[168:171], v[8:11]\n"
  "v_cvt_pk_bf16_f32 v64, v208, v209\n"
  "v_mfma_f32_16x16x32_f16 v[128:131], v[224:227], v[176:179], v[128:131]\n"
  "s_waitcnt lgkmcnt(2)\n"
  "ds_read_b128 v[224:227], v3 offset:14336\n"
  "v_cvt_pk_bf16_f32 v65, v210, v211\n"
  "v_mfma_f32_16x16x32_f16 v[12:15], v[228:231], v[168:171], v[12:15]\n"
  "v_cvt_pk_bf16_f32 v66, v212, v213\n"
  "v_mfma_f32_16x16x32_f16 v[132:135], v[228:231], v[176:179], v[132:135]\n"
  "s_waitcnt lgkmcnt(2)\n"
  "ds_read_b128 v[228:231], v3 offset:15360\n"
  "v_cvt_pk_bf16_f32 v67, v214, v215\n"
  "v_mfma_f32_16x16x32_f16 v[16:19], v[216:219], v[168:171], v[16:19]\n"
  "v_lshlrev_b32 v208, 16, v72\n"
  "v_mfma_f32_16x16x32_f16 v[136:139], v[216:219], v[176:179], v[136:139]\n"
  "s_waitcnt lgkmcnt(2)\n"
  "ds_read_b128 v[216:219], v2 offset:20480\n"
  "v_lshlrev_b32 v210, 16, v73\n"
  "v_mfma_f32_16x16x32_f16 v[20:23], v[220:223], v[168:171], v[20:23]\n"
  "v_lshlrev_b32 v212, 16, v74\n"
  "v_mfma_f32_16x16x32_f16 v[140:143], v[220:223], v[176:179], v[140:143]\n"
  "s_waitcnt lgkmcnt(2)\n"
  "ds_read_b128 v[220:223], v2 offset:21504\n"
  "v_lshlrev_b32 v214, 16, v75\n"
  "v_mfma_f32_16x16x32_f16 v[24:27], v[224:227], v[168:171], v[24:27]\n"
  "v_and_b32 v209, 0xffff0000, v72\n"
  "v_mfma_f32_16x16x32_f16 v[144:147], v[224:227], v[176:179], v[144:147]\n"
  "s_waitcnt lgkmcnt(2)\n"
  "ds_read_b128 v[224:227], v2 offset:22528\n"
  "v_and_b32 v211, 0xffff0000, v73\n"
  "v_mfma_f32_16x16x32_f16 v[28:31], v[228:231], v[168:171], v[28:31]\n"
  "v_and_b32 v213, 0xffff0000, v74\n"
  "v_mfma_f32_16x16x32_f16 v[148:151], v[228:231], v[176:179], v[148:151]\n"
  "s_waitcnt lgkmcnt(2)\n"
  "ds_read_b128 v[228:231], v2 offset:23552\n"
  "v_and_b32 v215, 0xffff0000, v75\n"
  "v_mfma_f32_16x16x32_f16 v[8:11], v[216:219], v[184:187], v[8:11]\n"
  "v_mul_f32 v208, s51, v208\n"
  "v_mfma_f32_16x16x32_f16 v[128:131], v[216:219], v[104:107], v[128:131]\n"
  "s_waitcnt lgkmcnt(2)\n"
  "ds_read_b128 v[216:219], v2 offset:24576\n"
  "v_mfma_f32_16x16x32_f16 v[12:15], v[220:223], v[184:187], v[12:15]\n"
  "v_mul_f32 v209, s51, v209\n"
  "v_mfma_f32_16x16x32_f16 v[132:135], v[220:223], v[104:107], v[132:135]\n"
  "s_waitcnt lgkmcnt(2)\n"
  "ds_read_b128 v[220:223], v2 offset:25600\n"
  "v_mul_f32 v210, s51, v210\n"
  "v_mfma_f32_16x16x32_f16 v[16:19], v[224:227], v[184:187], v[16:19]\n"
  "v_mfma_f32_16x16x32_f16 v[136:139], v[224:227], v[104:107], v[136:139]\n"
  "s_waitcnt lgkmcnt(2)\n"
  "ds_read_b128 v[224:227], v0 offset:6144\n"
  "v_mul_f32 v211, s51, v211\n"
  "v_mfma_f32_16x16x32_f16 v[20:23], v[228:231], v[184:187], v[20:23]\n"
  "v_mul_f32 v212, s51, v212\n"
  "v_mfma_f32_16x16x32_f16 v[140:143], v[228:231], v[104:107], v[140:143]\n"
  "s_waitcnt lgkmcnt(2)\n"
  "ds_read_b128 v[228:231], v0 offset:8192\n"
  "v_mfma_f32_16x16x32_f16 v[24:27], v[216:219], v[184:187], v[24:27]\n"
  "v_mul_f32 v213, s51, v213\n"
  "v_mfma_f32_16x16x32_f16 v[144:147], v[216:219], v[104:107], v[144:147]\n"
  "s_waitcnt lgkmcnt(2)\n"
  "ds_read_b128 v[216:219], v0 offset:10240\n"
  "v_mul_f32 v214, s51, v214\n"
  "v_mfma_f32_16x16x32_f16 v[28:31], v[220:223], v[184:187], v[28:31]\n"
  "v_mfma_f32_16x16x32_f16 v[148:151], v[220:223], v[104:107], v[148:151]\n"
  "ds_read_b128 v[220:223], v2 offset:26624\n"
  "v_mul_f32 v215, s51, v215\n"
  "s_waitcnt vmcnt(5)\n"
  "v_mfma_f32_16x16x32_bf16 v[8:11], v[248:251], v[200:203], v[8:11]\n"
  "v_cvt_pk_bf16_f32 v72, v208, v209\n"
  "v_mfma_f32_16x16x32_bf16 v[12:15], v[252:255], v[200:203], v[12:15]\n"
  "s_waitcnt vmcnt(4)\n"
  "v_mfma_f32_16x16x32_bf16 v[16:19], v[248:251], v[204:207], v[16:19]\n"
  "v_cvt_pk_bf16_f32 v73, v210, v211\n"
  "v_mfma_f32_16x16x32_bf16 v[20:23], v[252:255], v[204:207], v[20:23]\n"
  "v_cvt_pk_bf16_f32 v74, v212, v213\n"
  "s_waitcnt vmcnt(3)\n"
  "v_mfma_f32_16x16x32_bf16 v[24:27], v[248:251], v[232:235], v[24:27]\n"
  "v_mfma_f32_16x16x32_bf16 v[28:31], v[252:255], v[232:235], v[28:31]\n"
  "s_waitcnt lgkmcnt(3)\n"
  "buffer_load_dwordx4 v[200:203], v5, s[40:43], s53 offen offset:0 \n"
  "buffer_load_dwordx4 v[204:207], v5, s[40:43], s53 offen offset:64 \n"
  "buffer_load_dwordx4 v[232:235], v5, s[40:43], s53 offen offset:128 \n"
  "v_cvt_pk_bf16_f32 v75, v214, v215\n"
  "v_mfma_f32_16x16x32_bf16 v[152:155], v[224:227], v[80:83], 0\n"
  "v_mfma_f32_16x16x32_bf16 v[156:159], v[224:227], v[84:87], 0\n"
  "s_waitcnt lgkmcnt(2)\n"
  "v_mfma_f32_16x16x32_bf16 v[160:163], v[224:227], v[60:63], 0\n"
  "ds_read_b128 v[224:227], v2 offset:27648\n"
  "v_mfma_f32_16x16x32_bf16 v[152:155], v[228:231], v[88:91], v[152:155]\n"
  "v_mfma_f32_16x16x32_bf16 v[156:159], v[228:231], v[92:95], v[156:159]\n"
  "v_mfma_f32_16x16x32_bf16 v[160:163], v[228:231], v[68:71], v[160:163]\n"
  "s_waitcnt lgkmcnt(2)\n"
  "ds_read_b128 v[228:231], v2 offset:28672\n"
  "v_mfma_f32_16x16x32_bf16 v[152:155], v[216:219], v[96:99], v[152:155]\n"
  "v_mfma_f32_16x16x32_bf16 v[156:159], v[216:219], v[100:103], v[156:159]\n"
  "s_waitcnt lgkmcnt(2)\n"
  "v_mfma_f32_16x16x32_bf16 v[160:163], v[216:219], v[76:79], v[160:163]\n"
  "ds_read_b128 v[216:219], v2 offset:29696\n"
  "v_mfma_f32_16x16x32_f16 v[8:11], v[220:223], v[188:191], v[8:11]\n"
  "s_waitcnt lgkmcnt(2)\n"
  "v_mfma_f32_16x16x32_f16 v[128:131], v[220:223], v[108:111], v[128:131]\n"
  "ds_read_b128 v[220:223], v2 offset:30720\n"
  "v_mfma_f32_16x16x32_f16 v[12:15], v[224:227], v[188:191], v[12:15]\n"
  "s_waitcnt lgkmcnt(2)\n"
  "v_mfma_f32_16x16x32_f16 v[132:135], v[224:227], v[108:111], v[132:135]\n"
  "ds_read_b128 v[224:227], v2 offset:31744\n"
  "v_mfma_f32_16x16x32_f16 v[16:19], v[228:231], v[188:191], v[16:19]\n"
  "s_waitcnt lgkmcnt(2)\n"
  "v_mfma_f32_16x16x32_f16 v[136:139], v[228:231], v[108:111], v[136:139]\n"
  "ds_read_b128 v[228:231], v2 offset:32768\n"
  "v_mfma_f32_16x16x32_f16 v[20:23], v[216:219], v[188:191], v[20:23]\n"
  "s_waitcnt lgkmcnt(2)\n"
  "v_mfma_f32_16x16x32_f16 v[140:143], v[216:219], v[108:111], v[140:143]\n"
  "ds_read_b128 v[216:219], v2 offset:33792\n"
  "v_mfma_f32_16x16x32_f16 v[24:27], v[220:223], v[188:191], v[24:27]\n"
  "s_waitcnt lgkmcnt(2)\n"
  "v_mfma_f32_16x16x32_f16 v[144:147], v[220:223], v[108:111], v[144:147]\n"
  "ds_read_b128 v[220:223], v2 offset:34816\n"
  "v_mfma_f32_16x16x32_f16 v[28:31], v[224:227], v[188:191], v[28:31]\n"
  "v_mfma_f32_16x16x32_f16 v[148:151], v[224:227], v[108:111], v[148:151]\n"
  "ds_read_b128 v[224:227], v2 offset:35840\n"
  "s_waitcnt vmcnt(5)\n"
  "v_mfma_f32_16x16x32_bf16 v[128:131], v[248:251], v[236:239], v[128:131]\n"
  "v_mfma_f32_16x16x32_bf16 v[132:135], v[252:255], v[236:239], v[132:135]\n"
  "s_waitcnt vmcnt(4)\n"
  "v_mfma_f32_16x16x32_bf16 v[136:139], v[248:251], v[240:243], v[136:139]\n"
  "v_mfma_f32_16x16x32_bf16 v[140:143], v[252:255], v[240:243], v[140:143]\n"
  "s_waitcnt vmcnt(3)\n"
  "v_mfma_f32_16x16x32_bf16 v[144:147], v[248:251], v[244:247], v[144:147]\n"
  "s_waitcnt lgkmcnt(2)\n"
  "v_mfma_f32_16x16x32_bf16 v[148:151], v[252:255], v[244:247], v[148:151]\n"
  "buffer_load_dwordx4 v[236:239], v5, s[40:43], s61 offen offset:0 \n"
  "buffer_load_dwordx4 v[240:243], v5, s[40:43], s61 offen offset:64 \n"
  "buffer_load_dwordx4 v[244:247], v5, s[40:43], s61 offen offset:128 \n"
  "s_setprio 3\n"
  "v_mov_b32 v208, 163328\n"
  "ds_read_b128 v[208:211], v208\n"
  "s_waitcnt lgkmcnt(0)\n"
  "v_readfirstlane_b32 s94, v209\n"
  "v_readfirstlane_b32 s92, v210\n"
  "v_cvt_f32_f16 v208, v209\n"
  "v_mul_f32 v208, v208, v210\n"
  "s_nop 0\n"
  "v_readfirstlane_b32 s93, v208\n"
  "s_cmp_lt_u32 s94, 0x1800\n"
  "s_cselect_b32 s95, 2, 0\n"
  "s_cmp_ge_u32 s94, 0x7c00\n"
  "s_cselect_b32 s95, 2, s95\n"
  "s_cmp_ge_u32 s92, 0x47000000\n"
  "s_cselect_b32 s95, 2, s95\n"
  "s_cmp_ge_u32 s93, 0x47000000\n"
  "s_cselect_b32 s95, 2, s95\n"
  "s_cmp_lg_u32 s95, 0\n"
  "s_cbranch_scc1 .Lexit%=\n"
  "s_branch .Lg0%=\n"
  ".p2alignl 6, 0xBF800000\n"
  ".Lg0%=:\n"
  "v_mfma_f32_16x16x32_f16 v[8:11], v[228:231], v[192:195], v[8:11]\n"
  "v_lshlrev_b32_sdwa v156, 1, v156 dst_sel:DWORD dst_unused:UNUSED_PAD src0_sel:DWORD src1_sel:WORD_1\n"
  "v_mfma_f32_16x16x32_f16 v[128:131], v[228:231], v[112:115], v[128:131]\n"
  "ds_read_b128 v[228:231], v2 offset:36864\n"
  "v_lshlrev_b32_sdwa v157, 1, v157 dst_sel:DWORD dst_unused:UNUSED_PAD src0_sel:DWORD src1_sel:WORD_1\n"
  "v_mfma_f32_16x16x32_f16 v[12:15], v[216:219], v[192:195], v[12:15]\n"
  "s_waitcnt lgkmcnt(2)\n"
  "ds_read_u16 v156, v156\n"
  "v_lshlrev_b32_sdwa v158, 1, v158 dst_sel:DWORD dst_unused:UNUSED_PAD src0_sel:DWORD src1_sel:WORD_1\n"
  "v_mfma_f32_16x16x32_f16 v[132:135], v[216:219], v[112:115], v[132:135]\n"
  "ds_read_b128 v[216:219], v2 offset:37888\n"
  "v_cvt_pk_f16_f32 v184, v152, v153\n"
  "v_cvt_pk_f16_f32 v185, v154, v155\n"
  "v_mfma_f32_16x16x32_f16 v[16:19], v[220:223], v[192:195], v[16:19]\n"
  "v_pk_fma_f16 v208, v184, v184, s56\n"
  "v_mfma_f32_16x16x32_f16 v[136:139], v[220:223], v[112:115], v[136:139]\n"
  "s_waitcnt lgkmcnt(2)\n"
  "ds_read_b128 v[220:223], v0 offset:6400\n"
  "ds_read_u16_d16_hi v157, v157\n"
  "v_pk_fma_f16 v209, v185, v185, s56\n"
  "v_mfma_f32_16x16x32_f16 v[20:23], v[224:227], v[192:195], v[20:23]\n"
  "v_lshlrev_b32_sdwa v159, 1, v159 dst_sel:DWORD dst_unused:UNUSED_PAD src0_sel:DWORD src1_sel:WORD_1\n"
  "v_mfma_f32_16x16x32_f16 v[140:143], v[224:227], v[112:115], v[140:143]\n"
  "ds_read_b128 v[224:227], v0 offset:8448\n"
  "ds_read_u16 v158, v158\n"
  "v_lshlrev_b32_sdwa v160, 1, v160 dst_sel:DWORD dst_unused:UNUSED_PAD src0_sel:DWORD src1_sel:WORD_1\n"
  "v_mfma_f32_16x16x32_f16 v[24:27], v[228:231], v[192:195], v[24:27]\n"
  "s_waitcnt lgkmcnt(4)\n"
  "v_pk_mul_f16 v208, v184, v208\n"
  "v_mfma_f32_16x16x32_f16 v[144:147], v[228:231], v[112:115], v[144:147]\n"
  "ds_read_b128 v[228:231], v0 offset:10496\n"
  "ds_read_u16_d16_hi v159, v159\n"
  "v_pk_mul_f16 v209, v185, v209\n"
  "v_lshlrev_b32_sdwa v161, 1, v161 dst_sel:DWORD dst_unused:UNUSED_PAD src0_sel:DWORD src1_sel:WORD_1\n"
  "v_mfma_f32_16x16x32_f16 v[28:31], v[216:219], v[192:195], v[28:31]\n"
  "v_lshlrev_b32_sdwa v162, 1, v162 dst_sel:DWORD dst_unused:UNUSED_PAD src0_sel:DWORD src1_sel:WORD_1\n"
  "v_mfma_f32_16x16x32_f16 v[148:151], v[216:219], v[112:115], v[148:151]\n"
  "s_waitcnt lgkmcnt(5)\n"
  "ds_read_b128 v[216:219], v2 offset:38912\n"
  "ds_read_u16 v160, v160\n"
  "v_exp_f16 v212, v208\n"
  "v_mfma_f32_16x16x32_bf16 v[168:171], v[220:223], v[80:83], 0\n"
  "v_mfma_f32_16x16x32_bf16 v[172:175], v[220:223], v[84:87], 0\n"
  "ds_read_u16_d16_hi v161, v161\n"
  "v_exp_f16 v213, v209\n"
  "v_mfma_f32_16x16x32_bf16 v[176:179], v[220:223], v[60:63], 0\n"
  "s_waitcnt lgkmcnt(6)\n"
  "ds_read_b128 v[220:223], v2 offset:39936\n"
  "v_mfma_f32_16x16x32_bf16 v[168:171], v[224:227], v[88:91], v[168:171]\n"
  "v_lshlrev_b32_sdwa v163, 1, v163 dst_sel:DWORD dst_unused:UNUSED_PAD src0_sel:DWORD src1_sel:WORD_1\n"
  "v_mfma_f32_16x16x32_bf16 v[172:175], v[224:227], v[92:95], v[172:175]\n"
  "ds_read_u16 v162, v162\n"
  "v_exp_f16_sdwa v212, v208 dst_sel:WORD_1 dst_unused:UNUSED_PRESERVE src0_sel:WORD_1\n"
  "v_mfma_f32_16x16x32_bf16 v[176:179], v[224:227], v[68:71], v[176:179]\n"
  "s_waitcnt lgkmcnt(6)\n"
  "ds_read_b128 v[224:227], v2 offset:40960\n"
  "v_exp_f16_sdwa v213, v209 dst_sel:WORD_1 dst_unused:UNUSED_PRESERVE src0_sel:WORD_1\n"
  "v_mfma_f32_16x16x32_bf16 v[168:171], v[228:231], v[96:99], v[168:171]\n"
  "ds_read_u16_d16_hi v163, v163\n"
  "v_mfma_f32_16x16x32_bf16 v[172:175], v[228:231], v[100:103], v[172:175]\n"
  "v_pk_add_f16 v212, v212, 1.0 op_sel_hi:[1,0]\n"
  "v_mfma_f32_16x16x32_bf16 v[176:179], v[228:231], v[76:79], v[176:179]\n"
  "s_waitcnt lgkmcnt(3)\n"
  "ds_read_b128 v[228:231], v2 offset:41984\n"
  "v_pk_add_f16 v213, v213, 1.0 op_sel_hi:[1,0]\n"
  "v_mfma_f32_16x16x32_f16 v[8:11], v[216:219], v[196:199], v[8:11]\n"
  "v_rcp_f16 v208, v212\n"
  "v_mfma_f32_16x16x32_f16 v[128:131], v[216:219], v[116:119], v[128:131]\n"
  "ds_read_b128 v[216:219], v2 offset:43008\n"
  "v_mfma_f32_16x16x32_f16 v[12:15], v[220:223], v[196:199], v[12:15]\n"
  "s_waitcnt lgkmcnt(3)\n"
  "v_rcp_f16 v209, v213\n"
  "v_mfma_f32_16x16x32_f16 v[132:135], v[220:223], v[116:119], v[132:135]\n"
  "ds_read_b128 v[220:223], v2 offset:44032\n"
  "v_rcp_f16_sdwa v208, v212 dst_sel:WORD_1 dst_unused:UNUSED_PRESERVE src0_sel:WORD_1\n"
  "v_mfma_f32_16x16x32_f16 v[16:19], v[224:227], v[196:199], v[16:19]\n"
  "v_mfma_f32_16x16x32_f16 v[136:139], v[224:227], v[116:119], v[136:139]\n"
  "s_waitcnt lgkmcnt(1)\n"
  "ds_read_b128 v[224:227], v0 offset:12288\n"
  "v_rcp_f16_sdwa v209, v213 dst_sel:WORD_1 dst_unused:UNUSED_PRESERVE src0_sel:WORD_1\n"
  "v_mfma_f32_16x16x32_f16 v[20:23], v[228:231], v[196:199], v[20:23]\n"
  "v_mfma_f32_16x16x32_f16 v[140:143], v[228:231], v[116:119], v[140:143]\n"
  "ds_read_b128 v[228:231], v0 offset:14336\n"
  "v_or_b32 v188, v156, v157\n"
  "v_mfma_f32_16x16x32_f16 v[24:27], v[216:219], v[196:199], v[24:27]\n"
  "s_waitcnt lgkmcnt(2)\n"
  "v_pk_mul_f16 v184, v184, v208\n"
  "v_pk_mul_f16 v185, v185, v209\n"
  "v_mfma_f32_16x16x32_f16 v[144:147], v[216:219], v[116:119], v[144:147]\n"
  "ds_read_b128 v[216:219], v0 offset:16384\n"
  "v_or_b32 v189, v158, v159\n"
  "v_mfma_f32_16x16x32_f16 v[28:31], v[220:223], v[196:199], v[28:31]\n"
  "v_or_b32 v164, v160, v161\n"
  "v_mfma_f32_16x16x32_f16 v[148:151], v[220:223], v[116:119], v[148:151]\n"
  "s_waitcnt lgkmcnt(2)\n"
  "ds_read_b128 v[220:223], v0 offset:12544\n"
  "ds_read_b128 v[104:107], v4 offset:0\n"
  "ds_read_b128 v[108:111], v4 offset:16\n"
  "ds_read_b128 v[112:115], v4 offset:128\n"
  "ds_read_b128 v[116:119], v4 offset:144\n"
  "ds_read_b128 v[120:123], v4 offset:256\n"
  "ds_read_b128 v[124:127], v4 offset:272\n"
  "v_or_b32 v165, v162, v163\n"
  "v_mfma_f32_16x16x32_bf16 v[152:155], v[224:227], v[80:83], 0\n"
  "v_lshlrev_b32_sdwa v172, 1, v172 dst_sel:DWORD dst_unused:UNUSED_PAD src0_sel:DWORD src1_sel:WORD_1\n"
  "v_lshlrev_b32_sdwa v173, 1, v173 dst_sel:DWORD dst_unused:UNUSED_PAD src0_sel:DWORD src1_sel:WORD_1\n"
  "v_lshlrev_b32_sdwa v174, 1, v174 dst_sel:DWORD dst_unused:UNUSED_PAD src0_sel:DWORD src1_sel:WORD_1\n"
  "v_cvt_pk_f16_f32 v186, v168, v169\n"
  "v_cvt_pk_f16_f32 v187, v170, v171\n"
  "v_pk_fma_f16 v208, v186, v186, s56\n"
  "v_pk_fma_f16 v209, v187, v187, s56\n"
  "v_mfma_f32_16x16x32_bf16 v[156:159], v[224:227], v[84:87], 0\n"
  "ds_read_u16 v172, v172\n"
  "v_lshlrev_b32_sdwa v175, 1, v175 dst_sel:DWORD dst_unused:UNUSED_PAD src0_sel:DWORD src1_sel:WORD_1\n"
  "v_cvt_pk_bf16_f32 v8, v8, v9\n"
  "v_cvt_pk_bf16_f32 v9, v10, v11\n"
  "v_cvt_pk_bf16_f32 v10, v12, v13\n"
  "v_cvt_pk_bf16_f32 v11, v14, v15\n"
  "v_cvt_pk_bf16_f32 v16, v16, v17\n"
  "v_cvt_pk_bf16_f32 v17, v18, v19\n"
  "v_mfma_f32_16x16x32_bf16 v[160:163], v[224:227], v[60:63], 0\n"
  "s_waitcnt lgkmcnt(9)\n"
  "ds_read_b128 v[224:227], v0 offset:14592\n"
  "ds_read_u16_d16_hi v173, v173\n"
  "ds_read_u16 v174, v174\n"
  "v_cvt_pk_bf16_f32 v18, v20, v21\n"
  "v_cvt_pk_bf16_f32 v19, v22, v23\n"
  "v_cvt_pk_bf16_f32 v24, v24, v25\n"
  "v_cvt_pk_bf16_f32 v25, v26, v27\n"
  "v_cvt_pk_bf16_f32 v26, v28, v29\n"
  "v_cvt_pk_bf16_f32 v27, v30, v31\n"
  "buffer_store_dwordx4 v[8:11], v5, s[44:47], s52 offen offset:0 sc1\n"
  "buffer_store_dwordx4 v[16:19], v5, s[44:47], s52 offen offset:64 sc1\n"
  "buffer_store_dwordx4 v[24:27], v5, s[44:47], s52 offen offset:128 sc1\n"
  "v_lshlrev_b32_sdwa v176, 1, v176 dst_sel:DWORD dst_unused:UNUSED_PAD src0_sel:DWORD src1_sel:WORD_1\n"
  "v_mfma_f32_16x16x32_bf16 v[152:155], v[228:231], v[88:91], v[152:155]\n"
  "ds_read_u16_d16_hi v175, v175\n"
  "ds_read_u16 v176, v176\n"
  "v_pk_mul_f16 v208, v186, v208\n"
  "v_pk_mul_f16 v209, v187, v209\n"
  "v_lshlrev_b32_sdwa v177, 1, v177 dst_sel:DWORD dst_unused:UNUSED_PAD src0_sel:DWORD src1_sel:WORD_1\n"
  "v_lshlrev_b32_sdwa v178, 1, v178 dst_sel:DWORD dst_unused:UNUSED_PAD src0_sel:DWORD src1_sel:WORD_1\n"
  "v_exp_f16 v212, v208\n"
  "v_exp_f16 v213, v209\n"
  "v_mfma_f32_16x16x32_bf16 v[156:159], v[228:231], v[92:95], v[156:159]\n"
  "ds_read_u16_d16_hi v177, v177\n"
  "v_lshlrev_b32_sdwa v179, 1, v179 dst_sel:DWORD dst_unused:UNUSED_PAD src0_sel:DWORD src1_sel:WORD_1\n"
  "v_exp_f16_sdwa v212, v208 dst_sel:WORD_1 dst_unused:UNUSED_PRESERVE src0_sel:WORD_1\n"
  "v_exp_f16_sdwa v213, v209 dst_sel:WORD_1 dst_unused:UNUSED_PRESERVE src0_sel:WORD_1\n"
  "v_mfma_f32_16x16x32_bf16 v[160:163], v[228:231], v[68:71], v[160:163]\n"
  "s_waitcnt lgkmcnt(14)\n"
  "ds_read_b128 v[228:231], v0 offset:16640\n"
  "ds_read_u16 v178, v178\n"
  "ds_read_u16_d16_hi v179, v179\n"
  "v_pk_add_f16 v212, v212, 1.0 op_sel_hi:[1,0]\n"
  "v_cvt_pk_bf16_f32 v128, v128, v129\n"
  "v_cvt_pk_bf16_f32 v129, v130, v131\n"
  "v_cvt_pk_bf16_f32 v130, v132, v133\n"
  "v_cvt_pk_bf16_f32 v131, v134, v135\n"
  "v_cvt_pk_bf16_f32 v136, v136, v137\n"
  "v_cvt_pk_bf16_f32 v137, v138, v139\n"
  "v_mfma_f32_16x16x32_bf16 v[152:155], v[216:219], v[96:99], v[152:155]\n"
  "v_cvt_pk_bf16_f32 v138, v140, v141\n"
  "v_cvt_pk_bf16_f32 v139, v142, v143\n"
  "v_cvt_pk_bf16_f32 v144, v144, v145\n"
  "v_cvt_pk_bf16_f32 v145, v146, v147\n"
  "v_cvt_pk_bf16_f32 v146, v148, v149\n"
  "v_cvt_pk_bf16_f32 v147, v150, v151\n"
  "buffer_store_dwordx4 v[128:131], v5, s[44:47], s60 offen offset:0 sc1\n"
  "buffer_store_dwordx4 v[136:139], v5, s[44:47], s60 offen offset:64 sc1\n"
  "buffer_store_dwordx4 v[144:147], v5, s[44:47], s60 offen offset:128 sc1\n"
  "v_pk_add_f16 v213, v213, 1.0 op_sel_hi:[1,0]\n"
  "v_mfma_f32_16x16x32_bf16 v[156:159], v[216:219], v[100:103], v[156:159]\n"
  "v_rcp_f16 v208, v212\n"
  "v_rcp_f16 v209, v213\n"
  "v_rcp_f16_sdwa v208, v212 dst_sel:WORD_1 dst_unused:UNUSED_PRESERVE src0_sel:WORD_1\n"
  "v_rcp_f16_sdwa v209, v213 dst_sel:WORD_1 dst_unused:UNUSED_PRESERVE src0_sel:WORD_1\n"
  "v_mfma_f32_16x16x32_bf16 v[160:163], v[216:219], v[76:79], v[160:163]\n"
  "s_waitcnt lgkmcnt(0)\n"
  "ds_read_b128 v[216:219], v2 offset:20480\n"
  "v_or_b32 v190, v172, v173\n"
  "v_pk_mul_f16 v186, v186, v208\n"
  "v_pk_mul_f16 v187, v187, v209\n"
  "v_or_b32 v191, v174, v175\n"
  "v_or_b32 v166, v176, v177\n"
  "v_or_b32 v167, v178, v179\n"
  "v_mfma_f32_16x16x32_bf16 v[168:171], v[220:223], v[80:83], 0\n"
  "v_lshlrev_b32_sdwa v156, 1, v156 dst_sel:DWORD dst_unused:UNUSED_PAD src0_sel:DWORD src1_sel:WORD_1\n"
  "v_lshlrev_b32_sdwa v157, 1, v157 dst_sel:DWORD dst_unused:UNUSED_PAD src0_sel:DWORD src1_sel:WORD_1\n"
  "v_mfma_f32_16x16x32_bf16 v[172:175], v[220:223], v[84:87], 0\n"
  "ds_read_u16 v156, v156\n"
  "v_lshlrev_b32_sdwa v158, 1, v158 dst_sel:DWORD dst_unused:UNUSED_PAD src0_sel:DWORD src1_sel:WORD_1\n"
  "v_cvt_pk_f16_f32 v192, v152, v153\n"
  "v_mfma_f32_16x16x32_bf16 v[176:179], v[220:223], v[60:63], 0\n"
  "ds_read_b128 v[220:223], v2 offset:21504\n"
  "v_cvt_pk_f16_f32 v193, v154, v155\n"
  "v_pk_fma_f16 v208, v192, v192, s56\n"
  "v_mfma_f32_16x16x32_bf16 v[168:171], v[224:227], v[88:91], v[168:171]\n"
  "ds_read_u16_d16_hi v157, v157\n"
  "v_pk_fma_f16 v209, v193, v193, s56\n"
  "v_lshlrev_b32_sdwa v159, 1, v159 dst_sel:DWORD dst_unused:UNUSED_PAD src0_sel:DWORD src1_sel:WORD_1\n"
  "v_mfma_f32_16x16x32_bf16 v[172:175], v[224:227], v[92:95], v[172:175]\n"
  "ds_read_u16 v158, v158\n"
  "v_lshlrev_b32_sdwa v160, 1, v160 dst_sel:DWORD dst_unused:UNUSED_PAD src0_sel:DWORD src1_sel:WORD_1\n"
  "v_pk_mul_f16 v208, v192, v208\n"
  "v_pk_mul_f16 v209, v193, v209\n"
  "v_mfma_f32_16x16x32_bf16 v[176:179], v[224:227], v[68:71], v[176:179]\n"
  "ds_read_b128 v[224:227], v2 offset:22528\n"
  "ds_read_u16_d16_hi v159, v159\n"
  "v_lshlrev_b32_sdwa v161, 1, v161 dst_sel:DWORD dst_unused:UNUSED_PAD src0_sel:DWORD src1_sel:WORD_1\n"
  "v_lshlrev_b32_sdwa v162, 1, v162 dst_sel:DWORD dst_unused:UNUSED_PAD src0_sel:DWORD src1_sel:WORD_1\n"
  "v_mfma_f32_16x16x32_bf16 v[168:171], v[228:231], v[96:99], v[168:171]\n"
  "ds_read_u16 v160, v160\n"
  "v_exp_f16 v212, v208\n"
  "v_mfma_f32_16x16x32_bf16 v[172:175], v[228:231], v[100:103], v[172:175]\n"
  "ds_read_u16_d16_hi v161, v161\n"
  "v_exp_f16 v213, v209\n"
  "v_mfma_f32_16x16x32_bf16 v[176:179], v[228:231], v[76:79], v[176:179]\n"
  "s_waitcnt lgkmcnt(8)\n"
  "ds_read_b128 v[228:231], v0 offset:18432\n"
  "v_lshlrev_b32_sdwa v163, 1, v163 dst_sel:DWORD dst_unused:UNUSED_PAD src0_sel:DWORD src1_sel:WORD_1\n"
  "v_exp_f16_sdwa v212, v208 dst_sel:WORD_1 dst_unused:UNUSED_PRESERVE src0_sel:WORD_1\n"
  "v_mfma_f32_16x16x32_f16 v[128:131], v[216:219], v[164:167], v[104:107]\n"
  "ds_read_u16 v162, v162\n"
  "v_exp_f16_sdwa v213, v209 dst_sel:WORD_1 dst_unused:UNUSED_PRESERVE src0_sel:WORD_1\n"
  "v_mfma_f32_16x16x32_f16 v[8:11], v[216:219], v[188:191], v[104:107]\n"
  "ds_read_u16_d16_hi v163, v163\n"
  "v_pk_add_f16 v212, v212, 1.0 op_sel_hi:[1,0]\n"
  "v_mfma_f32_16x16x32_f16 v[104:107], v[216:219], v[184:187], v[104:107]\n"
  "s_waitcnt lgkmcnt(9)\n"
  "ds_read_b128 v[216:219], v0 offset:20480\n"
  "v_pk_add_f16 v213, v213, 1.0 op_sel_hi:[1,0]\n"
  "v_rcp_f16 v208, v212\n"
  "v_mfma_f32_16x16x32_f16 v[132:135], v[220:223], v[164:167], v[108:111]\n"
  "v_rcp_f16 v209, v213\n"
  "v_mfma_f32_16x16x32_f16 v[12:15], v[220:223], v[188:191], v[108:111]\n"
  "v_rcp_f16_sdwa v208, v212 dst_sel:WORD_1 dst_unused:UNUSED_PRESERVE src0_sel:WORD_1\n"
  "v_mfma_f32_16x16x32_f16 v[108:111], v[220:223], v[184:187], v[108:111]\n"
  "s_waitcnt lgkmcnt(6)\n"
  "ds_read_b128 v[220:223], v0 offset:22528\n"
  "v_rcp_f16_sdwa v209, v213 dst_sel:WORD_1 dst_unused:UNUSED_PRESERVE src0_sel:WORD_1\n"
  "v_mfma_f32_16x16x32_f16 v[136:139], v[224:227], v[164:167], v[112:115]\n"
  "v_or_b32 v196, v156, v157\n"
  "v_pk_mul_f16 v192, v192, v208\n"
  "v_mfma_f32_16x16x32_f16 v[16:19], v[224:227], v[188:191], v[112:115]\n"
  "v_pk_mul_f16 v193, v193, v209\n"
  "v_or_b32 v197, v158, v159\n"
  "v_mfma_f32_16x16x32_f16 v[112:115], v[224:227], v[184:187], v[112:115]\n"
  "s_waitcnt lgkmcnt(2)\n"
  "ds_read_b128 v[224:227], v2 offset:23552\n"
  "v_or_b32 v180, v160, v161\n"
  "v_or_b32 v181, v162, v163\n"
  "v_mfma_f32_16x16x32_bf16 v[152:155], v[228:231], v[80:83], 0\n"
  "v_lshlrev_b32_sdwa v172, 1, v172 dst_sel:DWORD dst_unused:UNUSED_PAD src0_sel:DWORD src1_sel:WORD_1\n"
  "v_lshlrev_b32_sdwa v173, 1, v173 dst_sel:DWORD dst_unused:UNUSED_PAD src0_sel:DWORD src1_sel:WORD_1\n"
  "v_mfma_f32_16x16x32_bf16 v[156:159], v[228:231], v[84:87], 0\n"
  "ds_read_u16 v172, v172\n"
  "v_lshlrev_b32_sdwa v174, 1, v174 dst_sel:DWORD dst_unused:UNUSED_PAD src0_sel:DWORD src1_sel:WORD_1\n"
  "v_cvt_pk_f16_f32 v194, v168, v169\n"
  "v_mfma_f32_16x16x32_bf16 v[160:163], v[228:231], v[60:63], 0\n"
  "s_waitcnt lgkmcnt(3)\n"
  "ds_read_b128 v[228:231], v2 offset:24576\n"
  "v_cvt_pk_f16_f32 v195, v170, v171\n"
  "v_pk_fma_f16 v208, v194, v194, s56\n"
  "v_mfma_f32_16x16x32_bf16 v[152:155], v[216:219], v[88:91], v[152:155]\n"
  "ds_read_u16_d16_hi v173, v173\n"
  "v_pk_fma_f16 v209, v195, v195, s56\n"
  "v_lshlrev_b32_sdwa v175, 1, v175 dst_sel:DWORD dst_unused:UNUSED_PAD src0_sel:DWORD src1_sel:WORD_1\n"
  "v_mfma_f32_16x16x32_bf16 v[156:159], v[216:219], v[92:95], v[156:159]\n"
  "ds_read_u16 v174, v174\n"
  "v_lshlrev_b32_sdwa v176, 1, v176 dst_sel:DWORD dst_unused:UNUSED_PAD src0_sel:DWORD src1_sel:WORD_1\n"
  "v_pk_mul_f16 v208, v194, v208\n"
  "v_pk_mul_f16 v209, v195, v209\n"
  "v_mfma_f32_16x16x32_bf16 v[160:163], v[216:219], v[68:71], v[160:163]\n"
  "s_waitcnt lgkmcnt(5)\n"
  "ds_read_b128 v[216:219], v2 offset:25600\n"
  "ds_read_u16_d16_hi v175, v175\n"
  "v_lshlrev_b32_sdwa v177, 1, v177 dst_sel:DWORD dst_unused:UNUSED_PAD src0_sel:DWORD src1_sel:WORD_1\n"
  "v_lshlrev_b32_sdwa v178, 1, v178 dst_sel:DWORD dst_unused:UNUSED_PAD src0_sel:DWORD src1_sel:WORD_1\n"
  "v_mfma_f32_16x16x32_bf16 v[152:155], v[220:223], v[96:99], v[152:155]\n"
  "ds_read_u16 v176, v176\n"
  "v_exp_f16 v212, v208\n"
  "v_mfma_f32_16x16x32_bf16 v[156:159], v[220:223], v[100:103], v[156:159]\n"
  "ds_read_u16_d16_hi v177, v177\n"
  "v_exp_f16 v213, v209\n"
  "v_mfma_f32_16x16x32_bf16 v[160:163], v[220:223], v[76:79], v[160:163]\n"
  "s_waitcnt lgkmcnt(8)\n"
  "ds_read_b128 v[220:223], v0 offset:18688\n"
  "v_lshlrev_b32_sdwa v179, 1, v179 dst_sel:DWORD dst_unused:UNUSED_PAD src0_sel:DWORD src1_sel:WORD_1\n"
  "v_exp_f16_sdwa v212, v208 dst_sel:WORD_1 dst_unused:UNUSED_PRESERVE src0_sel:WORD_1\n"
  "v_mfma_f32_16x16x32_f16 v[140:143], v[224:227], v[164:167], v[116:119]\n"
  "ds_read_u16 v178, v178\n"
  "v_exp_f16_sdwa v213, v209 dst_sel:WORD_1 dst_unused:UNUSED_PRESERVE src0_sel:WORD_1\n"
  "v_mfma_f32_16x16x32_f16 v[20:23], v[224:227], v[188:191], v[116:119]\n"
  "ds_read_u16_d16_hi v179, v179\n"
  "v_pk_add_f16 v212, v212, 1.0 op_sel_hi:[1,0]\n"
  "v_mfma_f32_16x16x32_f16 v[116:119], v[224:227], v[184:187], v[116:119]\n"
  "s_waitcnt lgkmcnt(9)\n"
  "ds_read_b128 v[224:227], v0 offset:20736\n"
  "v_pk_add_f16 v213, v213, 1.0 op_sel_hi:[1,0]\n"
  "v_rcp_f16 v208, v212\n"
  "v_mfma_f32_16x16x32_f16 v[144:147], v[228:231], v[164:167], v[120:123]\n"
  "v_rcp_f16 v209, v213\n"
  "v_mfma_f32_16x16x32_f16 v[24:27], v[228:231], v[188:191], v[120:123]\n"
  "v_rcp_f16_sdwa v208, v212 dst_sel:WORD_1 dst_unused:UNUSED_PRESERVE src0_sel:WORD_1\n"
  "v_mfma_f32_16x16x32_f16 v[120:123], v[228:231], v[184:187], v[120:123]\n"
  "s_waitcnt lgkmcnt(6)\n"
  "ds_read_b128 v[228:231], v0 offset:22784\n"
  "v_rcp_f16_sdwa v209, v213 dst_sel:WORD_1 dst_unused:UNUSED_PRESERVE src0_sel:WORD_1\n"
  "v_mfma_f32_16x16x32_f16 v[148:151], v[216:219], v[164:167], v[124:127]\n"
  "v_or_b32 v198, v172, v173\n"
  "v_pk_mul_f16 v194, v194, v208\n"
  "v_mfma_f32_16x16x32_f16 v[28:31], v[216:219], v[188:191], v[124:127]\n"
  "v_pk_mul_f16 v195, v195, v209\n"
  "v_or_b32 v199, v174, v175\n"
  "v_mfma_f32_16x16x32_f16 v[124:127], v[216:219], v[184:187], v[124:127]\n"
  "s_waitcnt lgkmcnt(2)\n"
  "ds_read_b128 v[216:219], v2 offset:26624\n"
  "v_or_b32 v182, v176, v177\n"
  "v_or_b32 v183, v178, v179\n"
  "v_mfma_f32_16x16x32_bf16 v[168:171], v[220:223], v[80:83], 0\n"
  "v_lshlrev_b32_sdwa v156, 1, v156 dst_sel:DWORD dst_unused:UNUSED_PAD src0_sel:DWORD src1_sel:WORD_1\n"
  "v_lshlrev_b32_sdwa v157, 1, v157 dst_sel:DWORD dst_unused:UNUSED_PAD src0_sel:DWORD src1_sel:WORD_1\n"
  "v_mfma_f32_16x16x32_bf16 v[172:175], v[220:223], v[84:87], 0\n"
  "ds_read_u16 v156, v156\n"
  "v_lshlrev_b32_sdwa v158, 1, v158 dst_sel:DWORD dst_unused:UNUSED_PAD src0_sel:DWORD src1_sel:WORD_1\n"
  "v_cvt_pk_f16_f32 v184, v152, v153\n"
  "v_mfma_f32_16x16x32_bf16 v[176:179], v[220:223], v[60:63], 0\n"
  "s_waitcnt lgkmcnt(3)\n"
  "ds_read_b128 v[220:223], v2 offset:27648\n"
  "v_cvt_pk_f16_f32 v185, v154, v155\n"
  "v_pk_fma_f16 v208, v184, v184, s56\n"
  "v_mfma_f32_16x16x32_bf16 v[168:171], v[224:227], v[88:91], v[168:171]\n"
  "ds_read_u16_d16_hi v157, v157\n"
  "v_pk_fma_f16 v209, v185, v185, s56\n"
  "v_lshlrev_b32_sdwa v159, 1, v159 dst_sel:DWORD dst_unused:UNUSED_PAD src0_sel:DWORD src1_sel:WORD_1\n"
  "v_mfma_f32_16x16x32_bf16 v[172:175], v[224:227], v[92:95], v[172:175]\n"
  "ds_read_u16 v158, v158\n"
  "v_lshlrev_b32_sdwa v160, 1, v160 dst_sel:DWORD dst_unused:UNUSED_PAD src0_sel:DWORD src1_sel:WORD_1\n"
  "v_pk_mul_f16 v208, v184, v208\n"
  "v_pk_mul_f16 v209, v185, v209\n"
  "v_mfma_f32_16x16x32_bf16 v[176:179], v[224:227], v[68:71], v[176:179]\n"
  "s_waitcnt lgkmcnt(5)\n"
  "ds_read_b128 v[224:227], v2 offset:28672\n"
  "ds_read_u16_d16_hi v159, v159\n"
  "v_lshlrev_b32_sdwa v161, 1, v161 dst_sel:DWORD dst_unused:UNUSED_PAD src0_sel:DWORD src1_sel:WORD_1\n"
  "v_lshlrev_b32_sdwa v162, 1, v162 dst_sel:DWORD dst_unused:UNUSED_PAD src0_sel:DWORD src1_sel:WORD_1\n"
  "v_mfma_f32_16x16x32_bf16 v[168:171], v[228:231], v[96:99], v[168:171]\n"
  "ds_read_u16 v160, v160\n"
  "v_exp_f16 v212, v208\n"
  "v_mfma_f32_16x16x32_bf16 v[172:175], v[228:231], v[100:103], v[172:175]\n"
  "ds_read_u16_d16_hi v161, v161\n"
  "v_exp_f16 v213, v209\n"
  "v_mfma_f32_16x16x32_bf16 v[176:179], v[228:231], v[76:79], v[176:179]\n"
  "s_waitcnt lgkmcnt(8)\n"
  "ds_read_b128 v[228:231], v0 offset:24576\n"
  "v_lshlrev_b32_sdwa v163, 1, v163 dst_sel:DWORD dst_unused:UNUSED_PAD src0_sel:DWORD src1_sel:WORD_1\n"
  "v_exp_f16_sdwa v212, v208 dst_sel:WORD_1 dst_unused:UNUSED_PRESERVE src0_sel:WORD_1\n"
  "v_mfma_f32_16x16x32_f16 v[104:107], v[216:219], v[192:195], v[104:107]\n"
  "ds_read_u16 v162, v162\n"
  "v_exp_f16_sdwa v213, v209 dst_sel:WORD_1 dst_unused:UNUSED_PRESERVE src0_sel:WORD_1\n"
  "v_mfma_f32_16x16x32_f16 v[8:11], v[216:219], v[196:199], v[8:11]\n"
  "ds_read_u16_d16_hi v163, v163\n"
  "v_pk_add_f16 v212, v212, 1.0 op_sel_hi:[1,0]\n"
  "v_mfma_f32_16x16x32_f16 v[128:131], v[216:219], v[180:183], v[128:131]\n"
  "s_waitcnt lgkmcnt(9)\n"
  "ds_read_b128 v[216:219], v0 offset:26624\n"
  "v_pk_add_f16 v213, v213, 1.0 op_sel_hi:[1,0]\n"
  "v_rcp_f16 v208, v212\n"
  "v_mfma_f32_16x16x32_f16 v[108:111], v[220:223], v[192:195], v[108:111]\n"
  "v_rcp_f16 v209, v213\n"
  "v_mfma_f32_16x16x32_f16 v[12:15], v[220:223], v[196:199], v[12:15]\n"
  "v_rcp_f16_sdwa v208, v212 dst_sel:WORD_1 dst_unused:UNUSED_PRESERVE src0_sel:WORD_1\n"
  "v_mfma_f32_16x16x32_f16 v[132:135], v[220:223], v[180:183], v[132:135]\n"
  "s_waitcnt lgkmcnt(6)\n"
  "ds_read_b128 v[220:223], v0 offset:28672\n"
  "v_rcp_f16_sdwa v209, v213 dst_sel:WORD_1 dst_unused:UNUSED_PRESERVE src0_sel:WORD_1\n"
  "v_mfma_f32_16x16x32_f16 v[112:115], v[224:227], v[192:195], v[112:115]\n"
  "v_or_b32 v188, v156, v157\n"
  "v_pk_mul_f16 v184, v184, v208\n"
  "v_mfma_f32_16x16x32_f16 v[16:19], v[224:227], v[196:199], v[16:19]\n"
  "v_pk_mul_f16 v185, v185, v209\n"
  "v_or_b32 v189, v158, v159\n"
  "v_mfma_f32_16x16x32_f16 v[136:139], v[224:227], v[180:183], v[136:139]\n"
  "s_waitcnt lgkmcnt(2)\n"
  "ds_read_b128 v[224:227], v2 offset:29696\n"
  "v_or_b32 v164, v160, v161\n"
  "v_or_b32 v165, v162, v163\n"
  "v_mfma_f32_16x16x32_bf16 v[152:155], v[228:231], v[80:83], 0\n"
  "v_lshlrev_b32_sdwa v172, 1, v172 dst_sel:DWORD dst_unused:UNUSED_PAD src0_sel:DWORD src1_sel:WORD_1\n"
  "v_lshlrev_b32_sdwa v173, 1, v173 dst_sel:DWORD dst_unused:UNUSED_PAD src0_sel:DWORD src1_sel:WORD_1\n"
  "v_mfma_f32_16x16x32_bf16 v[156:159], v[228:231], v[84:87], 0\n"
  "ds_read_u16 v172, v172\n"
  "v_lshlrev_b32_sdwa v174, 1, v174 dst_sel:DWORD dst_unused:UNUSED_PAD src0_sel:DWORD src1_sel:WORD_1\n"
  "v_cvt_pk_f16_f32 v186, v168, v169\n"
  "v_mfma_f32_16x16x32_bf16 v[160:163], v[228:231], v[60:63], 0\n"
  "s_waitcnt lgkmcnt(3)\n"
  "ds_read_b128 v[228:231], v2 offset:30720\n"
  "v_cvt_pk_f16_f32 v187, v170, v171\n"
  "v_pk_fma_f16 v208, v186, v186, s56\n"
  "v_mfma_f32_16x16x32_bf16 v[152:155], v[216:219], v[88:91], v[152:155]\n"
  "ds_read_u16_d16_hi v173, v173\n"
  "v_pk_fma_f16 v209, v187, v187, s56\n"
  "v_lshlrev_b32_sdwa v175, 1, v175 dst_sel:DWORD dst_unused:UNUSED_PAD src0_sel:DWORD src1_sel:WORD_1\n"
  "v_mfma_f32_16x16x32_bf16 v[156:159], v[216:219], v[92:95], v[156:159]\n"
  "ds_read_u16 v174, v174\n"
  "v_lshlrev_b32_sdwa v176, 1, v176 dst_sel:DWORD dst_unused:UNUSED_PAD src0_sel:DWORD src1_sel:WORD_1\n"
  "v_pk_mul_f16 v208, v186, v208\n"
  "v_pk_mul_f16 v209, v187, v209\n"
  "v_mfma_f32_16x16x32_bf16 v[160:163], v[216:219], v[68:71], v[160:163]\n"
  "s_waitcnt lgkmcnt(5)\n"
  "ds_read_b128 v[216:219], v2 offset:31744\n"
  "ds_read_u16_d16_hi v175, v175\n"
  "v_lshlrev_b32_sdwa v177, 1, v177 dst_sel:DWORD dst_unused:UNUSED_PAD src0_sel:DWORD src1_sel:WORD_1\n"
  "v_lshlrev_b32_sdwa v178, 1, v178 dst_sel:DWORD dst_unused:UNUSED_PAD src0_sel:DWORD src1_sel:WORD_1\n"
  "v_mfma_f32_16x16x32_bf16 v[152:155], v[220:223], v[96:99], v[152:155]\n"
  "ds_read_u16 v176, v176\n"
  "v_exp_f16 v212, v208\n"
  "v_mfma_f32_16x16x32_bf16 v[156:159], v[220:223], v[100:103], v[156:159]\n"
  "ds_read_u16_d16_hi v177, v177\n"
  "v_exp_f16 v213, v209\n"
  "v_mfma_f32_16x16x32_bf16 v[160:163], v[220:223], v[76:79], v[160:163]\n"
  "s_waitcnt lgkmcnt(8)\n"
  "ds_read_b128 v[220:223], v0 offset:24832\n"
  "v_lshlrev_b32_sdwa v179, 1, v179 dst_sel:DWORD dst_unused:UNUSED_PAD src0_sel:DWORD src1_sel:WORD_1\n"
  "v_exp_f16_sdwa v212, v208 dst_sel:WORD_1 dst_unused:UNUSED_PRESERVE src0_sel:WORD_1\n"
  "v_mfma_f32_16x16x32_f16 v[116:119], v[224:227], v[192:195], v[116:119]\n"
  "ds_read_u16 v178, v178\n"
  "v_exp_f16_sdwa v213, v209 dst_sel:WORD_1 dst_unused:UNUSED_PRESERVE src0_sel:WORD_1\n"
  "v_mfma_f32_16x16x32_f16 v[20:23], v[224:227], v[196:199], v[20:23]\n"
  "ds_read_u16_d16_hi v179, v179\n"
  "v_pk_add_f16 v212, v212, 1.0 op_sel_hi:[1,0]\n"
  "v_mfma_f32_16x16x32_f16 v[140:143], v[224:227], v[180:183], v[140:143]\n"
  "s_waitcnt lgkmcnt(9)\n"
  "ds_read_b128 v[224:227], v0 offset:26880\n"
  "v_pk_add_f16 v213, v213, 1.0 op_sel_hi:[1,0]\n"
  "v_rcp_f16 v208, v212\n"
  "v_mfma_f32_16x16x32_f16 v[120:123], v[228:231], v[192:195], v[120:123]\n"
  "v_rcp_f16 v209, v213\n"
  "v_mfma_f32_16x16x32_f16 v[24:27], v[228:231], v[196:199], v[24:27]\n"
  "v_rcp_f16_sdwa v208, v212 dst_sel:WORD_1 dst_unused:UNUSED_PRESERVE src0_sel:WORD_1\n"
  "v_mfma_f32_16x16x32_f16 v[144:147], v[228:231], v[180:183], v[144:147]\n"
  "s_waitcnt lgkmcnt(6)\n"
  "ds_read_b128 v[228:231], v0 offset:28928\n"
  "v_rcp_f16_sdwa v209, v213 dst_sel:WORD_1 dst_unused:UNUSED_PRESERVE src0_sel:WORD_1\n"
  "v_mfma_f32_16x16x32_f16 v[124:127], v[216:219], v[192:195], v[124:127]\n"
  "v_or_b32 v190, v172, v173\n"
  "v_pk_mul_f16 v186, v186, v208\n"
  "v_mfma_f32_16x16x32_f16 v[28:31], v[216:219], v[196:199], v[28:31]\n"
  "v_pk_mul_f16 v187, v187, v209\n"
  "v_or_b32 v191, v174, v175\n"
  "v_mfma_f32_16x16x32_f16 v[148:151], v[216:219], v[180:183], v[148:151]\n"
  "s_waitcnt lgkmcnt(2)\n"
  "ds_read_b128 v[216:219], v2 offset:32768\n"
  "v_or_b32 v166, v176, v177\n"
  "v_or_b32 v167, v178, v179\n"
  "v_mfma_f32_16x16x32_bf16 v[168:171], v[220:223], v[80:83], 0\n"
  "v_lshlrev_b32_sdwa v156, 1, v156 dst_sel:DWORD dst_unused:UNUSED_PAD src0_sel:DWORD src1_sel:WORD_1\n"
  "v_lshlrev_b32_sdwa v157, 1, v157 dst_sel:DWORD dst_unused:UNUSED_PAD src0_sel:DWORD src1_sel:WORD_1\n"
  "v_mfma_f32_16x16x32_bf16 v[172:175], v[220:223], v[84:87], 0\n"
  "v_lshlrev_b32_sdwa v158, 1, v158 dst_sel:DWORD dst_unused:UNUSED_PAD src0_sel:DWORD src1_sel:WORD_1\n"
  "v_mfma_f32_16x16x32_bf16 v[176:179], v[220:223], v[60:63], 0\n"
  "s_waitcnt lgkmcnt(2)\n"
  "ds_read_b128 v[220:223], v2 offset:33792\n"
  "ds_read_u16 v156, v156\n"
  "v_cvt_pk_f16_f32 v192, v152, v153\n"
  "v_cvt_pk_f16_f32 v193, v154, v155\n"
  "s_waitcnt vmcnt(11)\n"
  "v_mfma_f32_16x16x32_bf16 v[104:107], v[248:251], v[200:203], v[104:107]\n"
  "ds_read_u16_d16_hi v157, v157\n"
  "v_pk_fma_f16 v208, v192, v192, s56\n"
  "v_mfma_f32_16x16x32_bf16 v[108:111], v[252:255], v[200:203], v[108:111]\n"
  "v_pk_fma_f16 v209, v193, v193, s56\n"
  "v_lshlrev_b32_sdwa v159, 1, v159 dst_sel:DWORD dst_unused:UNUSED_PAD src0_sel:DWORD src1_sel:WORD_1\n"
  "v_mfma_f32_16x16x32_bf16 v[168:171], v[224:227], v[88:91], v[168:171]\n"
  "s_waitcnt lgkmcnt(4)\n"
  "ds_read_u16 v158, v158\n"
  "v_lshlrev_b32_sdwa v160, 1, v160 dst_sel:DWORD dst_unused:UNUSED_PAD src0_sel:DWORD src1_sel:WORD_1\n"
  "v_mfma_f32_16x16x32_bf16 v[172:175], v[224:227], v[92:95], v[172:175]\n"
  "v_pk_mul_f16 v208, v192, v208\n"
  "v_pk_mul_f16 v209, v193, v209\n"
  "v_mfma_f32_16x16x32_bf16 v[176:179], v[224:227], v[68:71], v[176:179]\n"
  "ds_read_b128 v[224:227], v2 offset:34816\n"
  "ds_read_u16_d16_hi v159, v159\n"
  "v_lshlrev_b32_sdwa v161, 1, v161 dst_sel:DWORD dst_unused:UNUSED_PAD src0_sel:DWORD src1_sel:WORD_1\n"
  "v_lshlrev_b32_sdwa v162, 1, v162 dst_sel:DWORD dst_unused:UNUSED_PAD src0_sel:DWORD src1_sel:WORD_1\n"
  "v_mfma_f32_16x16x32_bf16 v[168:171], v[228:231], v[96:99], v[168:171]\n"
  "ds_read_u16 v160, v160\n"
  "v_exp_f16 v212, v208\n"
  "v_mfma_f32_16x16x32_bf16 v[172:175], v[228:231], v[100:103], v[172:175]\n"
  "v_exp_f16 v213, v209\n"
  "v_mfma_f32_16x16x32_bf16 v[176:179], v[228:231], v[76:79], v[176:179]\n"
  "ds_read_b128 v[228:231], v0 offset:30720\n"
  "ds_read_u16_d16_hi v161, v161\n"
  "s_waitcnt vmcnt(10)\n"
  "v_mfma_f32_16x16x32_bf16 v[112:115], v[248:251], v[204:207], v[112:115]\n"
  "s_waitcnt lgkmcnt(9)\n"
  "ds_read_u16 v162, v162\n"
  "v_lshlrev_b32_sdwa v163, 1, v163 dst_sel:DWORD dst_unused:UNUSED_PAD src0_sel:DWORD src1_sel:WORD_1\n"
  "v_exp_f16_sdwa v212, v208 dst_sel:WORD_1 dst_unused:UNUSED_PRESERVE src0_sel:WORD_1\n"
  "v_mfma_f32_16x16x32_bf16 v[116:119], v[252:255], v[204:207], v[116:119]\n"
  "v_exp_f16_sdwa v213, v209 dst_sel:WORD_1 dst_unused:UNUSED_PRESERVE src0_sel:WORD_1\n"
  "v_mfma_f32_16x16x32_f16 v[104:107], v[216:219], v[184:187], v[104:107]\n"
  "ds_read_u16_d16_hi v163, v163\n"
  "v_mfma_f32_16x16x32_f16 v[8:11], v[216:219], v[188:191], v[8:11]\n"
  "s_waitcnt lgkmcnt(10)\n"
  "v_pk_add_f16 v212, v212, 1.0 op_sel_hi:[1,0]\n"
  "v_pk_add_f16 v213, v213, 1.0 op_sel_hi:[1,0]\n"
  "v_mfma_f32_16x16x32_f16 v[128:131], v[216:219], v[164:167], v[128:131]\n"
  "ds_read_b128 v[216:219], v0 offset:32768\n"
  "v_rcp_f16 v208, v212\n"
  "v_mfma_f32_16x16x32_f16 v[108:111], v[220:223], v[184:187], v[108:111]\n"
  "v_rcp_f16 v209, v213\n"
  "v_mfma_f32_16x16x32_f16 v[12:15], v[220:223], v[188:191], v[12:15]\n"
  "v_mfma_f32_16x16x32_f16 v[132:135], v[220:223], v[164:167], v[132:135]\n"
  "ds_read_b128 v[220:223], v0 offset:34816\n"
  "v_rcp_f16_sdwa v208, v212 dst_sel:WORD_1 dst_unused:UNUSED_PRESERVE src0_sel:WORD_1\n"
  "s_waitcnt vmcnt(9)\n"
  "v_mfma_f32_16x16x32_bf16 v[120:123], v[248:251], v[232:235], v[120:123]\n"
  "v_rcp_f16_sdwa v209, v213 dst_sel:WORD_1 dst_unused:UNUSED_PRESERVE src0_sel:WORD_1\n"
  "v_mfma_f32_16x16x32_bf16 v[124:127], v[252:255], v[232:235], v[124:127]\n"
  "s_waitcnt lgkmcnt(7)\n"
  "buffer_load_dwordx4 v[200:203], v5, s[40:43], s58 offen offset:0 \n"
  "buffer_load_dwordx4 v[204:207], v5, s[40:43], s58 offen offset:64 \n"
  "buffer_load_dwordx4 v[232:235], v5, s[40:43], s58 offen offset:128 \n"
  "v_or_b32 v196, v156, v157\n"
  "v_mfma_f32_16x16x32_f16 v[112:115], v[224:227], v[184:187], v[112:115]\n"
  "v_pk_mul_f16 v192, v192, v208\n"
  "v_pk_mul_f16 v193, v193, v209\n"
  "v_mfma_f32_16x16x32_f16 v[16:19], v[224:227], v[188:191], v[16:19]\n"
  "v_or_b32 v197, v158, v159\n"
  "v_mfma_f32_16x16x32_f16 v[136:139], v[224:227], v[164:167], v[136:139]\n"
  "s_waitcnt lgkmcnt(2)\n"
  "ds_read_b128 v[224:227], v2 offset:35840\n"
  "v_or_b32 v180, v160, v161\n"
  "v_or_b32 v181, v162, v163\n"
  "v_mfma_f32_16x16x32_bf16 v[152:155], v[228:231], v[80:83], 0\n"
  "v_lshlrev_b32_sdwa v172, 1, v172 dst_sel:DWORD dst_unused:UNUSED_PAD src0_sel:DWORD src1_sel:WORD_1\n"
  "v_lshlrev_b32_sdwa v173, 1, v173 dst_sel:DWORD dst_unused:UNUSED_PAD src0_sel:DWORD src1_sel:WORD_1\n"
  "v_mfma_f32_16x16x32_bf16 v[156:159], v[228:231], v[84:87], 0\n"
  "ds_read_u16 v172, v172\n"
  "v_lshlrev_b32_sdwa v174, 1, v174 dst_sel:DWORD dst_unused:UNUSED_PAD src0_sel:DWORD src1_sel:WORD_1\n"
  "v_cvt_pk_f16_f32 v194, v168, v169\n"
  "v_mfma_f32_16x16x32_bf16 v[160:163], v[228:231], v[60:63], 0\n"
  "s_waitcnt lgkmcnt(3)\n"
  "ds_read_b128 v[228:231], v2 offset:36864\n"
  "v_cvt_pk_f16_f32 v195, v170, v171\n"
  "v_pk_fma_f16 v208, v194, v194, s56\n"
  "v_mfma_f32_16x16x32_bf16 v[152:155], v[216:219], v[88:91], v[152:155]\n"
  "ds_read_u16_d16_hi v173, v173\n"
  "v_pk_fma_f16 v209, v195, v195, s56\n"
  "v_lshlrev_b32_sdwa v175, 1, v175 dst_sel:DWORD dst_unused:UNUSED_PAD src0_sel:DWORD src1_sel:WORD_1\n"
  "v_mfma_f32_16x16x32_bf16 v[156:159], v[216:219], v[92:95], v[156:159]\n"
  "ds_read_u16 v174, v174\n"
  "v_lshlrev_b32_sdwa v176, 1, v176 dst_sel:DWORD dst_unused:UNUSED_PAD src0_sel:DWORD src1_sel:WORD_1\n"
  "v_pk_mul_f16 v208, v194, v208\n"
  "v_pk_mul_f16 v209, v195, v209\n"
  "v_mfma_f32_16x16x32_bf16 v[160:163], v[216:219], v[68:71], v[160:163]\n"
  "s_waitcnt lgkmcnt(5)\n"
  "ds_read_b128 v[216:219], v2 offset:37888\n"
  "ds_read_u16_d16_hi v175, v175\n"
  "v_lshlrev_b32_sdwa v177, 1, v177 dst_sel:DWORD dst_unused:UNUSED_PAD src0_sel:DWORD src1_sel:WORD_1\n"
  "v_lshlrev_b32_sdwa v178, 1, v178 dst_sel:DWORD dst_unused:UNUSED_PAD src0_sel:DWORD src1_sel:WORD_1\n"
  "v_mfma_f32_16x16x32_bf16 v[152:155], v[220:223], v[96:99], v[152:155]\n"
  "ds_read_u16 v176, v176\n"
  "v_exp_f16 v212, v208\n"
  "v_mfma_f32_16x16x32_bf16 v[156:159], v[220:223], v[100:103], v[156:159]\n"
  "ds_read_u16_d16_hi v177, v177\n"
  "v_exp_f16 v213, v209\n"
  "v_mfma_f32_16x16x32_bf16 v[160:163], v[220:223], v[76:79], v[160:163]\n"
  "s_waitcnt lgkmcnt(8)\n"
  "ds_read_b128 v[220:223], v0 offset:30976\n"
  "v_lshlrev_b32_sdwa v179, 1, v179 dst_sel:DWORD dst_unused:UNUSED_PAD src0_sel:DWORD src1_sel:WORD_1\n"
  "v_exp_f16_sdwa v212, v208 dst_sel:WORD_1 dst_unused:UNUSED_PRESERVE src0_sel:WORD_1\n"
  "v_mfma_f32_16x16x32_f16 v[116:119], v[224:227], v[184:187], v[116:119]\n"
  "ds_read_u16 v178, v178\n"
  "v_exp_f16_sdwa v213, v209 dst_sel:WORD_1 dst_unused:UNUSED_PRESERVE src0_sel:WORD_1\n"
  "v_mfma_f32_16x16x32_f16 v[20:23], v[224:227], v[188:191], v[20:23]\n"
  "ds_read_u16_d16_hi v179, v179\n"
  "v_pk_add_f16 v212, v212, 1.0 op_sel_hi:[1,0]\n"
  "v_mfma_f32_16x16x32_f16 v[140:143], v[224:227], v[164:167], v[140:143]\n"
  "s_waitcnt lgkmcnt(9)\n"
  "ds_read_b128 v[224:227], v0 offset:33024\n"
  "v_pk_add_f16 v213, v213, 1.0 op_sel_hi:[1,0]\n"
  "v_rcp_f16 v208, v212\n"
  "v_mfma_f32_16x16x32_f16 v[120:123], v[228:231], v[184:187], v[120:123]\n"
  "v_rcp_f16 v209, v213\n"
  "v_mfma_f32_16x16x32_f16 v[24:27], v[228:231], v[188:191], v[24:27]\n"
  "v_rcp_f16_sdwa v208, v212 dst_sel:WORD_1 dst_unused:UNUSED_PRESERVE src0_sel:WORD_1\n"
  "v_mfma_f32_16x16x32_f16 v[144:147], v[228:231], v[164:167], v[144:147]\n"
  "s_waitcnt lgkmcnt(6)\n"
  "ds_read_b128 v[228:231], v0 offset:35072\n"
  "v_rcp_f16_sdwa v209, v213 dst_sel:WORD_1 dst_unused:UNUSED_PRESERVE src0_sel:WORD_1\n"
  "v_mfma_f32_16x16x32_f16 v[124:127], v[216:219], v[184:187], v[124:127]\n"
  "v_or_b32 v198, v172, v173\n"
  "v_pk_mul_f16 v194, v194, v208\n"
  "v_mfma_f32_16x16x32_f16 v[28:31], v[216:219], v[188:191], v[28:31]\n"
  "v_pk_mul_f16 v195, v195, v209\n"
  "v_or_b32 v199, v174, v175\n"
  "v_mfma_f32_16x16x32_f16 v[148:151], v[216:219], v[164:167], v[148:151]\n"
  "s_waitcnt lgkmcnt(2)\n"
  "ds_read_b128 v[216:219], v2 offset:38912\n"
  "v_or_b32 v182, v176, v177\n"
  "v_or_b32 v183, v178, v179\n"
  "v_mfma_f32_16x16x32_bf16 v[168:171], v[220:223], v[80:83], 0\n"
  "v_lshlrev_b32_sdwa v156, 1, v156 dst_sel:DWORD dst_unused:UNUSED_PAD src0_sel:DWORD src1_sel:WORD_1\n"
  "v_lshlrev_b32_sdwa v157, 1, v157 dst_sel:DWORD dst_unused:UNUSED_PAD src0_sel:DWORD src1_sel:WORD_1\n"
  "v_mfma_f32_16x16x32_bf16 v[172:175], v[220:223], v[84:87], 0\n"
  "ds_read_u16 v156, v156\n"
  "v_lshlrev_b32_sdwa v158, 1, v158 dst_sel:DWORD dst_unused:UNUSED_PAD src0_sel:DWORD src1_sel:WORD_1\n"
  "v_cvt_pk_f16_f32 v184, v152, v153\n"
  "v_mfma_f32_16x16x32_bf16 v[176:179], v[220:223], v[60:63], 0\n"
  "s_waitcnt lgkmcnt(3)\n"
  "ds_read_b128 v[220:223], v2 offset:39936\n"
  "v_cvt_pk_f16_f32 v185, v154, v155\n"
  "v_pk_fma_f16 v208, v184, v184, s56\n"
  "v_mfma_f32_16x16x32_bf16 v[168:171], v[224:227], v[88:91], v[168:171]\n"
  "ds_read_u16_d16_hi v157, v157\n"
  "v_pk_fma_f16 v209, v185, v185, s56\n"
  "v_lshlrev_b32_sdwa v159, 1, v159 dst_sel:DWORD dst_unused:UNUSED_PAD src0_sel:DWORD src1_sel:WORD_1\n"
  "v_mfma_f32_16x16x32_bf16 v[172:175], v[224:227], v[92:95], v[172:175]\n"
  "ds_read_u16 v158, v158\n"
  "v_lshlrev_b32_sdwa v160, 1, v160 dst_sel:DWORD dst_unused:UNUSED_PAD src0_sel:DWORD src1_sel:WORD_1\n"
  "v_pk_mul_f16 v208, v184, v208\n"
  "v_pk_mul_f16 v209, v185, v209\n"
  "v_mfma_f32_16x16x32_bf16 v[176:179], v[224:227], v[68:71], v[176:179]\n"
  "s_waitcnt lgkmcnt(5)\n"
  "ds_read_b128 v[224:227], v2 offset:40960\n"
  "ds_read_u16_d16_hi v159, v159\n"
  "v_lshlrev_b32_sdwa v161, 1, v161 dst_sel:DWORD dst_unused:UNUSED_PAD src0_sel:DWORD src1_sel:WORD_1\n"
  "v_lshlrev_b32_sdwa v162, 1, v162 dst_sel:DWORD dst_unused:UNUSED_PAD src0_sel:DWORD src1_sel:WORD_1\n"
  "v_mfma_f32_16x16x32_bf16 v[168:171], v[228:231], v[96:99], v[168:171]\n"
  "ds_read_u16 v160, v160\n"
  "v_exp_f16 v212, v208\n"
  "v_mfma_f32_16x16x32_bf16 v[172:175], v[228:231], v[100:103], v[172:175]\n"
  "ds_read_u16_d16_hi v161, v161\n"
  "v_exp_f16 v213, v209\n"
  "v_mfma_f32_16x16x32_bf16 v[176:179], v[228:231], v[76:79], v[176:179]\n"
  "s_waitcnt lgkmcnt(8)\n"
  "ds_read_b128 v[228:231], v0 offset:36864\n"
  "v_lshlrev_b32_sdwa v163, 1, v163 dst_sel:DWORD dst_unused:UNUSED_PAD src0_sel:DWORD src1_sel:WORD_1\n"
  "v_exp_f16_sdwa v212, v208 dst_sel:WORD_1 dst_unused:UNUSED_PRESERVE src0_sel:WORD_1\n"
  "v_mfma_f32_16x16x32_f16 v[104:107], v[216:219], v[192:195], v[104:107]\n"
  "ds_read_u16 v162, v162\n"
  "v_exp_f16_sdwa v213, v209 dst_sel:WORD_1 dst_unused:UNUSED_PRESERVE src0_sel:WORD_1\n"
  "v_mfma_f32_16x16x32_f16 v[8:11], v[216:219], v[196:199], v[8:11]\n"
  "ds_read_u16_d16_hi v163, v163\n"
  "v_pk_add_f16 v212, v212, 1.0 op_sel_hi:[1,0]\n"
  "v_mfma_f32_16x16x32_f16 v[128:131], v[216:219], v[180:183], v[128:131]\n"
  "s_waitcnt lgkmcnt(9)\n"
  "ds_read_b128 v[216:219], v0 offset:38912\n"
  "v_pk_add_f16 v213, v213, 1.0 op_sel_hi:[1,0]\n"
  "v_rcp_f16 v208, v212\n"
  "v_mfma_f32_16x16x32_f16 v[108:111], v[220:223], v[192:195], v[108:111]\n"
  "v_rcp_f16 v209, v213\n"
  "v_mfma_f32_16x16x32_f16 v[12:15], v[220:223], v[196:199], v[12:15]\n"
  "v_rcp_f16_sdwa v208, v212 dst_sel:WORD_1 dst_unused:UNUSED_PRESERVE src0_sel:WORD_1\n"
  "v_mfma_f32_16x16x32_f16 v[132:135], v[220:223], v[180:183], v[132:135]\n"
  "s_waitcnt lgkmcnt(6)\n"
  "ds_read_b128 v[220:223], v0 offset:40960\n"
  "v_rcp_f16_sdwa v209, v213 dst_sel:WORD_1 dst_unused:UNUSED_PRESERVE src0_sel:WORD_1\n"
  "v_mfma_f32_16x16x32_f16 v[112:115], v[224:227], v[192:195], v[112:115]\n"
  "v_or_b32 v188, v156, v157\n"
  "v_pk_mul_f16 v184, v184, v208\n"
  "v_mfma_f32_16x16x32_f16 v[16:19], v[224:227], v[196:199], v[16:19]\n"
  "v_pk_mul_f16 v185, v185, v209\n"
  "v_or_b32 v189, v158, v159\n"
  "v_mfma_f32_16x16x32_f16 v[136:139], v[224:227], v[180:183], v[136:139]\n"
  "s_waitcnt lgkmcnt(2)\n"
  "ds_read_b128 v[224:227], v2 offset:41984\n"
  "v_or_b32 v164, v160, v161\n"
  "v_or_b32 v165, v162, v163\n"
  "v_mfma_f32_16x16x32_bf16 v[152:155], v[228:231], v[80:83], 0\n"
  "v_lshlrev_b32_sdwa v172, 1, v172 dst_sel:DWORD dst_unused:UNUSED_PAD src0_sel:DWORD src1_sel:WORD_1\n"
  "v_lshlrev_b32_sdwa v173, 1, v173 dst_sel:DWORD dst_unused:UNUSED_PAD src0_sel:DWORD src1_sel:WORD_1\n"
  "v_mfma_f32_16x16x32_bf16 v[156:159], v[228:231], v[84:87], 0\n"
  "ds_read_u16 v172, v172\n"
  "v_lshlrev_b32_sdwa v174, 1, v174 dst_sel:DWORD dst_unused:UNUSED_PAD src0_sel:DWORD src1_sel:WORD_1\n"
  "v_cvt_pk_f16_f32 v186, v168, v169\n"
  "v_mfma_f32_16x16x32_bf16 v[160:163], v[228:231], v[60:63], 0\n"
  "s_waitcnt lgkmcnt(3)\n"
  "ds_read_b128 v[228:231], v2 offset:43008\n"
  "v_cvt_pk_f16_f32 v187, v170, v171\n"
  "v_pk_fma_f16 v208, v186, v186, s56\n"
  "v_mfma_f32_16x16x32_bf16 v[152:155], v[216:219], v[88:91], v[152:155]\n"
  "ds_read_u16_d16_hi v173, v173\n"
  "v_pk_fma_f16 v209, v187, v187, s56\n"
  "v_lshlrev_b32_sdwa v175, 1, v175 dst_sel:DWORD dst_unused:UNUSED_PAD src0_sel:DWORD src1_sel:WORD_1\n"
  "v_mfma_f32_16x16x32_bf16 v[156:159], v[216:219], v[92:95], v[156:159]\n"
  "ds_read_u16 v174, v174\n"
  "v_lshlrev_b32_sdwa v176, 1, v176 dst_sel:DWORD dst_unused:UNUSED_PAD src0_sel:DWORD src1_sel:WORD_1\n"
  "v_pk_mul_f16 v208, v186, v208\n"
  "v_pk_mul_f16 v209, v187, v209\n"
  "v_mfma_f32_16x16x32_bf16 v[160:163], v[216:219], v[68:71], v[160:163]\n"
  "s_waitcnt lgkmcnt(5)\n"
  "ds_read_b128 v[216:219], v2 offset:44032\n"
  "ds_read_u16_d16_hi v175, v175\n"
  "v_lshlrev_b32_sdwa v177, 1, v177 dst_sel:DWORD dst_unused:UNUSED_PAD src0_sel:DWORD src1_sel:WORD_1\n"
  "v_lshlrev_b32_sdwa v178, 1, v178 dst_sel:DWORD dst_unused:UNUSED_PAD src0_sel:DWORD src1_sel:WORD_1\n"
  "v_mfma_f32_16x16x32_bf16 v[152:155], v[220:223], v[96:99], v[152:155]\n"
  "ds_read_u16 v176, v176\n"
  "v_exp_f16 v212, v208\n"
  "v_mfma_f32_16x16x32_bf16 v[156:159], v[220:223], v[100:103], v[156:159]\n"
  "ds_read_u16_d16_hi v177, v177\n"
  "v_exp_f16 v213, v209\n"
  "v_mfma_f32_16x16x32_bf16 v[160:163], v[220:223], v[76:79], v[160:163]\n"
  "s_waitcnt lgkmcnt(8)\n"
  "ds_read_b128 v[220:223], v0 offset:37120\n"
  "v_lshlrev_b32_sdwa v179, 1, v179 dst_sel:DWORD dst_unused:UNUSED_PAD src0_sel:DWORD src1_sel:WORD_1\n"
  "v_exp_f16_sdwa v212, v208 dst_sel:WORD_1 dst_unused:UNUSED_PRESERVE src0_sel:WORD_1\n"
  "v_mfma_f32_16x16x32_f16 v[116:119], v[224:227], v[192:195], v[116:119]\n"
  "ds_read_u16 v178, v178\n"
  "v_exp_f16_sdwa v213, v209 dst_sel:WORD_1 dst_unused:UNUSED_PRESERVE src0_sel:WORD_1\n"
  "v_mfma_f32_16x16x32_f16 v[20:23], v[224:227], v[196:199], v[20:23]\n"
  "ds_read_u16_d16_hi v179, v179\n"
  "v_pk_add_f16 v212, v212, 1.0 op_sel_hi:[1,0]\n"
  "v_mfma_f32_16x16x32_f16 v[140:143], v[224:227], v[180:183], v[140:143]\n"
  "s_waitcnt lgkmcnt(9)\n"
  "ds_read_b128 v[224:227], v0 offset:39168\n"
  "v_pk_add_f16 v213, v213, 1.0 op_sel_hi:[1,0]\n"
  "v_rcp_f16 v208, v212\n"
  "v_mfma_f32_16x16x32_f16 v[120:123], v[228:231], v[192:195], v[120:123]\n"
  "v_rcp_f16 v209, v213\n"
  "v_mfma_f32_16x16x32_f16 v[24:27], v[228:231], v[196:199], v[24:27]\n"
  "v_rcp_f16_sdwa v208, v212 dst_sel:WORD_1 dst_unused:UNUSED_PRESERVE src0_sel:WORD_1\n"
  "v_mfma_f32_16x16x32_f16 v[144:147], v[228:231], v[180:183], v[144:147]\n"
  "s_waitcnt lgkmcnt(6)\n"
  "ds_read_b128 v[228:231], v0 offset:41216\n"
  "v_rcp_f16_sdwa v209, v213 dst_sel:WORD_1 dst_unused:UNUSED_PRESERVE src0_sel:WORD_1\n"
  "v_mfma_f32_16x16x32_f16 v[124:127], v[216:219], v[192:195], v[124:127]\n"
  "v_or_b32 v190, v172, v173\n"
  "v_pk_mul_f16 v186, v186, v208\n"
  "v_mfma_f32_16x16x32_f16 v[28:31], v[216:219], v[196:199], v[28:31]\n"
  "v_pk_mul_f16 v187, v187, v209\n"
  "v_or_b32 v191, v174, v175\n"
  "v_mfma_f32_16x16x32_f16 v[148:151], v[216:219], v[180:183], v[148:151]\n"
  "s_waitcnt lgkmcnt(2)\n"
  "ds_read_b128 v[216:219], v2 offset:45056\n"
  "v_or_b32 v166, v176, v177\n"
  "v_or_b32 v167, v178, v179\n"
  "s_setprio 2\n"
  "v_mfma_f32_16x16x32_bf16 v[168:171], v[220:223], v[80:83], 0\n"
  "v_lshlrev_b32_sdwa v156, 1, v156 dst_sel:DWORD dst_unused:UNUSED_PAD src0_sel:DWORD src1_sel:WORD_1\n"
  "v_lshlrev_b32_sdwa v157, 1, v157 dst_sel:DWORD dst_unused:UNUSED_PAD src0_sel:DWORD src1_sel:WORD_1\n"
  "v_mfma_f32_16x16x32_bf16 v[172:175], v[220:223], v[84:87], 0\n"
  "ds_read_u16 v156, v156\n"
  "v_lshlrev_b32_sdwa v158, 1, v158 dst_sel:DWORD dst_unused:UNUSED_PAD src0_sel:DWORD src1_sel:WORD_1\n"
  "v_cvt_pk_f16_f32 v192, v152, v153\n"
  "v_mfma_f32_16x16x32_bf16 v[176:179], v[220:223], v[60:63], 0\n"
  "s_waitcnt lgkmcnt(3)\n"
  "ds_read_b128 v[220:223], v2 offset:46080\n"
  "v_cvt_pk_f16_f32 v193, v154, v155\n"
  "v_pk_fma_f16 v208, v192, v192, s56\n"
  "v_mfma_f32_16x16x32_bf16 v[168:171], v[224:227], v[88:91], v[168:171]\n"
  "ds_read_u16_d16_hi v157, v157\n"
  "v_pk_fma_f16 v209, v193, v193, s56\n"
  "v_lshlrev_b32_sdwa v159, 1, v159 dst_sel:DWORD dst_unused:UNUSED_PAD src0_sel:DWORD src1_sel:WORD_1\n"
  "v_mfma_f32_16x16x32_bf16 v[172:175], v[224:227], v[92:95], v[172:175]\n"
  "ds_read_u16 v158, v158\n"
  "v_lshlrev_b32_sdwa v160, 1, v160 dst_sel:DWORD dst_unused:UNUSED_PAD src0_sel:DWORD src1_sel:WORD_1\n"
  "v_pk_mul_f16 v208, v192, v208\n"
  "v_pk_mul_f16 v209, v193, v209\n"
  "v_mfma_f32_16x16x32_bf16 v[176:179], v[224:227], v[68:71], v[176:179]\n"
  "s_waitcnt lgkmcnt(5)\n"
  "ds_read_b128 v[224:227], v2 offset:47104\n"
  "ds_read_u16_d16_hi v159, v159\n"
  "v_lshlrev_b32_sdwa v161, 1, v161 dst_sel:DWORD dst_unused:UNUSED_PAD src0_sel:DWORD src1_sel:WORD_1\n"
  "v_lshlrev_b32_sdwa v162, 1, v162 dst_sel:DWORD dst_unused:UNUSED_PAD src0_sel:DWORD src1_sel:WORD_1\n"
  "v_mfma_f32_16x16x32_bf16 v[168:171], v[228:231], v[96:99], v[168:171]\n"
  "ds_read_u16 v160, v160\n"
  "v_exp_f16 v212, v208\n"
  "v_mfma_f32_16x16x32_bf16 v[172:175], v[228:231], v[100:103], v[172:175]\n"
  "ds_read_u16_d16_hi v161, v161\n"
  "v_exp_f16 v213, v209\n"
  "v_mfma_f32_16x16x32_bf16 v[176:179], v[228:231], v[76:79], v[176:179]\n"
  "s_waitcnt lgkmcnt(8)\n"
  "ds_read_b128 v[228:231], v0 offset:43008\n"
  "v_lshlrev_b32_sdwa v163, 1, v163 dst_sel:DWORD dst_unused:UNUSED_PAD src0_sel:DWORD src1_sel:WORD_1\n"
  "v_exp_f16_sdwa v212, v208 dst_sel:WORD_1 dst_unused:UNUSED_PRESERVE src0_sel:WORD_1\n"
  "v_mfma_f32_16x16x32_f16 v[104:107], v[216:219], v[184:187], v[104:107]\n"
  "ds_read_u16 v162, v162\n"
  "v_exp_f16_sdwa v213, v209 dst_sel:WORD_1 dst_unused:UNUSED_PRESERVE src0_sel:WORD_1\n"
  "v_mfma_f32_16x16x32_f16 v[8:11], v[216:219], v[188:191], v[8:11]\n"
  "ds_read_u16_d16_hi v163, v163\n"
  "v_pk_add_f16 v212, v212, 1.0 op_sel_hi:[1,0]\n"
  "v_mfma_f32_16x16x32_f16 v[128:131], v[216:219], v[164:167], v[128:131]\n"
  "s_waitcnt lgkmcnt(9)\n"
  "ds_read_b128 v[216:219], v0 offset:45056\n"
  "v_pk_add_f16 v213, v213, 1.0 op_sel_hi:[1,0]\n"
  "v_rcp_f16 v208, v212\n"
  "v_mfma_f32_16x16x32_f16 v[108:111], v[220:223], v[184:187], v[108:111]\n"
  "v_rcp_f16 v209, v213\n"
  "v_mfma_f32_16x16x32_f16 v[12:15], v[220:223], v[188:191], v[12:15]\n"
  "v_rcp_f16_sdwa v208, v212 dst_sel:WORD_1 dst_unused:UNUSED_PRESERVE src0_sel:WORD_1\n"
  "v_mfma_f32_16x16x32_f16 v[132:135], v[220:223], v[164:167], v[132:135]\n"
  "s_waitcnt lgkmcnt(6)\n"
  "ds_read_b128 v[220:223], v0 offset:47104\n"
  "v_rcp_f16_sdwa v209, v213 dst_sel:WORD_1 dst_unused:UNUSED_PRESERVE src0_sel:WORD_1\n"
  "v_mfma_f32_16x16x32_f16 v[112:115], v[224:227], v[184:187], v[112:115]\n"
  "v_or_b32 v196, v156, v157\n"
  "v_pk_mul_f16 v192, v192, v208\n"
  "v_mfma_f32_16x16x32_f16 v[16:19], v[224:227], v[188:191], v[16:19]\n"
  "v_pk_mul_f16 v193, v193, v209\n"
  "v_or_b32 v197, v158, v159\n"
  "v_mfma_f32_16x16x32_f16 v[136:139], v[224:227], v[164:167], v[136:139]\n"
  "s_waitcnt lgkmcnt(2)\n"
  "ds_read_b128 v[224:227], v2 offset:48128\n"
  "v_or_b32 v180, v160, v161\n"
  "v_or_b32 v181, v162, v163\n"
  "v_mfma_f32_16x16x32_bf16 v[152:155], v[228:231], v[80:83], 0\n"
  "v_lshlrev_b32_sdwa v172, 1, v172 dst_sel:DWORD dst_unused:UNUSED_PAD src0_sel:DWORD src1_sel:WORD_1\n"
  "v_lshlrev_b32_sdwa v173, 1, v173 dst_sel:DWORD dst_unused:UNUSED_PAD src0_sel:DWORD src1_sel:WORD_1\n"
  "v_mfma_f32_16x16x32_bf16 v[156:159], v[228:231], v[84:87], 0\n"
  "ds_read_u16 v172, v172\n"
  "v_lshlrev_b32_sdwa v174, 1, v174 dst_sel:DWORD dst_unused:UNUSED_PAD src0_sel:DWORD src1_sel:WORD_1\n"
  "v_cvt_pk_f16_f32 v194, v168, v169\n"
  "v_mfma_f32_16x16x32_bf16 v[160:163], v[228:231], v[60:63], 0\n"
  "s_waitcnt lgkmcnt(3)\n"
  "ds_read_b128 v[228:231], v2 offset:49152\n"
  "v_cvt_pk_f16_f32 v195, v170, v171\n"
  "v_pk_fma_f16 v208, v194, v194, s56\n"
  "v_mfma_f32_16x16x32_bf16 v[152:155], v[216:219], v[88:91], v[152:155]\n"
  "ds_read_u16_d16_hi v173, v173\n"
  "v_pk_fma_f16 v209, v195, v195, s56\n"
  "v_lshlrev_b32_sdwa v175, 1, v175 dst_sel:DWORD dst_unused:UNUSED_PAD src0_sel:DWORD src1_sel:WORD_1\n"
  "v_mfma_f32_16x16x32_bf16 v[156:159], v[216:219], v[92:95], v[156:159]\n"
  "ds_read_u16 v174, v174\n"
  "v_lshlrev_b32_sdwa v176, 1, v176 dst_sel:DWORD dst_unused:UNUSED_PAD src0_sel:DWORD src1_sel:WORD_1\n"
  "v_pk_mul_f16 v208, v194, v208\n"
  "v_pk_mul_f16 v209, v195, v209\n"
  "v_mfma_f32_16x16x32_bf16 v[160:163], v[216:219], v[68:71], v[160:163]\n"
  "s_waitcnt lgkmcnt(5)\n"
  "ds_read_b128 v[216:219], v2 offset:50176\n"
  "ds_read_u16_d16_hi v175, v175\n"
  "v_lshlrev_b32_sdwa v177, 1, v177 dst_sel:DWORD dst_unused:UNUSED_PAD src0_sel:DWORD src1_sel:WORD_1\n"
  "v_lshlrev_b32_sdwa v178, 1, v178 dst_sel:DWORD dst_unused:UNUSED_PAD src0_sel:DWORD src1_sel:WORD_1\n"
  "v_mfma_f32_16x16x32_bf16 v[152:155], v[220:223], v[96:99], v[152:155]\n"
  "ds_read_u16 v176, v176\n"
  "v_exp_f16 v212, v208\n"
  "v_mfma_f32_16x16x32_bf16 v[156:159], v[220:223], v[100:103], v[156:159]\n"
  "ds_read_u16_d16_hi v177, v177\n"
  "v_exp_f16 v213, v209\n"
  "v_mfma_f32_16x16x32_bf16 v[160:163], v[220:223], v[76:79], v[160:163]\n"
  "s_waitcnt lgkmcnt(8)\n"
  "ds_read_b128 v[220:223], v0 offset:43264\n"
  "v_lshlrev_b32_sdwa v179, 1, v179 dst_sel:DWORD dst_unused:UNUSED_PAD src0_sel:DWORD src1_sel:WORD_1\n"
  "v_exp_f16_sdwa v212, v208 dst_sel:WORD_1 dst_unused:UNUSED_PRESERVE src0_sel:WORD_1\n"
  "v_mfma_f32_16x16x32_f16 v[116:119], v[224:227], v[184:187], v[116:119]\n"
  "ds_read_u16 v178, v178\n"
  "v_exp_f16_sdwa v213, v209 dst_sel:WORD_1 dst_unused:UNUSED_PRESERVE src0_sel:WORD_1\n"
  "v_mfma_f32_16x16x32_f16 v[20:23], v[224:227], v[188:191], v[20:23]\n"
  "ds_read_u16_d16_hi v179, v179\n"
  "v_pk_add_f16 v212, v212, 1.0 op_sel_hi:[1,0]\n"
  "v_mfma_f32_16x16x32_f16 v[140:143], v[224:227], v[164:167], v[140:143]\n"
  "s_waitcnt lgkmcnt(9)\n"
  "ds_read_b128 v[224:227], v0 offset:45312\n"
  "v_pk_add_f16 v213, v213, 1.0 op_sel_hi:[1,0]\n"
  "v_rcp_f16 v208, v212\n"
  "v_mfma_f32_16x16x32_f16 v[120:123], v[228:231], v[184:187], v[120:123]\n"
  "v_rcp_f16 v209, v213\n"
  "v_mfma_f32_16x16x32_f16 v[24:27], v[228:231], v[188:191], v[24:27]\n"
  "v_rcp_f16_sdwa v208, v212 dst_sel:WORD_1 dst_unused:UNUSED_PRESERVE src0_sel:WORD_1\n"
  "v_mfma_f32_16x16x32_f16 v[144:147], v[228:231], v[164:167], v[144:147]\n"
  "s_waitcnt lgkmcnt(6)\n"
  "ds_read_b128 v[228:231], v0 offset:47360\n"
  "v_rcp_f16_sdwa v209, v213 dst_sel:WORD_1 dst_unused:UNUSED_PRESERVE src0_sel:WORD_1\n"
  "v_mfma_f32_16x16x32_f16 v[124:127], v[216:219], v[184:187], v[124:127]\n"
  "v_or_b32 v198, v172, v173\n"
  "v_pk_mul_f16 v194, v194, v208\n"
  "v_mfma_f32_16x16x32_f16 v[28:31], v[216:219], v[188:191], v[28:31]\n"
  "v_pk_mul_f16 v195, v195, v209\n"
  "v_or_b32 v199, v174, v175\n"
  "v_mfma_f32_16x16x32_f16 v[148:151], v[216:219], v[164:167], v[148:151]\n"
  "s_waitcnt lgkmcnt(2)\n"
  "ds_read_b128 v[216:219], v2 offset:51200\n"
  "v_or_b32 v182, v176, v177\n"
  "v_or_b32 v183, v178, v179\n"
  "v_mfma_f32_16x16x32_bf16 v[168:171], v[220:223], v[80:83], 0\n"
  "v_lshlrev_b32_sdwa v156, 1, v156 dst_sel:DWORD dst_unused:UNUSED_PAD src0_sel:DWORD src1_sel:WORD_1\n"
  "v_lshlrev_b32_sdwa v157, 1, v157 dst_sel:DWORD dst_unused:UNUSED_PAD src0_sel:DWORD src1_sel:WORD_1\n"
  "v_mfma_f32_16x16x32_bf16 v[172:175], v[220:223], v[84:87], 0\n"
  "v_lshlrev_b32_sdwa v158, 1, v158 dst_sel:DWORD dst_unused:UNUSED_PAD src0_sel:DWORD src1_sel:WORD_1\n"
  "v_mfma_f32_16x16x32_bf16 v[176:179], v[220:223], v[60:63], 0\n"
  "s_waitcnt lgkmcnt(2)\n"
  "ds_read_b128 v[220:223], v2 offset:52224\n"
  "ds_read_u16 v156, v156\n"
  "v_cvt_pk_f16_f32 v184, v152, v153\n"
  "v_cvt_pk_f16_f32 v185, v154, v155\n"
  "s_waitcnt vmcnt(11)\n"
  "v_mfma_f32_16x16x32_bf16 v[8:11], v[248:251], v[236:239], v[8:11]\n"
  "ds_read_u16_d16_hi v157, v157\n"
  "v_pk_fma_f16 v208, v184, v184, s56\n"
  "v_mfma_f32_16x16x32_bf16 v[12:15], v[252:255], v[236:239], v[12:15]\n"
  "v_pk_fma_f16 v209, v185, v185, s56\n"
  "v_lshlrev_b32_sdwa v159, 1, v159 dst_sel:DWORD dst_unused:UNUSED_PAD src0_sel:DWORD src1_sel:WORD_1\n"
  "v_mfma_f32_16x16x32_bf16 v[168:171], v[224:227], v[88:91], v[168:171]\n"
  "s_waitcnt lgkmcnt(4)\n"
  "ds_read_u16 v158, v158\n"
  "v_lshlrev_b32_sdwa v160, 1, v160 dst_sel:DWORD dst_unused:UNUSED_PAD src0_sel:DWORD src1_sel:WORD_1\n"
  "v_mfma_f32_16x16x32_bf16 v[172:175], v[224:227], v[92:95], v[172:175]\n"
  "v_pk_mul_f16 v208, v184, v208\n"
  "v_pk_mul_f16 v209, v185, v209\n"
  "v_mfma_f32_16x16x32_bf16 v[176:179], v[224:227], v[68:71], v[176:179]\n"
  "ds_read_b128 v[224:227], v2 offset:53248\n"
  "ds_read_u16_d16_hi v159, v159\n"
  "v_lshlrev_b32_sdwa v161, 1, v161 dst_sel:DWORD dst_unused:UNUSED_PAD src0_sel:DWORD src1_sel:WORD_1\n"
  "v_lshlrev_b32_sdwa v162, 1, v162 dst_sel:DWORD dst_unused:UNUSED_PAD src0_sel:DWORD src1_sel:WORD_1\n"
  "v_mfma_f32_16x16x32_bf16 v[168:171], v[228:231], v[96:99], v[168:171]\n"
  "ds_read_u16 v160, v160\n"
  "v_exp_f16 v212, v208\n"
  "v_mfma_f32_16x16x32_bf16 v[172:175], v[228:231], v[100:103], v[172:175]\n"
  "v_exp_f16 v213, v209\n"
  "v_mfma_f32_16x16x32_bf16 v[176:179], v[228:231], v[76:79], v[176:179]\n"
  "ds_read_b128 v[228:231], v0 offset:49152\n"
  "ds_read_u16_d16_hi v161, v161\n"
  "s_waitcnt vmcnt(10)\n"
  "v_mfma_f32_16x16x32_bf16 v[16:19], v[248:251], v[240:243], v[16:19]\n"
  "s_waitcnt lgkmcnt(9)\n"
  "ds_read_u16 v162, v162\n"
  "v_lshlrev_b32_sdwa v163, 1, v163 dst_sel:DWORD dst_unused:UNUSED_PAD src0_sel:DWORD src1_sel:WORD_1\n"
  "v_exp_f16_sdwa v212, v208 dst_sel:WORD_1 dst_unused:UNUSED_PRESERVE src0_sel:WORD_1\n"
  "v_mfma_f32_16x16x32_bf16 v[20:23], v[252:255], v[240:243], v[20:23]\n"
  "v_exp_f16_sdwa v213, v209 dst_sel:WORD_1 dst_unused:UNUSED_PRESERVE src0_sel:WORD_1\n"
  "v_mfma_f32_16x16x32_f16 v[104:107], v[216:219], v[192:195], v[104:107]\n"
  "ds_read_u16_d16_hi v163, v163\n"
  "v_mfma_f32_16x16x32_f16 v[8:11], v[216:219], v[196:199], v[8:11]\n"
  "s_waitcnt lgkmcnt(10)\n"
  "v_pk_add_f16 v212, v212, 1.0 op_sel_hi:[1,0]\n"
  "v_pk_add_f16 v213, v213, 1.0 op_sel_hi:[1,0]\n"
  "v_mfma_f32_16x16x32_f16 v[128:131], v[216:219], v[180:183], v[128:131]\n"
  "ds_read_b128 v[216:219], v0 offset:51200\n"
  "v_rcp_f16 v208, v212\n"
  "v_mfma_f32_16x16x32_f16 v[108:111], v[220:223], v[192:195], v[108:111]\n"
  "v_rcp_f16 v209, v213\n"
  "v_mfma_f32_16x16x32_f16 v[12:15], v[220:223], v[196:199], v[12:15]\n"
  "v_mfma_f32_16x16x32_f16 v[132:135], v[220:223], v[180:183], v[132:135]\n"
  "ds_read_b128 v[220:223], v0 offset:53248\n"
  "v_rcp_f16_sdwa v208, v212 dst_sel:WORD_1 dst_unused:UNUSED_PRESERVE src0_sel:WORD_1\n"
  "s_waitcnt vmcnt(9)\n"
  "v_mfma_f32_16x16x32_bf16 v[24:27], v[248:251], v[244:247], v[24:27]\n"
  "v_rcp_f16_sdwa v209, v213 dst_sel:WORD_1 dst_unused:UNUSED_PRESERVE src0_sel:WORD_1\n"
  "v_mfma_f32_16x16x32_bf16 v[28:31], v[252:255], v[244:247], v[28:31]\n"
  "s_waitcnt lgkmcnt(7)\n"
  "buffer_load_dwordx4 v[236:239], v5, s[40:43], s59 offen offset:0 \n"
  "buffer_load_dwordx4 v[240:243], v5, s[40:43], s59 offen offset:64 \n"
  "buffer_load_dwordx4 v[244:247], v5, s[40:43], s59 offen offset:128 \n"
  "v_or_b32 v188, v156, v157\n"
  "v_mfma_f32_16x16x32_f16 v[112:115], v[224:227], v[192:195], v[112:115]\n"
  "v_pk_mul_f16 v184, v184, v208\n"
  "v_pk_mul_f16 v185, v185, v209\n"
  "v_mfma_f32_16x16x32_f16 v[16:19], v[224:227], v[196:199], v[16:19]\n"
  "v_or_b32 v189, v158, v159\n"
  "v_mfma_f32_16x16x32_f16 v[136:139], v[224:227], v[180:183], v[136:139]\n"
  "s_waitcnt lgkmcnt(2)\n"
  "ds_read_b128 v[224:227], v2 offset:54272\n"
  "v_or_b32 v164, v160, v161\n"
  "v_or_b32 v165, v162, v163\n"
  "v_mfma_f32_16x16x32_bf16 v[152:155], v[228:231], v[80:83], 0\n"
  "v_lshlrev_b32_sdwa v172, 1, v172 dst_sel:DWORD dst_unused:UNUSED_PAD src0_sel:DWORD src1_sel:WORD_1\n"
  "v_lshlrev_b32_sdwa v173, 1, v173 dst_sel:DWORD dst_unused:UNUSED_PAD src0_sel:DWORD src1_sel:WORD_1\n"
  "v_mfma_f32_16x16x32_bf16 v[156:159], v[228:231], v[84:87], 0\n"
  "ds_read_u16 v172, v172\n"
  "v_lshlrev_b32_sdwa v174, 1, v174 dst_sel:DWORD dst_unused:UNUSED_PAD src0_sel:DWORD src1_sel:WORD_1\n"
  "v_cvt_pk_f16_f32 v186, v168, v169\n"
  "v_mfma_f32_16x16x32_bf16 v[160:163], v[228:231], v[60:63], 0\n"
  "s_waitcnt lgkmcnt(3)\n"
  "ds_read_b128 v[228:231], v2 offset:55296\n"
  "v_cvt_pk_f16_f32 v187, v170, v171\n"
  "v_pk_fma_f16 v208, v186, v186, s56\n"
  "v_mfma_f32_16x16x32_bf16 v[152:155], v[216:219], v[88:91], v[152:155]\n"
  "ds_read_u16_d16_hi v173, v173\n"
  "v_pk_fma_f16 v209, v187, v187, s56\n"
  "v_lshlrev_b32_sdwa v175, 1, v175 dst_sel:DWORD dst_unused:UNUSED_PAD src0_sel:DWORD src1_sel:WORD_1\n"
  "v_mfma_f32_16x16x32_bf16 v[156:159], v[216:219], v[92:95], v[156:159]\n"
  "ds_read_u16 v174, v174\n"
  "v_lshlrev_b32_sdwa v176, 1, v176 dst_sel:DWORD dst_unused:UNUSED_PAD src0_sel:DWORD src1_sel:WORD_1\n"
  "v_pk_mul_f16 v208, v186, v208\n"
  "v_pk_mul_f16 v209, v187, v209\n"
  "v_mfma_f32_16x16x32_bf16 v[160:163], v[216:219], v[68:71], v[160:163]\n"
  "s_waitcnt lgkmcnt(5)\n"
  "ds_read_b128 v[216:219], v2 offset:56320\n"
  "ds_read_u16_d16_hi v175, v175\n"
  "v_lshlrev_b32_sdwa v177, 1, v177 dst_sel:DWORD dst_unused:UNUSED_PAD src0_sel:DWORD src1_sel:WORD_1\n"
  "v_lshlrev_b32_sdwa v178, 1, v178 dst_sel:DWORD dst_unused:UNUSED_PAD src0_sel:DWORD src1_sel:WORD_1\n"
  "v_mfma_f32_16x16x32_bf16 v[152:155], v[220:223], v[96:99], v[152:155]\n"
  "ds_read_u16 v176, v176\n"
  "v_exp_f16 v212, v208\n"
  "v_mfma_f32_16x16x32_bf16 v[156:159], v[220:223], v[100:103], v[156:159]\n"
  "ds_read_u16_d16_hi v177, v177\n"
  "v_exp_f16 v213, v209\n"
  "v_mfma_f32_16x16x32_bf16 v[160:163], v[220:223], v[76:79], v[160:163]\n"
  "s_waitcnt lgkmcnt(8)\n"
  "ds_read_b128 v[220:223], v0 offset:49408\n"
  "v_lshlrev_b32_sdwa v179, 1, v179 dst_sel:DWORD dst_unused:UNUSED_PAD src0_sel:DWORD src1_sel:WORD_1\n"
  "v_exp_f16_sdwa v212, v208 dst_sel:WORD_1 dst_unused:UNUSED_PRESERVE src0_sel:WORD_1\n"
  "v_mfma_f32_16x16x32_f16 v[116:119], v[224:227], v[192:195], v[116:119]\n"
  "ds_read_u16 v178, v178\n"
  "v_exp_f16_sdwa v213, v209 dst_sel:WORD_1 dst_unused:UNUSED_PRESERVE src0_sel:WORD_1\n"
  "v_mfma_f32_16x16x32_f16 v[20:23], v[224:227], v[196:199], v[20:23]\n"
  "ds_read_u16_d16_hi v179, v179\n"
  "v_pk_add_f16 v212, v212, 1.0 op_sel_hi:[1,0]\n"
  "v_mfma_f32_16x16x32_f16 v[140:143], v[224:227], v[180:183], v[140:143]\n"
  "s_waitcnt lgkmcnt(9)\n"
  "ds_read_b128 v[224:227], v0 offset:51456\n"
  "v_pk_add_f16 v213, v213, 1.0 op_sel_hi:[1,0]\n"
  "v_rcp_f16 v208, v212\n"
  "v_mfma_f32_16x16x32_f16 v[120:123], v[228:231], v[192:195], v[120:123]\n"
  "v_rcp_f16 v209, v213\n"
  "v_mfma_f32_16x16x32_f16 v[24:27], v[228:231], v[196:199], v[24:27]\n"
  "v_rcp_f16_sdwa v208, v212 dst_sel:WORD_1 dst_unused:UNUSED_PRESERVE src0_sel:WORD_1\n"
  "v_mfma_f32_16x16x32_f16 v[144:147], v[228:231], v[180:183], v[144:147]\n"
  "s_waitcnt lgkmcnt(6)\n"
  "ds_read_b128 v[228:231], v0 offset:53504\n"
  "v_rcp_f16_sdwa v209, v213 dst_sel:WORD_1 dst_unused:UNUSED_PRESERVE src0_sel:WORD_1\n"
  "v_mfma_f32_16x16x32_f16 v[124:127], v[216:219], v[192:195], v[124:127]\n"
  "v_or_b32 v190, v172, v173\n"
  "v_pk_mul_f16 v186, v186, v208\n"
  "v_mfma_f32_16x16x32_f16 v[28:31], v[216:219], v[196:199], v[28:31]\n"
  "v_pk_mul_f16 v187, v187, v209\n"
  "v_or_b32 v191, v174, v175\n"
  "v_mfma_f32_16x16x32_f16 v[148:151], v[216:219], v[180:183], v[148:151]\n"
  "s_waitcnt lgkmcnt(2)\n"
  "ds_read_b128 v[216:219], v2 offset:57344\n"
  "v_or_b32 v166, v176, v177\n"
  "v_or_b32 v167, v178, v179\n"
  "v_mfma_f32_16x16x32_bf16 v[168:171], v[220:223], v[80:83], 0\n"
  "v_lshlrev_b32_sdwa v156, 1, v156 dst_sel:DWORD dst_unused:UNUSED_PAD src0_sel:DWORD src1_sel:WORD_1\n"
  "v_lshlrev_b32_sdwa v157, 1, v157 dst_sel:DWORD dst_unused:UNUSED_PAD src0_sel:DWORD src1_sel:WORD_1\n"
  "v_mfma_f32_16x16x32_bf16 v[172:175], v[220:223], v[84:87], 0\n"
  "ds_read_u16 v156, v156\n"
  "v_lshlrev_b32_sdwa v158, 1, v158 dst_sel:DWORD dst_unused:UNUSED_PAD src0_sel:DWORD src1_sel:WORD_1\n"
  "v_cvt_pk_f16_f32 v192, v152, v153\n"
  "v_mfma_f32_16x16x32_bf16 v[176:179], v[220:223], v[60:63], 0\n"
  "s_waitcnt lgkmcnt(3)\n"
  "ds_read_b128 v[220:223], v2 offset:58368\n"
  "v_cvt_pk_f16_f32 v193, v154, v155\n"
  "v_pk_fma_f16 v208, v192, v192, s56\n"
  "v_mfma_f32_16x16x32_bf16 v[168:171], v[224:227], v[88:91], v[168:171]\n"
  "ds_read_u16_d16_hi v157, v157\n"
  "v_pk_fma_f16 v209, v193, v193, s56\n"
  "v_lshlrev_b32_sdwa v159, 1, v159 dst_sel:DWORD dst_unused:UNUSED_PAD src0_sel:DWORD src1_sel:WORD_1\n"
  "v_mfma_f32_16x16x32_bf16 v[172:175], v[224:227], v[92:95], v[172:175]\n"
  "ds_read_u16 v158, v158\n"
  "v_lshlrev_b32_sdwa v160, 1, v160 dst_sel:DWORD dst_unused:UNUSED_PAD src0_sel:DWORD src1_sel:WORD_1\n"
  "v_pk_mul_f16 v208, v192, v208\n"
  "v_pk_mul_f16 v209, v193, v209\n"
  "v_mfma_f32_16x16x32_bf16 v[176:179], v[224:227], v[68:71], v[176:179]\n"
  "s_waitcnt lgkmcnt(5)\n"
  "ds_read_b128 v[224:227], v2 offset:59392\n"
  "ds_read_u16_d16_hi v159, v159\n"
  "v_lshlrev_b32_sdwa v161, 1, v161 dst_sel:DWORD dst_unused:UNUSED_PAD src0_sel:DWORD src1_sel:WORD_1\n"
  "v_lshlrev_b32_sdwa v162, 1, v162 dst_sel:DWORD dst_unused:UNUSED_PAD src0_sel:DWORD src1_sel:WORD_1\n"
  "v_mfma_f32_16x16x32_bf16 v[168:171], v[228:231], v[96:99], v[168:171]\n"
  "ds_read_u16 v160, v160\n"
  "v_exp_f16 v212, v208\n"
  "v_mfma_f32_16x16x32_bf16 v[172:175], v[228:231], v[100:103], v[172:175]\n"
  "ds_read_u16_d16_hi v161, v161\n"
  "v_exp_f16 v213, v209\n"
  "v_mfma_f32_16x16x32_bf16 v[176:179], v[228:231], v[76:79], v[176:179]\n"
  "s_waitcnt lgkmcnt(8)\n"
  "ds_read_b128 v[228:231], v0 offset:55296\n"
  "v_lshlrev_b32_sdwa v163, 1, v163 dst_sel:DWORD dst_unused:UNUSED_PAD src0_sel:DWORD src1_sel:WORD_1\n"
  "v_exp_f16_sdwa v212, v208 dst_sel:WORD_1 dst_unused:UNUSED_PRESERVE src0_sel:WORD_1\n"
  "v_mfma_f32_16x16x32_f16 v[104:107], v[216:219], v[184:187], v[104:107]\n"
  "ds_read_u16 v162, v162\n"
  "v_exp_f16_sdwa v213, v209 dst_sel:WORD_1 dst_unused:UNUSED_PRESERVE src0_sel:WORD_1\n"
  "v_mfma_f32_16x16x32_f16 v[8:11], v[216:219], v[188:191], v[8:11]\n"
  "ds_read_u16_d16_hi v163, v163\n"
  "v_pk_add_f16 v212, v212, 1.0 op_sel_hi:[1,0]\n"
  "v_mfma_f32_16x16x32_f16 v[128:131], v[216:219], v[164:167], v[128:131]\n"
  "s_waitcnt lgkmcnt(9)\n"
  "ds_read_b128 v[216:219], v0 offset:57344\n"
  "v_pk_add_f16 v213, v213, 1.0 op_sel_hi:[1,0]\n"
  "v_rcp_f16 v208, v212\n"
  "v_mfma_f32_16x16x32_f16 v[108:111], v[220:223], v[184:187], v[108:111]\n"
  "v_rcp_f16 v209, v213\n"
  "v_mfma_f32_16x16x32_f16 v[12:15], v[220:223], v[188:191], v[12:15]\n"
  "v_rcp_f16_sdwa v208, v212 dst_sel:WORD_1 dst_unused:UNUSED_PRESERVE src0_sel:WORD_1\n"
  "v_mfma_f32_16x16x32_f16 v[132:135], v[220:223], v[164:167], v[132:135]\n"
  "s_waitcnt lgkmcnt(6)\n"
  "ds_read_b128 v[220:223], v0 offset:59392\n"
  "v_rcp_f16_sdwa v209, v213 dst_sel:WORD_1 dst_unused:UNUSED_PRESERVE src0_sel:WORD_1\n"
  "v_mfma_f32_16x16x32_f16 v[112:115], v[224:227], v[184:187], v[112:115]\n"
  "v_or_b32 v196, v156, v157\n"
  "v_pk_mul_f16 v192, v192, v208\n"
  "v_mfma_f32_16x16x32_f16 v[16:19], v[224:227], v[188:191], v[16:19]\n"
  "v_pk_mul_f16 v193, v193, v209\n"
  "v_or_b32 v197, v158, v159\n"
  "v_mfma_f32_16x16x32_f16 v[136:139], v[224:227], v[164:167], v[136:139]\n"
  "s_waitcnt lgkmcnt(2)\n"
  "ds_read_b128 v[224:227], v2 offset:60416\n"
  "v_or_b32 v180, v160, v161\n"
  "v_or_b32 v181, v162, v163\n"
  "v_mfma_f32_16x16x32_bf16 v[152:155], v[228:231], v[80:83], 0\n"
  "v_lshlrev_b32_sdwa v172, 1, v172 dst_sel:DWORD dst_unused:UNUSED_PAD src0_sel:DWORD src1_sel:WORD_1\n"
  "v_lshlrev_b32_sdwa v173, 1, v173 dst_sel:DWORD dst_unused:UNUSED_PAD src0_sel:DWORD src1_sel:WORD_1\n"
  "v_mfma_f32_16x16x32_bf16 v[156:159], v[228:231], v[84:87], 0\n"
  "ds_read_u16 v172, v172\n"
  "v_lshlrev_b32_sdwa v174, 1, v174 dst_sel:DWORD dst_unused:UNUSED_PAD src0_sel:DWORD src1_sel:WORD_1\n"
  "v_cvt_pk_f16_f32 v194, v168, v169\n"
  "v_mfma_f32_16x16x32_bf16 v[160:163], v[228:231], v[60:63], 0\n"
  "s_waitcnt lgkmcnt(3)\n"
  "ds_read_b128 v[228:231], v2 offset:61440\n"
  "v_cvt_pk_f16_f32 v195, v170, v171\n"
  "v_pk_fma_f16 v208, v194, v194, s56\n"
  "v_mfma_f32_16x16x32_bf16 v[152:155], v[216:219], v[88:91], v[152:155]\n"
  "ds_read_u16_d16_hi v173, v173\n"
  "v_pk_fma_f16 v209, v195, v195, s56\n"
  "v_lshlrev_b32_sdwa v175, 1, v175 dst_sel:DWORD dst_unused:UNUSED_PAD src0_sel:DWORD src1_sel:WORD_1\n"
  "v_mfma_f32_16x16x32_bf16 v[156:159], v[216:219], v[92:95], v[156:159]\n"
  "ds_read_u16 v174, v174\n"
  "v_lshlrev_b32_sdwa v176, 1, v176 dst_sel:DWORD dst_unused:UNUSED_PAD src0_sel:DWORD src1_sel:WORD_1\n"
  "v_pk_mul_f16 v208, v194, v208\n"
  "v_pk_mul_f16 v209, v195, v209\n"
  "v_mfma_f32_16x16x32_bf16 v[160:163], v[216:219], v[68:71], v[160:163]\n"
  "s_waitcnt lgkmcnt(5)\n"
  "ds_read_b128 v[216:219], v2 offset:62464\n"
  "ds_read_u16_d16_hi v175, v175\n"
  "v_lshlrev_b32_sdwa v177, 1, v177 dst_sel:DWORD dst_unused:UNUSED_PAD src0_sel:DWORD src1_sel:WORD_1\n"
  "v_lshlrev_b32_sdwa v178, 1, v178 dst_sel:DWORD dst_unused:UNUSED_PAD src0_sel:DWORD src1_sel:WORD_1\n"
  "v_mfma_f32_16x16x32_bf16 v[152:155], v[220:223], v[96:99], v[152:155]\n"
  "ds_read_u16 v176, v176\n"
  "v_exp_f16 v212, v208\n"
  "v_mfma_f32_16x16x32_bf16 v[156:159], v[220:223], v[100:103], v[156:159]\n"
  "ds_read_u16_d16_hi v177, v177\n"
  "v_exp_f16 v213, v209\n"
  "v_mfma_f32_16x16x32_bf16 v[160:163], v[220:223], v[76:79], v[160:163]\n"
  "s_waitcnt lgkmcnt(8)\n"
  "ds_read_b128 v[220:223], v0 offset:55552\n"
  "v_lshlrev_b32_sdwa v179, 1, v179 dst_sel:DWORD dst_unused:UNUSED_PAD src0_sel:DWORD src1_sel:WORD_1\n"
  "v_exp_f16_sdwa v212, v208 dst_sel:WORD_1 dst_unused:UNUSED_PRESERVE src0_sel:WORD_1\n"
  "v_mfma_f32_16x16x32_f16 v[116:119], v[224:227], v[184:187], v[116:119]\n"
  "ds_read_u16 v178, v178\n"
  "v_exp_f16_sdwa v213, v209 dst_sel:WORD_1 dst_unused:UNUSED_PRESERVE src0_sel:WORD_1\n"
  "v_mfma_f32_16x16x32_f16 v[20:23], v[224:227], v[188:191], v[20:23]\n"
  "ds_read_u16_d16_hi v179, v179\n"
  "v_pk_add_f16 v212, v212, 1.0 op_sel_hi:[1,0]\n"
  "v_mfma_f32_16x16x32_f16 v[140:143], v[224:227], v[164:167], v[140:143]\n"
  "s_waitcnt lgkmcnt(9)\n"
  "ds_read_b128 v[224:227], v0 offset:57600\n"
  "v_pk_add_f16 v213, v213, 1.0 op_sel_hi:[1,0]\n"
  "v_rcp_f16 v208, v212\n"
  "v_mfma_f32_16x16x32_f16 v[120:123], v[228:231], v[184:187], v[120:123]\n"
  "v_rcp_f16 v209, v213\n"
  "v_mfma_f32_16x16x32_f16 v[24:27], v[228:231], v[188:191], v[24:27]\n"
  "v_rcp_f16_sdwa v208, v212 dst_sel:WORD_1 dst_unused:UNUSED_PRESERVE src0_sel:WORD_1\n"
  "v_mfma_f32_16x16x32_f16 v[144:147], v[228:231], v[164:167], v[144:147]\n"
  "s_waitcnt lgkmcnt(6)\n"
  "ds_read_b128 v[228:231], v0 offset:59648\n"
  "v_rcp_f16_sdwa v209, v213 dst_sel:WORD_1 dst_unused:UNUSED_PRESERVE src0_sel:WORD_1\n"
  "v_mfma_f32_16x16x32_f16 v[124:127], v[216:219], v[184:187], v[124:127]\n"
  "v_or_b32 v198, v172, v173\n"
  "v_pk_mul_f16 v194, v194, v208\n"
  "v_mfma_f32_16x16x32_f16 v[28:31], v[216:219], v[188:191], v[28:31]\n"
  "v_pk_mul_f16 v195, v195, v209\n"
  "v_or_b32 v199, v174, v175\n"
  "v_mfma_f32_16x16x32_f16 v[148:151], v[216:219], v[164:167], v[148:151]\n"
  "s_waitcnt lgkmcnt(2)\n"
  "ds_read_b128 v[216:219], v2 offset:63488\n"
  "v_or_b32 v182, v176, v177\n"
  "v_or_b32 v183, v178, v179\n"
  "v_mfma_f32_16x16x32_bf16 v[168:171], v[220:223], v[80:83], 0\n"
  "v_lshlrev_b32_sdwa v156, 1, v156 dst_sel:DWORD dst_unused:UNUSED_PAD src0_sel:DWORD src1_sel:WORD_1\n"
  "v_lshlrev_b32_sdwa v157, 1, v157 dst_sel:DWORD dst_unused:UNUSED_PAD src0_sel:DWORD src1_sel:WORD_1\n"
  "v_mfma_f32_16x16x32_bf16 v[172:175], v[220:223], v[84:87], 0\n"
  "ds_read_u16 v156, v156\n"
  "v_lshlrev_b32_sdwa v158, 1, v158 dst_sel:DWORD dst_unused:UNUSED_PAD src0_sel:DWORD src1_sel:WORD_1\n"
  "v_cvt_pk_f16_f32 v184, v152, v153\n"
  "v_mfma_f32_16x16x32_bf16 v[176:179], v[220:223], v[60:63], 0\n"
  "s_waitcnt lgkmcnt(3)\n"
  "ds_read_b128 v[220:223], v2 offset:64512\n"
  "v_cvt_pk_f16_f32 v185, v154, v155\n"
  "v_pk_fma_f16 v208, v184, v184, s56\n"
  "v_mfma_f32_16x16x32_bf16 v[168:171], v[224:227], v[88:91], v[168:171]\n"
  "ds_read_u16_d16_hi v157, v157\n"
  "v_pk_fma_f16 v209, v185, v185, s56\n"
  "v_lshlrev_b32_sdwa v159, 1, v159 dst_sel:DWORD dst_unused:UNUSED_PAD src0_sel:DWORD src1_sel:WORD_1\n"
  "v_mfma_f32_16x16x32_bf16 v[172:175], v[224:227], v[92:95], v[172:175]\n"
  "ds_read_u16 v158, v158\n"
  "v_lshlrev_b32_sdwa v160, 1, v160 dst_sel:DWORD dst_unused:UNUSED_PAD src0_sel:DWORD src1_sel:WORD_1\n"
  "v_pk_mul_f16 v208, v184, v208\n"
  "v_pk_mul_f16 v209, v185, v209\n"
  "v_mfma_f32_16x16x32_bf16 v[176:179], v[224:227], v[68:71], v[176:179]\n"
  "s_waitcnt lgkmcnt(5)\n"
  "ds_read_b128 v[224:227], v3 offset:0\n"
  "ds_read_u16_d16_hi v159, v159\n"
  "v_lshlrev_b32_sdwa v161, 1, v161 dst_sel:DWORD dst_unused:UNUSED_PAD src0_sel:DWORD src1_sel:WORD_1\n"
  "v_lshlrev_b32_sdwa v162, 1, v162 dst_sel:DWORD dst_unused:UNUSED_PAD src0_sel:DWORD src1_sel:WORD_1\n"
  "v_mfma_f32_16x16x32_bf16 v[168:171], v[228:231], v[96:99], v[168:171]\n"
  "ds_read_u16 v160, v160\n"
  "v_exp_f16 v212, v208\n"
  "v_mfma_f32_16x16x32_bf16 v[172:175], v[228:231], v[100:103], v[172:175]\n"
  "ds_read_u16_d16_hi v161, v161\n"
  "v_exp_f16 v213, v209\n"
  "v_mfma_f32_16x16x32_bf16 v[176:179], v[228:231], v[76:79], v[176:179]\n"
  "s_waitcnt lgkmcnt(8)\n"
  "ds_read_b128 v[228:231], v0 offset:61440\n"
  "v_lshlrev_b32_sdwa v163, 1, v163 dst_sel:DWORD dst_unused:UNUSED_PAD src0_sel:DWORD src1_sel:WORD_1\n"
  "v_exp_f16_sdwa v212, v208 dst_sel:WORD_1 dst_unused:UNUSED_PRESERVE src0_sel:WORD_1\n"
  "v_mfma_f32_16x16x32_f16 v[104:107], v[216:219], v[192:195], v[104:107]\n"
  "ds_read_u16 v162, v162\n"
  "v_exp_f16_sdwa v213, v209 dst_sel:WORD_1 dst_unused:UNUSED_PRESERVE src0_sel:WORD_1\n"
  "v_mfma_f32_16x16x32_f16 v[8:11], v[216:219], v[196:199], v[8:11]\n"
  "ds_read_u16_d16_hi v163, v163\n"
  "v_pk_add_f16 v212, v212, 1.0 op_sel_hi:[1,0]\n"
  "v_mfma_f32_16x16x32_f16 v[128:131], v[216:219], v[180:183], v[128:131]\n"
  "s_waitcnt lgkmcnt(9)\n"
  "ds_read_b128 v[216:219], v0 offset:63488\n"
  "v_pk_add_f16 v213, v213, 1.0 op_sel_hi:[1,0]\n"
  "v_rcp_f16 v208, v212\n"
  "v_mfma_f32_16x16x32_f16 v[108:111], v[220:223], v[192:195], v[108:111]\n"
  "v_rcp_f16 v209, v213\n"
  "v_mfma_f32_16x16x32_f16 v[12:15], v[220:223], v[196:199], v[12:15]\n"
  "v_rcp_f16_sdwa v208, v212 dst_sel:WORD_1 dst_unused:UNUSED_PRESERVE src0_sel:WORD_1\n"
  "v_mfma_f32_16x16x32_f16 v[132:135], v[220:223], v[180:183], v[132:135]\n"
  "s_waitcnt lgkmcnt(6)\n"
  "ds_read_b128 v[220:223], v1 offset:6144\n"
  "v_rcp_f16_sdwa v209, v213 dst_sel:WORD_1 dst_unused:UNUSED_PRESERVE src0_sel:WORD_1\n"
  "v_mfma_f32_16x16x32_f16 v[112:115], v[224:227], v[192:195], v[112:115]\n"
  "v_or_b32 v188, v156, v157\n"
  "v_pk_mul_f16 v184, v184, v208\n"
  "v_mfma_f32_16x16x32_f16 v[16:19], v[224:227], v[196:199], v[16:19]\n"
  "v_pk_mul_f16 v185, v185, v209\n"
  "v_or_b32 v189, v158, v159\n"
  "v_mfma_f32_16x16x32_f16 v[136:139], v[224:227], v[180:183], v[136:139]\n"
  "s_waitcnt lgkmcnt(2)\n"
  "ds_read_b128 v[224:227], v3 offset:1024\n"
  "v_or_b32 v164, v160, v161\n"
  "v_or_b32 v165, v162, v163\n"
  "v_mfma_f32_16x16x32_bf16 v[152:155], v[228:231], v[80:83], 0\n"
  "v_lshlrev_b32_sdwa v172, 1, v172 dst_sel:DWORD dst_unused:UNUSED_PAD src0_sel:DWORD src1_sel:WORD_1\n"
  "v_lshlrev_b32_sdwa v173, 1, v173 dst_sel:DWORD dst_unused:UNUSED_PAD src0_sel:DWORD src1_sel:WORD_1\n"
  "v_mfma_f32_16x16x32_bf16 v[156:159], v[228:231], v[84:87], 0\n"
  "ds_read_u16 v172, v172\n"
  "v_lshlrev_b32_sdwa v174, 1, v174 dst_sel:DWORD dst_unused:UNUSED_PAD src0_sel:DWORD src1_sel:WORD_1\n"
  "v_cvt_pk_f16_f32 v186, v168, v169\n"
  "v_mfma_f32_16x16x32_bf16 v[160:163], v[228:231], v[60:63], 0\n"
  "s_waitcnt lgkmcnt(3)\n"
  "ds_read_b128 v[228:231], v3 offset:2048\n"
  "v_cvt_pk_f16_f32 v187, v170, v171\n"
  "v_pk_fma_f16 v208, v186, v186, s56\n"
  "v_mfma_f32_16x16x32_bf16 v[152:155], v[216:219], v[88:91], v[152:155]\n"
  "ds_read_u16_d16_hi v173, v173\n"
  "v_pk_fma_f16 v209, v187, v187, s56\n"
  "v_lshlrev_b32_sdwa v175, 1, v175 dst_sel:DWORD dst_unused:UNUSED_PAD src0_sel:DWORD src1_sel:WORD_1\n"
  "v_mfma_f32_16x16x32_bf16 v[156:159], v[216:219], v[92:95], v[156:159]\n"
  "ds_read_u16 v174, v174\n"
  "v_lshlrev_b32_sdwa v176, 1, v176 dst_sel:DWORD dst_unused:UNUSED_PAD src0_sel:DWORD src1_sel:WORD_1\n"
  "v_pk_mul_f16 v208, v186, v208\n"
  "v_pk_mul_f16 v209, v187, v209\n"
  "v_mfma_f32_16x16x32_bf16 v[160:163], v[216:219], v[68:71], v[160:163]\n"
  "s_waitcnt lgkmcnt(5)\n"
  "ds_read_b128 v[216:219], v3 offset:3072\n"
  "ds_read_u16_d16_hi v175, v175\n"
  "v_lshlrev_b32_sdwa v177, 1, v177 dst_sel:DWORD dst_unused:UNUSED_PAD src0_sel:DWORD src1_sel:WORD_1\n"
  "v_lshlrev_b32_sdwa v178, 1, v178 dst_sel:DWORD dst_unused:UNUSED_PAD src0_sel:DWORD src1_sel:WORD_1\n"
  "v_mfma_f32_16x16x32_bf16 v[152:155], v[220:223], v[96:99], v[152:155]\n"
  "ds_read_u16 v176, v176\n"
  "v_exp_f16 v212, v208\n"
  "v_mfma_f32_16x16x32_bf16 v[156:159], v[220:223], v[100:103], v[156:159]\n"
  "ds_read_u16_d16_hi v177, v177\n"
  "v_exp_f16 v213, v209\n"
  "v_mfma_f32_16x16x32_bf16 v[160:163], v[220:223], v[76:79], v[160:163]\n"
  "s_waitcnt lgkmcnt(8)\n"
  "ds_read_b128 v[220:223], v0 offset:61696\n"
  "v_lshlrev_b32_sdwa v179, 1, v179 dst_sel:DWORD dst_unused:UNUSED_PAD src0_sel:DWORD src1_sel:WORD_1\n"
  "v_exp_f16_sdwa v212, v208 dst_sel:WORD_1 dst_unused:UNUSED_PRESERVE src0_sel:WORD_1\n"
  "v_mfma_f32_16x16x32_f16 v[116:119], v[224:227], v[192:195], v[116:119]\n"
  "ds_read_u16 v178, v178\n"
  "v_exp_f16_sdwa v213, v209 dst_sel:WORD_1 dst_unused:UNUSED_PRESERVE src0_sel:WORD_1\n"
  "v_mfma_f32_16x16x32_f16 v[20:23], v[224:227], v[196:199], v[20:23]\n"
  "ds_read_u16_d16_hi v179, v179\n"
  "v_pk_add_f16 v212, v212, 1.0 op_sel_hi:[1,0]\n"
  "v_mfma_f32_16x16x32_f16 v[140:143], v[224:227], v[180:183], v[140:143]\n"
  "s_waitcnt lgkmcnt(9)\n"
  "ds_read_b128 v[224:227], v0 offset:63744\n"
  "v_pk_add_f16 v213, v213, 1.0 op_sel_hi:[1,0]\n"
  "v_rcp_f16 v208, v212\n"
  "v_mfma_f32_16x16x32_f16 v[120:123], v[228:231], v[192:195], v[120:123]\n"
  "v_rcp_f16 v209, v213\n"
  "v_mfma_f32_16x16x32_f16 v[24:27], v[228:231], v[196:199], v[24:27]\n"
  "v_rcp_f16_sdwa v208, v212 dst_sel:WORD_1 dst_unused:UNUSED_PRESERVE src0_sel:WORD_1\n"
  "v_mfma_f32_16x16x32_f16 v[144:147], v[228:231], v[180:183], v[144:147]\n"
  "s_waitcnt lgkmcnt(6)\n"
  "ds_read_b128 v[228:231], v1 offset:6400\n"
  "v_rcp_f16_sdwa v209, v213 dst_sel:WORD_1 dst_unused:UNUSED_PRESERVE src0_sel:WORD_1\n"
  "v_mfma_f32_16x16x32_f16 v[124:127], v[216:219], v[192:195], v[124:127]\n"
  "v_or_b32 v190, v172, v173\n"
  "v_pk_mul_f16 v186, v186, v208\n"
  "v_mfma_f32_16x16x32_f16 v[28:31], v[216:219], v[196:199], v[28:31]\n"
  "v_pk_mul_f16 v187, v187, v209\n"
  "v_or_b32 v191, v174, v175\n"
  "v_mfma_f32_16x16x32_f16 v[148:151], v[216:219], v[180:183], v[148:151]\n"
  "s_waitcnt lgkmcnt(2)\n"
  "ds_read_b128 v[216:219], v3 offset:4096\n"
  "v_or_b32 v166, v176, v177\n"
  "v_or_b32 v167, v178, v179\n"
  "v_mfma_f32_16x16x32_bf16 v[168:171], v[220:223], v[80:83], 0\n"
  "v_lshlrev_b32_sdwa v156, 1, v156 dst_sel:DWORD dst_unused:UNUSED_PAD src0_sel:DWORD src1_sel:WORD_1\n"
  "v_lshlrev_b32_sdwa v157, 1, v157 dst_sel:DWORD dst_unused:UNUSED_PAD src0_sel:DWORD src1_sel:WORD_1\n"
  "v_mfma_f32_16x16x32_bf16 v[172:175], v[220:223], v[84:87], 0\n"
  "v_lshlrev_b32_sdwa v158, 1, v158 dst_sel:DWORD dst_unused:UNUSED_PAD src0_sel:DWORD src1_sel:WORD_1\n"
  "v_mfma_f32_16x16x32_bf16 v[176:179], v[220:223], v[60:63], 0\n"
  "s_waitcnt lgkmcnt(2)\n"
  "ds_read_b128 v[220:223], v3 offset:5120\n"
  "ds_read_u16 v156, v156\n"
  "v_cvt_pk_f16_f32 v192, v152, v153\n"
  "v_cvt_pk_f16_f32 v193, v154, v155\n"
  "s_waitcnt vmcnt(5)\n"
  "v_mfma_f32_16x16x32_bf16 v[128:131], v[248:251], v[200:203], v[128:131]\n"
  "ds_read_u16_d16_hi v157, v157\n"
  "v_pk_fma_f16 v208, v192, v192, s56\n"
  "v_mfma_f32_16x16x32_bf16 v[132:135], v[252:255], v[200:203], v[132:135]\n"
  "v_pk_fma_f16 v209, v193, v193, s56\n"
  "v_lshlrev_b32_sdwa v159, 1, v159 dst_sel:DWORD dst_unused:UNUSED_PAD src0_sel:DWORD src1_sel:WORD_1\n"
  "v_mfma_f32_16x16x32_bf16 v[168:171], v[224:227], v[88:91], v[168:171]\n"
  "s_waitcnt lgkmcnt(4)\n"
  "ds_read_u16 v158, v158\n"
  "v_lshlrev_b32_sdwa v160, 1, v160 dst_sel:DWORD dst_unused:UNUSED_PAD src0_sel:DWORD src1_sel:WORD_1\n"
  "v_mfma_f32_16x16x32_bf16 v[172:175], v[224:227], v[92:95], v[172:175]\n"
  "v_pk_mul_f16 v208, v192, v208\n"
  "v_pk_mul_f16 v209, v193, v209\n"
  "v_mfma_f32_16x16x32_bf16 v[176:179], v[224:227], v[68:71], v[176:179]\n"
  "ds_read_b128 v[224:227], v3 offset:6144\n"
  "ds_read_u16_d16_hi v159, v159\n"
  "v_lshlrev_b32_sdwa v161, 1, v161 dst_sel:DWORD dst_unused:UNUSED_PAD src0_sel:DWORD src1_sel:WORD_1\n"
  "v_lshlrev_b32_sdwa v162, 1, v162 dst_sel:DWORD dst_unused:UNUSED_PAD src0_sel:DWORD src1_sel:WORD_1\n"
  "v_mfma_f32_16x16x32_bf16 v[168:171], v[228:231], v[96:99], v[168:171]\n"
  "ds_read_u16 v160, v160\n"
  "v_exp_f16 v212, v208\n"
  "v_mfma_f32_16x16x32_bf16 v[172:175], v[228:231], v[100:103], v[172:175]\n"
  "v_exp_f16 v213, v209\n"
  "v_mfma_f32_16x16x32_bf16 v[176:179], v[228:231], v[76:79], v[176:179]\n"
  "ds_read_b128 v[228:231], v1 offset:8192\n"
  "ds_read_u16_d16_hi v161, v161\n"
  "s_waitcnt vmcnt(4)\n"
  "v_mfma_f32_16x16x32_bf16 v[136:139], v[248:251], v[204:207], v[136:139]\n"
  "s_waitcnt lgkmcnt(9)\n"
  "ds_read_u16 v162, v162\n"
  "v_lshlrev_b32_sdwa v163, 1, v163 dst_sel:DWORD dst_unused:UNUSED_PAD src0_sel:DWORD src1_sel:WORD_1\n"
  "v_exp_f16_sdwa v212, v208 dst_sel:WORD_1 dst_unused:UNUSED_PRESERVE src0_sel:WORD_1\n"
  "v_mfma_f32_16x16x32_bf16 v[140:143], v[252:255], v[204:207], v[140:143]\n"
  "v_exp_f16_sdwa v213, v209 dst_sel:WORD_1 dst_unused:UNUSED_PRESERVE src0_sel:WORD_1\n"
  "v_mfma_f32_16x16x32_f16 v[104:107], v[216:219], v[184:187], v[104:107]\n"
  "ds_read_u16_d16_hi v163, v163\n"
  "v_mfma_f32_16x16x32_f16 v[8:11], v[216:219], v[188:191], v[8:11]\n"
  "s_waitcnt lgkmcnt(10)\n"
  "v_pk_add_f16 v212, v212, 1.0 op_sel_hi:[1,0]\n"
  "v_pk_add_f16 v213, v213, 1.0 op_sel_hi:[1,0]\n"
  "v_mfma_f32_16x16x32_f16 v[128:131], v[216:219], v[164:167], v[128:131]\n"
  "ds_read_b128 v[216:219], v1 offset:10240\n"
  "v_rcp_f16 v208, v212\n"
  "v_mfma_f32_16x16x32_f16 v[108:111], v[220:223], v[184:187], v[108:111]\n"
  "v_rcp_f16 v209, v213\n"
  "v_mfma_f32_16x16x32_f16 v[12:15], v[220:223], v[188:191], v[12:15]\n"
  "v_mfma_f32_16x16x32_f16 v[132:135], v[220:223], v[164:167], v[132:135]\n"
  "ds_read_b128 v[220:223], v1 offset:12288\n"
  "v_rcp_f16_sdwa v208, v212 dst_sel:WORD_1 dst_unused:UNUSED_PRESERVE src0_sel:WORD_1\n"
  "s_waitcnt vmcnt(3)\n"
  "v_mfma_f32_16x16x32_bf16 v[144:147], v[248:251], v[232:235], v[144:147]\n"
  "v_rcp_f16_sdwa v209, v213 dst_sel:WORD_1 dst_unused:UNUSED_PRESERVE src0_sel:WORD_1\n"
  "v_mfma_f32_16x16x32_bf16 v[148:151], v[252:255], v[232:235], v[148:151]\n"
  "s_waitcnt lgkmcnt(7)\n"
  "buffer_load_dwordx4 v[200:203], v5, s[40:43], s63 offen offset:0 \n"
  "buffer_load_dwordx4 v[204:207], v5, s[40:43], s63 offen offset:64 \n"
  "buffer_load_dwordx4 v[232:235], v5, s[40:43], s63 offen offset:128 \n"
  "v_or_b32 v196, v156, v157\n"
  "v_mfma_f32_16x16x32_f16 v[112:115], v[224:227], v[184:187], v[112:115]\n"
  "v_pk_mul_f16 v192, v192, v208\n"
  "v_pk_mul_f16 v193, v193, v209\n"
  "v_mfma_f32_16x16x32_f16 v[16:19], v[224:227], v[188:191], v[16:19]\n"
  "v_or_b32 v197, v158, v159\n"
  "v_mfma_f32_16x16x32_f16 v[136:139], v[224:227], v[164:167], v[136:139]\n"
  "s_waitcnt lgkmcnt(2)\n"
  "ds_read_b128 v[224:227], v3 offset:7168\n"
  "v_or_b32 v180, v160, v161\n"
  "v_or_b32 v181, v162, v163\n"
  "v_mfma_f32_16x16x32_bf16 v[152:155], v[228:231], v[80:83], 0\n"
  "v_lshlrev_b32_sdwa v172, 1, v172 dst_sel:DWORD dst_unused:UNUSED_PAD src0_sel:DWORD src1_sel:WORD_1\n"
  "v_lshlrev_b32_sdwa v173, 1, v173 dst_sel:DWORD dst_unused:UNUSED_PAD src0_sel:DWORD src1_sel:WORD_1\n"
  "v_mfma_f32_16x16x32_bf16 v[156:159], v[228:231], v[84:87], 0\n"
  "ds_read_u16 v172, v172\n"
  "v_lshlrev_b32_sdwa v174, 1, v174 dst_sel:DWORD dst_unused:UNUSED_PAD src0_sel:DWORD src1_sel:WORD_1\n"
  "v_cvt_pk_f16_f32 v194, v168, v169\n"
  "v_mfma_f32_16x16x32_bf16 v[160:163], v[228:231], v[60:63], 0\n"
  "s_waitcnt lgkmcnt(3)\n"
  "ds_read_b128 v[228:231], v3 offset:8192\n"
  "v_cvt_pk_f16_f32 v195, v170, v171\n"
  "v_pk_fma_f16 v208, v194, v194, s56\n"
  "v_mfma_f32_16x16x32_bf16 v[152:155], v[216:219], v[88:91], v[152:155]\n"
  "ds_read_u16_d16_hi v173, v173\n"
  "v_pk_fma_f16 v209, v195, v195, s56\n"
  "v_lshlrev_b32_sdwa v175, 1, v175 dst_sel:DWORD dst_unused:UNUSED_PAD src0_sel:DWORD src1_sel:WORD_1\n"
  "v_mfma_f32_16x16x32_bf16 v[156:159], v[216:219], v[92:95], v[156:159]\n"
  "ds_read_u16 v174, v174\n"
  "v_lshlrev_b32_sdwa v176, 1, v176 dst_sel:DWORD dst_unused:UNUSED_PAD src0_sel:DWORD src1_sel:WORD_1\n"
  "v_pk_mul_f16 v208, v194, v208\n"
  "v_pk_mul_f16 v209, v195, v209\n"
  "v_mfma_f32_16x16x32_bf16 v[160:163], v[216:219], v[68:71], v[160:163]\n"
  "s_waitcnt lgkmcnt(5)\n"
  "ds_read_b128 v[216:219], v3 offset:9216\n"
  "ds_read_u16_d16_hi v175, v175\n"
  "v_lshlrev_b32_sdwa v177, 1, v177 dst_sel:DWORD dst_unused:UNUSED_PAD src0_sel:DWORD src1_sel:WORD_1\n"
  "v_lshlrev_b32_sdwa v178, 1, v178 dst_sel:DWORD dst_unused:UNUSED_PAD src0_sel:DWORD src1_sel:WORD_1\n"
  "v_mfma_f32_16x16x32_bf16 v[152:155], v[220:223], v[96:99], v[152:155]\n"
  "ds_read_u16 v176, v176\n"
  "v_exp_f16 v212, v208\n"
  "v_mfma_f32_16x16x32_bf16 v[156:159], v[220:223], v[100:103], v[156:159]\n"
  "ds_read_u16_d16_hi v177, v177\n"
  "v_exp_f16 v213, v209\n"
  "v_mfma_f32_16x16x32_bf16 v[160:163], v[220:223], v[76:79], v[160:163]\n"
  "s_waitcnt lgkmcnt(8)\n"
  "ds_read_b128 v[220:223], v1 offset:8448\n"
  "v_lshlrev_b32_sdwa v179, 1, v179 dst_sel:DWORD dst_unused:UNUSED_PAD src0_sel:DWORD src1_sel:WORD_1\n"
  "v_exp_f16_sdwa v212, v208 dst_sel:WORD_1 dst_unused:UNUSED_PRESERVE src0_sel:WORD_1\n"
  "v_mfma_f32_16x16x32_f16 v[116:119], v[224:227], v[184:187], v[116:119]\n"
  "ds_read_u16 v178, v178\n"
  "v_exp_f16_sdwa v213, v209 dst_sel:WORD_1 dst_unused:UNUSED_PRESERVE src0_sel:WORD_1\n"
  "v_mfma_f32_16x16x32_f16 v[20:23], v[224:227], v[188:191], v[20:23]\n"
  "ds_read_u16_d16_hi v179, v179\n"
  "v_pk_add_f16 v212, v212, 1.0 op_sel_hi:[1,0]\n"
  "v_mfma_f32_16x16x32_f16 v[140:143], v[224:227], v[164:167], v[140:143]\n"
  "s_waitcnt lgkmcnt(9)\n"
  "ds_read_b128 v[224:227], v1 offset:10496\n"
  "v_pk_add_f16 v213, v213, 1.0 op_sel_hi:[1,0]\n"
  "v_rcp_f16 v208, v212\n"
  "v_mfma_f32_16x16x32_f16 v[120:123], v[228:231], v[184:187], v[120:123]\n"
  "v_rcp_f16 v209, v213\n"
  "v_mfma_f32_16x16x32_f16 v[24:27], v[228:231], v[188:191], v[24:27]\n"
  "v_rcp_f16_sdwa v208, v212 dst_sel:WORD_1 dst_unused:UNUSED_PRESERVE src0_sel:WORD_1\n"
  "v_mfma_f32_16x16x32_f16 v[144:147], v[228:231], v[164:167], v[144:147]\n"
  "s_waitcnt lgkmcnt(6)\n"
  "ds_read_b128 v[228:231], v1 offset:12544\n"
  "v_rcp_f16_sdwa v209, v213 dst_sel:WORD_1 dst_unused:UNUSED_PRESERVE src0_sel:WORD_1\n"
  "v_mfma_f32_16x16x32_f16 v[124:127], v[216:219], v[184:187], v[124:127]\n"
  "v_or_b32 v198, v172, v173\n"
  "v_pk_mul_f16 v194, v194, v208\n"
  "v_mfma_f32_16x16x32_f16 v[28:31], v[216:219], v[188:191], v[28:31]\n"
  "v_pk_mul_f16 v195, v195, v209\n"
  "v_or_b32 v199, v174, v175\n"
  "v_mfma_f32_16x16x32_f16 v[148:151], v[216:219], v[164:167], v[148:151]\n"
  "s_waitcnt lgkmcnt(2)\n"
  "ds_read_b128 v[216:219], v3 offset:10240\n"
  "v_or_b32 v182, v176, v177\n"
  "v_or_b32 v183, v178, v179\n"
  "v_mfma_f32_16x16x32_bf16 v[168:171], v[220:223], v[80:83], 0\n"
  "v_lshlrev_b32_sdwa v156, 1, v156 dst_sel:DWORD dst_unused:UNUSED_PAD src0_sel:DWORD src1_sel:WORD_1\n"
  "v_lshlrev_b32_sdwa v157, 1, v157 dst_sel:DWORD dst_unused:UNUSED_PAD src0_sel:DWORD src1_sel:WORD_1\n"
  "v_mfma_f32_16x16x32_bf16 v[172:175], v[220:223], v[84:87], 0\n"
  "ds_read_u16 v156, v156\n"
  "v_lshlrev_b32_sdwa v158, 1, v158 dst_sel:DWORD dst_unused:UNUSED_PAD src0_sel:DWORD src1_sel:WORD_1\n"
  "v_cvt_pk_f16_f32 v184, v152, v153\n"
  "v_mfma_f32_16x16x32_bf16 v[176:179], v[220:223], v[60:63], 0\n"
  "s_waitcnt lgkmcnt(3)\n"
  "ds_read_b128 v[220:223], v3 offset:11264\n"
  "v_cvt_pk_f16_f32 v185, v154, v155\n"
  "v_pk_fma_f16 v208, v184, v184, s56\n"
  "v_mfma_f32_16x16x32_bf16 v[168:171], v[224:227], v[88:91], v[168:171]\n"
  "ds_read_u16_d16_hi v157, v157\n"
  "v_pk_fma_f16 v209, v185, v185, s56\n"
  "v_lshlrev_b32_sdwa v159, 1, v159 dst_sel:DWORD dst_unused:UNUSED_PAD src0_sel:DWORD src1_sel:WORD_1\n"
  "v_mfma_f32_16x16x32_bf16 v[172:175], v[224:227], v[92:95], v[172:175]\n"
  "ds_read_u16 v158, v158\n"
  "v_lshlrev_b32_sdwa v160, 1, v160 dst_sel:DWORD dst_unused:UNUSED_PAD src0_sel:DWORD src1_sel:WORD_1\n"
  "v_pk_mul_f16 v208, v184, v208\n"
  "v_pk_mul_f16 v209, v185, v209\n"
  "v_mfma_f32_16x16x32_bf16 v[176:179], v[224:227], v[68:71], v[176:179]\n"
  "s_waitcnt lgkmcnt(5)\n"
  "ds_read_b128 v[224:227], v3 offset:12288\n"
  "ds_read_u16_d16_hi v159, v159\n"
  "v_lshlrev_b32_sdwa v161, 1, v161 dst_sel:DWORD dst_unused:UNUSED_PAD src0_sel:DWORD src1_sel:WORD_1\n"
  "v_lshlrev_b32_sdwa v162, 1, v162 dst_sel:DWORD dst_unused:UNUSED_PAD src0_sel:DWORD src1_sel:WORD_1\n"
  "v_mfma_f32_16x16x32_bf16 v[168:171], v[228:231], v[96:99], v[168:171]\n"
  "ds_read_u16 v160, v160\n"
  "v_exp_f16 v212, v208\n"
  "v_mfma_f32_16x16x32_bf16 v[172:175], v[228:231], v[100:103], v[172:175]\n"
  "ds_read_u16_d16_hi v161, v161\n"
  "v_exp_f16 v213, v209\n"
  "v_mfma_f32_16x16x32_bf16 v[176:179], v[228:231], v[76:79], v[176:179]\n"
  "s_waitcnt lgkmcnt(8)\n"
  "ds_read_b128 v[228:231], v1 offset:14336\n"
  "v_lshlrev_b32_sdwa v163, 1, v163 dst_sel:DWORD dst_unused:UNUSED_PAD src0_sel:DWORD src1_sel:WORD_1\n"
  "v_exp_f16_sdwa v212, v208 dst_sel:WORD_1 dst_unused:UNUSED_PRESERVE src0_sel:WORD_1\n"
  "v_mfma_f32_16x16x32_f16 v[104:107], v[216:219], v[192:195], v[104:107]\n"
  "ds_read_u16 v162, v162\n"
  "v_exp_f16_sdwa v213, v209 dst_sel:WORD_1 dst_unused:UNUSED_PRESERVE src0_sel:WORD_1\n"
  "v_mfma_f32_16x16x32_f16 v[8:11], v[216:219], v[196:199], v[8:11]\n"
  "ds_read_u16_d16_hi v163, v163\n"
  "v_pk_add_f16 v212, v212, 1.0 op_sel_hi:[1,0]\n"
  "v_mfma_f32_16x16x32_f16 v[128:131], v[216:219], v[180:183], v[128:131]\n"
  "s_waitcnt lgkmcnt(9)\n"
  "ds_read_b128 v[216:219], v1 offset:16384\n"
  "v_pk_add_f16 v213, v213, 1.0 op_sel_hi:[1,0]\n"
  "v_rcp_f16 v208, v212\n"
  "v_mfma_f32_16x16x32_f16 v[108:111], v[220:223], v[192:195], v[108:111]\n"
  "v_rcp_f16 v209, v213\n"
  "v_mfma_f32_16x16x32_f16 v[12:15], v[220:223], v[196:199], v[12:15]\n"
  "v_rcp_f16_sdwa v208, v212 dst_sel:WORD_1 dst_unused:UNUSED_PRESERVE src0_sel:WORD_1\n"
  "v_mfma_f32_16x16x32_f16 v[132:135], v[220:223], v[180:183], v[132:135]\n"
  "s_waitcnt lgkmcnt(6)\n"
  "ds_read_b128 v[220:223], v1 offset:18432\n"
  "v_rcp_f16_sdwa v209, v213 dst_sel:WORD_1 dst_unused:UNUSED_PRESERVE src0_sel:WORD_1\n"
  "v_mfma_f32_16x16x32_f16 v[112:115], v[224:227], v[192:195], v[112:115]\n"
  "v_or_b32 v188, v156, v157\n"
  "v_pk_mul_f16 v184, v184, v208\n"
  "v_mfma_f32_16x16x32_f16 v[16:19], v[224:227], v[196:199], v[16:19]\n"
  "v_pk_mul_f16 v185, v185, v209\n"
  "v_or_b32 v189, v158, v159\n"
  "v_mfma_f32_16x16x32_f16 v[136:139], v[224:227], v[180:183], v[136:139]\n"
  "s_waitcnt lgkmcnt(2)\n"
  "ds_read_b128 v[224:227], v3 offset:13312\n"
  "v_or_b32 v164, v160, v161\n"
  "v_or_b32 v165, v162, v163\n"
  "v_mfma_f32_16x16x32_bf16 v[152:155], v[228:231], v[80:83], 0\n"
  "v_lshlrev_b32_sdwa v172, 1, v172 dst_sel:DWORD dst_unused:UNUSED_PAD src0_sel:DWORD src1_sel:WORD_1\n"
  "v_lshlrev_b32_sdwa v173, 1, v173 dst_sel:DWORD dst_unused:UNUSED_PAD src0_sel:DWORD src1_sel:WORD_1\n"
  "v_mfma_f32_16x16x32_bf16 v[156:159], v[228:231], v[84:87], 0\n"
  "ds_read_u16 v172, v172\n"
  "v_lshlrev_b32_sdwa v174, 1, v174 dst_sel:DWORD dst_unused:UNUSED_PAD src0_sel:DWORD src1_sel:WORD_1\n"
  "v_cvt_pk_f16_f32 v186, v168, v169\n"
  "v_mfma_f32_16x16x32_bf16 v[160:163], v[228:231], v[60:63], 0\n"
  "s_waitcnt lgkmcnt(3)\n"
  "ds_read_b128 v[228:231], v3 offset:14336\n"
  "v_cvt_pk_f16_f32 v187, v170, v171\n"
  "v_pk_fma_f16 v208, v186, v186, s56\n"
  "v_mfma_f32_16x16x32_bf16 v[152:155], v[216:219], v[88:91], v[152:155]\n"
  "ds_read_u16_d16_hi v173, v173\n"
  "v_pk_fma_f16 v209, v187, v187, s56\n"
  "v_lshlrev_b32_sdwa v175, 1, v175 dst_sel:DWORD dst_unused:UNUSED_PAD src0_sel:DWORD src1_sel:WORD_1\n"
  "v_mfma_f32_16x16x32_bf16 v[156:159], v[216:219], v[92:95], v[156:159]\n"
  "ds_read_u16 v174, v174\n"
  "v_lshlrev_b32_sdwa v176, 1, v176 dst_sel:DWORD dst_unused:UNUSED_PAD src0_sel:DWORD src1_sel:WORD_1\n"
  "v_pk_mul_f16 v208, v186, v208\n"
  "v_pk_mul_f16 v209, v187, v209\n"
  "v_mfma_f32_16x16x32_bf16 v[160:163], v[216:219], v[68:71], v[160:163]\n"
  "s_waitcnt lgkmcnt(5)\n"
  "ds_read_b128 v[216:219], v3 offset:15360\n"
  "ds_read_u16_d16_hi v175, v175\n"
  "v_lshlrev_b32_sdwa v177, 1, v177 dst_sel:DWORD dst_unused:UNUSED_PAD src0_sel:DWORD src1_sel:WORD_1\n"
  "v_lshlrev_b32_sdwa v178, 1, v178 dst_sel:DWORD dst_unused:UNUSED_PAD src0_sel:DWORD src1_sel:WORD_1\n"
  "v_mfma_f32_16x16x32_bf16 v[152:155], v[220:223], v[96:99], v[152:155]\n"
  "ds_read_u16 v176, v176\n"
  "v_exp_f16 v212, v208\n"
  "v_mfma_f32_16x16x32_bf16 v[156:159], v[220:223], v[100:103], v[156:159]\n"
  "ds_read_u16_d16_hi v177, v177\n"
  "v_exp_f16 v213, v209\n"
  "v_mfma_f32_16x16x32_bf16 v[160:163], v[220:223], v[76:79], v[160:163]\n"
  "s_waitcnt lgkmcnt(8)\n"
  "ds_read_b128 v[220:223], v1 offset:14592\n"
  "v_lshlrev_b32_sdwa v179, 1, v179 dst_sel:DWORD dst_unused:UNUSED_PAD src0_sel:DWORD src1_sel:WORD_1\n"
  "v_exp_f16_sdwa v212, v208 dst_sel:WORD_1 dst_unused:UNUSED_PRESERVE src0_sel:WORD_1\n"
  "v_mfma_f32_16x16x32_f16 v[116:119], v[224:227], v[192:195], v[116:119]\n"
  "ds_read_u16 v178, v178\n"
  "v_exp_f16_sdwa v213, v209 dst_sel:WORD_1 dst_unused:UNUSED_PRESERVE src0_sel:WORD_1\n"
  "v_mfma_f32_16x16x32_f16 v[20:23], v[224:227], v[196:199], v[20:23]\n"
  "ds_read_u16_d16_hi v179, v179\n"
  "v_pk_add_f16 v212, v212, 1.0 op_sel_hi:[1,0]\n"
  "v_mfma_f32_16x16x32_f16 v[140:143], v[224:227], v[180:183], v[140:143]\n"
  "s_waitcnt lgkmcnt(9)\n"
  "ds_read_b128 v[224:227], v1 offset:16640\n"
  "v_pk_add_f16 v213, v213, 1.0 op_sel_hi:[1,0]\n"
  "v_rcp_f16 v208, v212\n"
  "v_mfma_f32_16x16x32_f16 v[120:123], v[228:231], v[192:195], v[120:123]\n"
  "v_rcp_f16 v209, v213\n"
  "v_mfma_f32_16x16x32_f16 v[24:27], v[228:231], v[196:199], v[24:27]\n"
  "v_rcp_f16_sdwa v208, v212 dst_sel:WORD_1 dst_unused:UNUSED_PRESERVE src0_sel:WORD_1\n"
  "v_mfma_f32_16x16x32_f16 v[144:147], v[228:231], v[180:183], v[144:147]\n"
  "s_waitcnt lgkmcnt(6)\n"
  "ds_read_b128 v[228:231], v1 offset:18688\n"
  "v_rcp_f16_sdwa v209, v213 dst_sel:WORD_1 dst_unused:UNUSED_PRESERVE src0_sel:WORD_1\n"
  "v_mfma_f32_16x16x32_f16 v[124:127], v[216:219], v[192:195], v[124:127]\n"
  "v_or_b32 v190, v172, v173\n"
  "v_pk_mul_f16 v186, v186, v208\n"
  "v_mfma_f32_16x16x32_f16 v[28:31], v[216:219], v[196:199], v[28:31]\n"
  "v_pk_mul_f16 v187, v187, v209\n"
  "v_or_b32 v191, v174, v175\n"
  "v_mfma_f32_16x16x32_f16 v[148:151], v[216:219], v[180:183], v[148:151]\n"
  "s_waitcnt lgkmcnt(2)\n"
  "ds_read_b128 v[216:219], v3 offset:16384\n"
  "v_or_b32 v166, v176, v177\n"
  "v_or_b32 v167, v178, v179\n"
  "v_mfma_f32_16x16x32_bf16 v[168:171], v[220:223], v[80:83], 0\n"
  "v_lshlrev_b32_sdwa v156, 1, v156 dst_sel:DWORD dst_unused:UNUSED_PAD src0_sel:DWORD src1_sel:WORD_1\n"
  "v_lshlrev_b32_sdwa v157, 1, v157 dst_sel:DWORD dst_unused:UNUSED_PAD src0_sel:DWORD src1_sel:WORD_1\n"
  "v_mfma_f32_16x16x32_bf16 v[172:175], v[220:223], v[84:87], 0\n"
  "ds_read_u16 v156, v156\n"
  "v_lshlrev_b32_sdwa v158, 1, v158 dst_sel:DWORD dst_unused:UNUSED_PAD src0_sel:DWORD src1_sel:WORD_1\n"
  "v_cvt_pk_f16_f32 v192, v152, v153\n"
  "v_mfma_f32_16x16x32_bf16 v[176:179], v[220:223], v[60:63], 0\n"
  "s_waitcnt lgkmcnt(3)\n"
  "ds_read_b128 v[220:223], v3 offset:17408\n"
  "v_cvt_pk_f16_f32 v193, v154, v155\n"
  "v_pk_fma_f16 v208, v192, v192, s56\n"
  "v_mfma_f32_16x16x32_bf16 v[168:171], v[224:227], v[88:91], v[168:171]\n"
  "ds_read_u16_d16_hi v157, v157\n"
  "v_pk_fma_f16 v209, v193, v193, s56\n"
  "v_lshlrev_b32_sdwa v159, 1, v159 dst_sel:DWORD dst_unused:UNUSED_PAD src0_sel:DWORD src1_sel:WORD_1\n"
  "v_mfma_f32_16x16x32_bf16 v[172:175], v[224:227], v[92:95], v[172:175]\n"
  "ds_read_u16 v158, v158\n"
  "v_lshlrev_b32_sdwa v160, 1, v160 dst_sel:DWORD dst_unused:UNUSED_PAD src0_sel:DWORD src1_sel:WORD_1\n"
  "v_pk_mul_f16 v208, v192, v208\n"
  "v_pk_mul_f16 v209, v193, v209\n"
  "v_mfma_f32_16x16x32_bf16 v[176:179], v[224:227], v[68:71], v[176:179]\n"
  "s_waitcnt lgkmcnt(5)\n"
  "ds_read_b128 v[224:227], v3 offset:18432\n"
  "ds_read_u16_d16_hi v159, v159\n"
  "v_lshlrev_b32_sdwa v161, 1, v161 dst_sel:DWORD dst_unused:UNUSED_PAD src0_sel:DWORD src1_sel:WORD_1\n"
  "v_lshlrev_b32_sdwa v162, 1, v162 dst_sel:DWORD dst_unused:UNUSED_PAD src0_sel:DWORD src1_sel:WORD_1\n"
  "v_mfma_f32_16x16x32_bf16 v[168:171], v[228:231], v[96:99], v[168:171]\n"
  "ds_read_u16 v160, v160\n"
  "v_exp_f16 v212, v208\n"
  "v_mfma_f32_16x16x32_bf16 v[172:175], v[228:231], v[100:103], v[172:175]\n"
  "ds_read_u16_d16_hi v161, v161\n"
  "v_exp_f16 v213, v209\n"
  "v_mfma_f32_16x16x32_bf16 v[176:179], v[228:231], v[76:79], v[176:179]\n"
  "s_waitcnt lgkmcnt(8)\n"
  "ds_read_b128 v[228:231], v0 offset:6144\n"
  "v_lshlrev_b32_sdwa v163, 1, v163 dst_sel:DWORD dst_unused:UNUSED_PAD src0_sel:DWORD src1_sel:WORD_1\n"
  "v_exp_f16_sdwa v212, v208 dst_sel:WORD_1 dst_unused:UNUSED_PRESERVE src0_sel:WORD_1\n"
  "v_mfma_f32_16x16x32_f16 v[104:107], v[216:219], v[184:187], v[104:107]\n"
  "ds_read_u16 v162, v162\n"
  "v_exp_f16_sdwa v213, v209 dst_sel:WORD_1 dst_unused:UNUSED_PRESERVE src0_sel:WORD_1\n"
  "v_mfma_f32_16x16x32_f16 v[8:11], v[216:219], v[188:191], v[8:11]\n"
  "ds_read_u16_d16_hi v163, v163\n"
  "v_pk_add_f16 v212, v212, 1.0 op_sel_hi:[1,0]\n"
  "v_mfma_f32_16x16x32_f16 v[128:131], v[216:219], v[164:167], v[128:131]\n"
  "s_waitcnt lgkmcnt(9)\n"
  "ds_read_b128 v[216:219], v0 offset:8192\n"
  "v_pk_add_f16 v213, v213, 1.0 op_sel_hi:[1,0]\n"
  "v_rcp_f16 v208, v212\n"
  "v_mfma_f32_16x16x32_f16 v[108:111], v[220:223], v[184:187], v[108:111]\n"
  "v_rcp_f16 v209, v213\n"
  "v_mfma_f32_16x16x32_f16 v[12:15], v[220:223], v[188:191], v[12:15]\n"
  "v_rcp_f16_sdwa v208, v212 dst_sel:WORD_1 dst_unused:UNUSED_PRESERVE src0_sel:WORD_1\n"
  "v_mfma_f32_16x16x32_f16 v[132:135], v[220:223], v[164:167], v[132:135]\n"
  "s_waitcnt lgkmcnt(6)\n"
  "ds_read_b128 v[220:223], v0 offset:10240\n"
  "v_rcp_f16_sdwa v209, v213 dst_sel:WORD_1 dst_unused:UNUSED_PRESERVE src0_sel:WORD_1\n"
  "v_mfma_f32_16x16x32_f16 v[112:115], v[224:227], v[184:187], v[112:115]\n"
  "v_or_b32 v196, v156, v157\n"
  "v_pk_mul_f16 v192, v192, v208\n"
  "v_mfma_f32_16x16x32_f16 v[16:19], v[224:227], v[188:191], v[16:19]\n"
  "v_pk_mul_f16 v193, v193, v209\n"
  "v_or_b32 v197, v158, v159\n"
  "v_mfma_f32_16x16x32_f16 v[136:139], v[224:227], v[164:167], v[136:139]\n"
  "s_waitcnt lgkmcnt(2)\n"
  "ds_read_b128 v[224:227], v3 offset:19456\n"
  "v_or_b32 v180, v160, v161\n"
  "v_or_b32 v181, v162, v163\n"
  "v_mfma_f32_16x16x32_bf16 v[152:155], v[228:231], v[32:35], 0\n"
  "v_lshlrev_b32_sdwa v172, 1, v172 dst_sel:DWORD dst_unused:UNUSED_PAD src0_sel:DWORD src1_sel:WORD_1\n"
  "v_lshlrev_b32_sdwa v173, 1, v173 dst_sel:DWORD dst_unused:UNUSED_PAD src0_sel:DWORD src1_sel:WORD_1\n"
  "v_mfma_f32_16x16x32_bf16 v[156:159], v[228:231], v[36:39], 0\n"
  "ds_read_u16 v172, v172\n"
  "v_lshlrev_b32_sdwa v174, 1, v174 dst_sel:DWORD dst_unused:UNUSED_PAD src0_sel:DWORD src1_sel:WORD_1\n"
  "v_cvt_pk_f16_f32 v194, v168, v169\n"
  "v_mfma_f32_16x16x32_bf16 v[160:163], v[228:231], v[56:59], 0\n"
  "s_waitcnt lgkmcnt(3)\n"
  "ds_read_b128 v[228:231], v3 offset:20480\n"
  "v_cvt_pk_f16_f32 v195, v170, v171\n"
  "v_pk_fma_f16 v208, v194, v194, s56\n"
  "v_mfma_f32_16x16x32_bf16 v[152:155], v[216:219], v[40:43], v[152:155]\n"
  "ds_read_u16_d16_hi v173, v173\n"
  "v_pk_fma_f16 v209, v195, v195, s56\n"
  "v_lshlrev_b32_sdwa v175, 1, v175 dst_sel:DWORD dst_unused:UNUSED_PAD src0_sel:DWORD src1_sel:WORD_1\n"
  "v_mfma_f32_16x16x32_bf16 v[156:159], v[216:219], v[44:47], v[156:159]\n"
  "ds_read_u16 v174, v174\n"
  "v_lshlrev_b32_sdwa v176, 1, v176 dst_sel:DWORD dst_unused:UNUSED_PAD src0_sel:DWORD src1_sel:WORD_1\n"
  "v_pk_mul_f16 v208, v194, v208\n"
  "v_pk_mul_f16 v209, v195, v209\n"
  "v_mfma_f32_16x16x32_bf16 v[160:163], v[216:219], v[64:67], v[160:163]\n"
  "s_waitcnt lgkmcnt(5)\n"
  "ds_read_b128 v[216:219], v3 offset:21504\n"
  "ds_read_u16_d16_hi v175, v175\n"
  "ds_read_b128 v[80:83], v4 offset:0\n"
  "ds_read_b128 v[84:87], v4 offset:16\n"
  "ds_read_b128 v[88:91], v4 offset:128\n"
  "ds_read_b128 v[92:95], v4 offset:144\n"
  "ds_read_b128 v[96:99], v4 offset:256\n"
  "ds_read_b128 v[100:103], v4 offset:272\n"
  "v_lshlrev_b32_sdwa v177, 1, v177 dst_sel:DWORD dst_unused:UNUSED_PAD src0_sel:DWORD src1_sel:WORD_1\n"
  "v_lshlrev_b32_sdwa v178, 1, v178 dst_sel:DWORD dst_unused:UNUSED_PAD src0_sel:DWORD src1_sel:WORD_1\n"
  "v_mfma_f32_16x16x32_bf16 v[152:155], v[220:223], v[48:51], v[152:155]\n"
  "ds_read_u16 v176, v176\n"
  "v_exp_f16 v212, v208\n"
  "v_mfma_f32_16x16x32_bf16 v[156:159], v[220:223], v[52:55], v[156:159]\n"
  "ds_read_u16_d16_hi v177, v177\n"
  "v_exp_f16 v213, v209\n"
  "v_mfma_f32_16x16x32_bf16 v[160:163], v[220:223], v[72:75], v[160:163]\n"
  "s_waitcnt lgkmcnt(14)\n"
  "ds_read_b128 v[220:223], v3 offset:22528\n"
  "v_lshlrev_b32_sdwa v179, 1, v179 dst_sel:DWORD dst_unused:UNUSED_PAD src0_sel:DWORD src1_sel:WORD_1\n"
  "v_exp_f16_sdwa v212, v208 dst_sel:WORD_1 dst_unused:UNUSED_PRESERVE src0_sel:WORD_1\n"
  "v_mfma_f32_16x16x32_f16 v[116:119], v[224:227], v[184:187], v[116:119]\n"
  "ds_read_u16 v178, v178\n"
  "v_exp_f16_sdwa v213, v209 dst_sel:WORD_1 dst_unused:UNUSED_PRESERVE src0_sel:WORD_1\n"
  "v_mfma_f32_16x16x32_f16 v[20:23], v[224:227], v[188:191], v[20:23]\n"
  "ds_read_u16_d16_hi v179, v179\n"
  "v_pk_add_f16 v212, v212, 1.0 op_sel_hi:[1,0]\n"
  "v_mfma_f32_16x16x32_f16 v[140:143], v[224:227], v[164:167], v[140:143]\n"
  "s_waitcnt lgkmcnt(15)\n"
  "ds_read_b128 v[224:227], v3 offset:23552\n"
  "v_pk_add_f16 v213, v213, 1.0 op_sel_hi:[1,0]\n"
  "v_rcp_f16 v208, v212\n"
  "v_mfma_f32_16x16x32_f16 v[120:123], v[228:231], v[184:187], v[120:123]\n"
  "v_rcp_f16 v209, v213\n"
  "v_mfma_f32_16x16x32_f16 v[24:27], v[228:231], v[188:191], v[24:27]\n"
  "v_rcp_f16_sdwa v208, v212 dst_sel:WORD_1 dst_unused:UNUSED_PRESERVE src0_sel:WORD_1\n"
  "v_mfma_f32_16x16x32_f16 v[144:147], v[228:231], v[164:167], v[144:147]\n"
  "s_waitcnt lgkmcnt(12)\n"
  "ds_read_b128 v[228:231], v3 offset:24576\n"
  "v_rcp_f16_sdwa v209, v213 dst_sel:WORD_1 dst_unused:UNUSED_PRESERVE src0_sel:WORD_1\n"
  "v_mfma_f32_16x16x32_f16 v[124:127], v[216:219], v[184:187], v[124:127]\n"
  "v_or_b32 v198, v172, v173\n"
  "v_pk_mul_f16 v194, v194, v208\n"
  "v_mfma_f32_16x16x32_f16 v[28:31], v[216:219], v[188:191], v[28:31]\n"
  "v_pk_mul_f16 v195, v195, v209\n"
  "v_or_b32 v199, v174, v175\n"
  "v_mfma_f32_16x16x32_f16 v[148:151], v[216:219], v[164:167], v[148:151]\n"
  "s_waitcnt lgkmcnt(2)\n"
  "ds_read_b128 v[216:219], v0 offset:6400\n"
  "v_or_b32 v182, v176, v177\n"
  "v_or_b32 v183, v178, v179\n"
  "v_mfma_f32_16x16x32_f16 v[104:107], v[220:223], v[192:195], v[104:107]\n"
  "v_lshlrev_b32_sdwa v156, 1, v156 dst_sel:DWORD dst_unused:UNUSED_PAD src0_sel:DWORD src1_sel:WORD_1\n"
  "v_mfma_f32_16x16x32_f16 v[8:11], v[220:223], v[196:199], v[8:11]\n"
  "v_lshlrev_b32_sdwa v157, 1, v157 dst_sel:DWORD dst_unused:UNUSED_PAD src0_sel:DWORD src1_sel:WORD_1\n"
  "v_lshlrev_b32_sdwa v158, 1, v158 dst_sel:DWORD dst_unused:UNUSED_PAD src0_sel:DWORD src1_sel:WORD_1\n"
  "v_mfma_f32_16x16x32_f16 v[128:131], v[220:223], v[180:183], v[128:131]\n"
  "s_waitcnt lgkmcnt(2)\n"
  "ds_read_b128 v[220:223], v0 offset:8448\n"
  "ds_read_u16 v156, v156\n"
  "v_cvt_pk_f16_f32 v184, v152, v153\n"
  "v_mfma_f32_16x16x32_f16 v[108:111], v[224:227], v[192:195], v[108:111]\n"
  "v_cvt_pk_f16_f32 v185, v154, v155\n"
  "v_pk_fma_f16 v208, v184, v184, s56\n"
  "v_mfma_f32_16x16x32_f16 v[12:15], v[224:227], v[196:199], v[12:15]\n"
  "ds_read_u16_d16_hi v157, v157\n"
  "v_pk_fma_f16 v209, v185, v185, s56\n"
  "v_mfma_f32_16x16x32_f16 v[132:135], v[224:227], v[180:183], v[132:135]\n"
  "s_waitcnt lgkmcnt(4)\n"
  "ds_read_b128 v[224:227], v0 offset:10496\n"
  "v_lshlrev_b32_sdwa v159, 1, v159 dst_sel:DWORD dst_unused:UNUSED_PAD src0_sel:DWORD src1_sel:WORD_1\n"
  "v_mfma_f32_16x16x32_f16 v[112:115], v[228:231], v[192:195], v[112:115]\n"
  "ds_read_u16 v158, v158\n"
  "v_lshlrev_b32_sdwa v160, 1, v160 dst_sel:DWORD dst_unused:UNUSED_PAD src0_sel:DWORD src1_sel:WORD_1\n"
  "v_pk_mul_f16 v208, v184, v208\n"
  "v_mfma_f32_16x16x32_f16 v[16:19], v[228:231], v[196:199], v[16:19]\n"
  "ds_read_u16_d16_hi v159, v159\n"
  "v_pk_mul_f16 v209, v185, v209\n"
  "v_mfma_f32_16x16x32_f16 v[136:139], v[228:231], v[180:183], v[136:139]\n"
  "s_waitcnt lgkmcnt(6)\n"
  "ds_read_b128 v[228:231], v3 offset:25600\n"
  "v_lshlrev_b32_sdwa v161, 1, v161 dst_sel:DWORD dst_unused:UNUSED_PAD src0_sel:DWORD src1_sel:WORD_1\n"
  "v_lshlrev_b32_sdwa v162, 1, v162 dst_sel:DWORD dst_unused:UNUSED_PAD src0_sel:DWORD src1_sel:WORD_1\n"
  "v_mfma_f32_16x16x32_bf16 v[168:171], v[216:219], v[32:35], 0\n"
  "ds_read_u16 v160, v160\n"
  "v_exp_f16 v212, v208\n"
  "v_mfma_f32_16x16x32_bf16 v[172:175], v[216:219], v[36:39], 0\n"
  "v_mfma_f32_16x16x32_bf16 v[176:179], v[216:219], v[56:59], 0\n"
  "s_waitcnt lgkmcnt(7)\n"
  "ds_read_b128 v[216:219], v3 offset:26624\n"
  "ds_read_u16_d16_hi v161, v161\n"
  "v_exp_f16 v213, v209\n"
  "v_mfma_f32_16x16x32_bf16 v[168:171], v[220:223], v[40:43], v[168:171]\n"
  "v_lshlrev_b32_sdwa v163, 1, v163 dst_sel:DWORD dst_unused:UNUSED_PAD src0_sel:DWORD src1_sel:WORD_1\n"
  "v_mfma_f32_16x16x32_bf16 v[172:175], v[220:223], v[44:47], v[172:175]\n"
  "ds_read_u16 v162, v162\n"
  "v_exp_f16_sdwa v212, v208 dst_sel:WORD_1 dst_unused:UNUSED_PRESERVE src0_sel:WORD_1\n"
  "v_mfma_f32_16x16x32_bf16 v[176:179], v[220:223], v[64:67], v[176:179]\n"
  "s_waitcnt lgkmcnt(7)\n"
  "ds_read_b128 v[220:223], v3 offset:27648\n"
  "v_exp_f16_sdwa v213, v209 dst_sel:WORD_1 dst_unused:UNUSED_PRESERVE src0_sel:WORD_1\n"
  "v_mfma_f32_16x16x32_bf16 v[168:171], v[224:227], v[48:51], v[168:171]\n"
  "ds_read_u16_d16_hi v163, v163\n"
  "v_pk_add_f16 v212, v212, 1.0 op_sel_hi:[1,0]\n"
  "v_mfma_f32_16x16x32_bf16 v[172:175], v[224:227], v[52:55], v[172:175]\n"
  "v_pk_add_f16 v213, v213, 1.0 op_sel_hi:[1,0]\n"
  "v_mfma_f32_16x16x32_bf16 v[176:179], v[224:227], v[72:75], v[176:179]\n"
  "s_waitcnt lgkmcnt(6)\n"
  "ds_read_b128 v[224:227], v0 offset:12288\n"
  "v_rcp_f16 v208, v212\n"
  "v_mfma_f32_16x16x32_f16 v[116:119], v[228:231], v[192:195], v[116:119]\n"
  "v_rcp_f16 v209, v213\n"
  "v_mfma_f32_16x16x32_f16 v[20:23], v[228:231], v[196:199], v[20:23]\n"
  "v_mfma_f32_16x16x32_f16 v[140:143], v[228:231], v[180:183], v[140:143]\n"
  "s_waitcnt lgkmcnt(5)\n"
  "ds_read_b128 v[228:231], v0 offset:14336\n"
  "v_rcp_f16_sdwa v208, v212 dst_sel:WORD_1 dst_unused:UNUSED_PRESERVE src0_sel:WORD_1\n"
  "v_mfma_f32_16x16x32_f16 v[120:123], v[216:219], v[192:195], v[120:123]\n"
  "v_rcp_f16_sdwa v209, v213 dst_sel:WORD_1 dst_unused:UNUSED_PRESERVE src0_sel:WORD_1\n"
  "v_mfma_f32_16x16x32_f16 v[24:27], v[216:219], v[196:199], v[24:27]\n"
  "v_mfma_f32_16x16x32_f16 v[144:147], v[216:219], v[180:183], v[144:147]\n"
  "s_waitcnt lgkmcnt(3)\n"
  "ds_read_b128 v[216:219], v0 offset:16384\n"
  "v_or_b32 v188, v156, v157\n"
  "v_pk_mul_f16 v184, v184, v208\n"
  "v_mfma_f32_16x16x32_f16 v[124:127], v[220:223], v[192:195], v[124:127]\n"
  "v_pk_mul_f16 v185, v185, v209\n"
  "v_mfma_f32_16x16x32_f16 v[28:31], v[220:223], v[196:199], v[28:31]\n"
  "v_or_b32 v189, v158, v159\n"
  "v_or_b32 v164, v160, v161\n"
  "v_mfma_f32_16x16x32_f16 v[148:151], v[220:223], v[180:183], v[148:151]\n"
  "s_waitcnt lgkmcnt(2)\n"
  "ds_read_b128 v[220:223], v0 offset:12544\n"
  "v_or_b32 v165, v162, v163\n"
  "v_mfma_f32_16x16x32_bf16 v[152:155], v[224:227], v[32:35], 0\n"
  "v_lshlrev_b32_sdwa v172, 1, v172 dst_sel:DWORD dst_unused:UNUSED_PAD src0_sel:DWORD src1_sel:WORD_1\n"
  "v_lshlrev_b32_sdwa v173, 1, v173 dst_sel:DWORD dst_unused:UNUSED_PAD src0_sel:DWORD src1_sel:WORD_1\n"
  "v_lshlrev_b32_sdwa v174, 1, v174 dst_sel:DWORD dst_unused:UNUSED_PAD src0_sel:DWORD src1_sel:WORD_1\n"
  "v_cvt_pk_f16_f32 v186, v168, v169\n"
  "v_cvt_pk_f16_f32 v187, v170, v171\n"
  "v_pk_fma_f16 v208, v186, v186, s56\n"
  "v_pk_fma_f16 v209, v187, v187, s56\n"
  "v_mfma_f32_16x16x32_bf16 v[156:159], v[224:227], v[36:39], 0\n"
  "ds_read_u16 v172, v172\n"
  "v_lshlrev_b32_sdwa v175, 1, v175 dst_sel:DWORD dst_unused:UNUSED_PAD src0_sel:DWORD src1_sel:WORD_1\n"
  "v_cvt_pk_bf16_f32 v104, v104, v105\n"
  "v_cvt_pk_bf16_f32 v105, v106, v107\n"
  "v_cvt_pk_bf16_f32 v106, v108, v109\n"
  "v_cvt_pk_bf16_f32 v107, v110, v111\n"
  "v_cvt_pk_bf16_f32 v112, v112, v113\n"
  "v_cvt_pk_bf16_f32 v113, v114, v115\n"
  "v_mfma_f32_16x16x32_bf16 v[160:163], v[224:227], v[56:59], 0\n"
  "s_waitcnt lgkmcnt(3)\n"
  "ds_read_b128 v[224:227], v0 offset:14592\n"
  "ds_read_u16_d16_hi v173, v173\n"
  "ds_read_u16 v174, v174\n"
  "v_cvt_pk_bf16_f32 v114, v116, v117\n"
  "v_cvt_pk_bf16_f32 v115, v118, v119\n"
  "v_cvt_pk_bf16_f32 v120, v120, v121\n"
  "v_cvt_pk_bf16_f32 v121, v122, v123\n"
  "v_cvt_pk_bf16_f32 v122, v124, v125\n"
  "v_cvt_pk_bf16_f32 v123, v126, v127\n"
  "buffer_store_dwordx4 v[104:107], v5, s[44:47], s53 offen offset:0 sc1\n"
  "buffer_store_dwordx4 v[112:115], v5, s[44:47], s53 offen offset:64 sc1\n"
  "buffer_store_dwordx4 v[120:123], v5, s[44:47], s53 offen offset:128 sc1\n"
  "v_lshlrev_b32_sdwa v176, 1, v176 dst_sel:DWORD dst_unused:UNUSED_PAD src0_sel:DWORD src1_sel:WORD_1\n"
  "v_mfma_f32_16x16x32_bf16 v[152:155], v[228:231], v[40:43], v[152:155]\n"
  "ds_read_u16_d16_hi v175, v175\n"
  "ds_read_u16 v176, v176\n"
  "v_pk_mul_f16 v208, v186, v208\n"
  "v_pk_mul_f16 v209, v187, v209\n"
  "v_lshlrev_b32_sdwa v177, 1, v177 dst_sel:DWORD dst_unused:UNUSED_PAD src0_sel:DWORD src1_sel:WORD_1\n"
  "v_lshlrev_b32_sdwa v178, 1, v178 dst_sel:DWORD dst_unused:UNUSED_PAD src0_sel:DWORD src1_sel:WORD_1\n"
  "v_exp_f16 v212, v208\n"
  "v_exp_f16 v213, v209\n"
  "v_mfma_f32_16x16x32_bf16 v[156:159], v[228:231], v[44:47], v[156:159]\n"
  "ds_read_u16_d16_hi v177, v177\n"
  "v_lshlrev_b32_sdwa v179, 1, v179 dst_sel:DWORD dst_unused:UNUSED_PAD src0_sel:DWORD src1_sel:WORD_1\n"
  "v_exp_f16_sdwa v212, v208 dst_sel:WORD_1 dst_unused:UNUSED_PRESERVE src0_sel:WORD_1\n"
  "v_exp_f16_sdwa v213, v209 dst_sel:WORD_1 dst_unused:UNUSED_PRESERVE src0_sel:WORD_1\n"
  "v_mfma_f32_16x16x32_bf16 v[160:163], v[228:231], v[64:67], v[160:163]\n"
  "s_waitcnt lgkmcnt(8)\n"
  "ds_read_b128 v[228:231], v0 offset:16640\n"
  "ds_read_u16 v178, v178\n"
  "ds_read_u16_d16_hi v179, v179\n"
  "v_pk_add_f16 v212, v212, 1.0 op_sel_hi:[1,0]\n"
  "v_cvt_pk_bf16_f32 v128, v128, v129\n"
  "v_cvt_pk_bf16_f32 v129, v130, v131\n"
  "v_cvt_pk_bf16_f32 v130, v132, v133\n"
  "v_cvt_pk_bf16_f32 v131, v134, v135\n"
  "v_cvt_pk_bf16_f32 v136, v136, v137\n"
  "v_cvt_pk_bf16_f32 v137, v138, v139\n"
  "v_mfma_f32_16x16x32_bf16 v[152:155], v[216:219], v[48:51], v[152:155]\n"
  "v_cvt_pk_bf16_f32 v138, v140, v141\n"
  "v_cvt_pk_bf16_f32 v139, v142, v143\n"
  "v_cvt_pk_bf16_f32 v144, v144, v145\n"
  "v_cvt_pk_bf16_f32 v145, v146, v147\n"
  "v_cvt_pk_bf16_f32 v146, v148, v149\n"
  "v_cvt_pk_bf16_f32 v147, v150, v151\n"
  "buffer_store_dwordx4 v[128:131], v5, s[44:47], s58 offen offset:0 sc1\n"
  "buffer_store_dwordx4 v[136:139], v5, s[44:47], s58 offen offset:64 sc1\n"
  "buffer_store_dwordx4 v[144:147], v5, s[44:47], s58 offen offset:128 sc1\n"
  "v_pk_add_f16 v213, v213, 1.0 op_sel_hi:[1,0]\n"
  "v_mfma_f32_16x16x32_bf16 v[156:159], v[216:219], v[52:55], v[156:159]\n"
  "v_rcp_f16 v208, v212\n"
  "v_rcp_f16 v209, v213\n"
  "v_rcp_f16_sdwa v208, v212 dst_sel:WORD_1 dst_unused:UNUSED_PRESERVE src0_sel:WORD_1\n"
  "v_rcp_f16_sdwa v209, v213 dst_sel:WORD_1 dst_unused:UNUSED_PRESERVE src0_sel:WORD_1\n"
  "v_mfma_f32_16x16x32_bf16 v[160:163], v[216:219], v[72:75], v[160:163]\n"
  "s_waitcnt lgkmcnt(0)\n"
  "ds_read_b128 v[216:219], v2 offset:20480\n"
  "v_or_b32 v190, v172, v173\n"
  "v_pk_mul_f16 v186, v186, v208\n"
  "v_pk_mul_f16 v187, v187, v209\n"
  "v_or_b32 v191, v174, v175\n"
  "v_or_b32 v166, v176, v177\n"
  "v_or_b32 v167, v178, v179\n"
  "v_mfma_f32_16x16x32_bf16 v[168:171], v[220:223], v[32:35], 0\n"
  "v_lshlrev_b32_sdwa v156, 1, v156 dst_sel:DWORD dst_unused:UNUSED_PAD src0_sel:DWORD src1_sel:WORD_1\n"
  "v_lshlrev_b32_sdwa v157, 1, v157 dst_sel:DWORD dst_unused:UNUSED_PAD src0_sel:DWORD src1_sel:WORD_1\n"
  "v_mfma_f32_16x16x32_bf16 v[172:175], v[220:223], v[36:39], 0\n"
  "ds_read_u16 v156, v156\n"
  "v_lshlrev_b32_sdwa v158, 1, v158 dst_sel:DWORD dst_unused:UNUSED_PAD src0_sel:DWORD src1_sel:WORD_1\n"
  "v_cvt_pk_f16_f32 v192, v152, v153\n"
  "v_mfma_f32_16x16x32_bf16 v[176:179], v[220:223], v[56:59], 0\n"
  "ds_read_b128 v[220:223], v2 offset:21504\n"
  "v_cvt_pk_f16_f32 v193, v154, v155\n"
  "v_pk_fma_f16 v208, v192, v192, s56\n"
  "v_mfma_f32_16x16x32_bf16 v[168:171], v[224:227], v[40:43], v[168:171]\n"
  "ds_read_u16_d16_hi v157, v157\n"
  "v_pk_fma_f16 v209, v193, v193, s56\n"
  "v_lshlrev_b32_sdwa v159, 1, v159 dst_sel:DWORD dst_unused:UNUSED_PAD src0_sel:DWORD src1_sel:WORD_1\n"
  "v_mfma_f32_16x16x32_bf16 v[172:175], v[224:227], v[44:47], v[172:175]\n"
  "ds_read_u16 v158, v158\n"
  "v_lshlrev_b32_sdwa v160, 1, v160 dst_sel:DWORD dst_unused:UNUSED_PAD src0_sel:DWORD src1_sel:WORD_1\n"
  "v_pk_mul_f16 v208, v192, v208\n"
  "v_pk_mul_f16 v209, v193, v209\n"
  "v_mfma_f32_16x16x32_bf16 v[176:179], v[224:227], v[64:67], v[176:179]\n"
  "ds_read_b128 v[224:227], v2 offset:22528\n"
  "ds_read_u16_d16_hi v159, v159\n"
  "v_lshlrev_b32_sdwa v161, 1, v161 dst_sel:DWORD dst_unused:UNUSED_PAD src0_sel:DWORD src1_sel:WORD_1\n"
  "v_lshlrev_b32_sdwa v162, 1, v162 dst_sel:DWORD dst_unused:UNUSED_PAD src0_sel:DWORD src1_sel:WORD_1\n"
  "v_mfma_f32_16x16x32_bf16 v[168:171], v[228:231], v[48:51], v[168:171]\n"
  "ds_read_u16 v160, v160\n"
  "v_exp_f16 v212, v208\n"
  "v_mfma_f32_16x16x32_bf16 v[172:175], v[228:231], v[52:55], v[172:175]\n"
  "ds_read_u16_d16_hi v161, v161\n"
  "v_exp_f16 v213, v209\n"
  "v_mfma_f32_16x16x32_bf16 v[176:179], v[228:231], v[72:75], v[176:179]\n"
  "s_waitcnt lgkmcnt(8)\n"
  "ds_read_b128 v[228:231], v0 offset:18432\n"
  "v_lshlrev_b32_sdwa v163, 1, v163 dst_sel:DWORD dst_unused:UNUSED_PAD src0_sel:DWORD src1_sel:WORD_1\n"
  "v_exp_f16_sdwa v212, v208 dst_sel:WORD_1 dst_unused:UNUSED_PRESERVE src0_sel:WORD_1\n"
  "v_mfma_f32_16x16x32_f16 v[128:131], v[216:219], v[164:167], v[80:83]\n"
  "ds_read_u16 v162, v162\n"
  "v_exp_f16_sdwa v213, v209 dst_sel:WORD_1 dst_unused:UNUSED_PRESERVE src0_sel:WORD_1\n"
  "v_mfma_f32_16x16x32_f16 v[104:107], v[216:219], v[188:191], v[80:83]\n"
  "ds_read_u16_d16_hi v163, v163\n"
  "v_pk_add_f16 v212, v212, 1.0 op_sel_hi:[1,0]\n"
  "v_mfma_f32_16x16x32_f16 v[80:83], v[216:219], v[184:187], v[80:83]\n"
  "s_waitcnt lgkmcnt(9)\n"
  "ds_read_b128 v[216:219], v0 offset:20480\n"
  "v_pk_add_f16 v213, v213, 1.0 op_sel_hi:[1,0]\n"
  "v_rcp_f16 v208, v212\n"
  "v_mfma_f32_16x16x32_f16 v[132:135], v[220:223], v[164:167], v[84:87]\n"
  "v_rcp_f16 v209, v213\n"
  "v_mfma_f32_16x16x32_f16 v[108:111], v[220:223], v[188:191], v[84:87]\n"
  "v_rcp_f16_sdwa v208, v212 dst_sel:WORD_1 dst_unused:UNUSED_PRESERVE src0_sel:WORD_1\n"
  "v_mfma_f32_16x16x32_f16 v[84:87], v[220:223], v[184:187], v[84:87]\n"
  "s_waitcnt lgkmcnt(6)\n"
  "ds_read_b128 v[220:223], v0 offset:22528\n"
  "v_rcp_f16_sdwa v209, v213 dst_sel:WORD_1 dst_unused:UNUSED_PRESERVE src0_sel:WORD_1\n"
  "v_mfma_f32_16x16x32_f16 v[136:139], v[224:227], v[164:167], v[88:91]\n"
  "v_or_b32 v196, v156, v157\n"
  "v_pk_mul_f16 v192, v192, v208\n"
  "v_mfma_f32_16x16x32_f16 v[112:115], v[224:227], v[188:191], v[88:91]\n"
  "v_pk_mul_f16 v193, v193, v209\n"
  "v_or_b32 v197, v158, v159\n"
  "v_mfma_f32_16x16x32_f16 v[88:91], v[224:227], v[184:187], v[88:91]\n"
  "s_waitcnt lgkmcnt(2)\n"
  "ds_read_b128 v[224:227], v2 offset:23552\n"
  "v_or_b32 v180, v160, v161\n"
  "v_or_b32 v181, v162, v163\n"
  "v_mfma_f32_16x16x32_bf16 v[152:155], v[228:231], v[32:35], 0\n"
  "v_lshlrev_b32_sdwa v172, 1, v172 dst_sel:DWORD dst_unused:UNUSED_PAD src0_sel:DWORD src1_sel:WORD_1\n"
  "v_lshlrev_b32_sdwa v173, 1, v173 dst_sel:DWORD dst_unused:UNUSED_PAD src0_sel:DWORD src1_sel:WORD_1\n"
  "v_lshlrev_b32_sdwa v174, 1, v174 dst_sel:DWORD dst_unused:UNUSED_PAD src0_sel:DWORD src1_sel:WORD_1\n"
  "v_mfma_f32_16x16x32_bf16 v[156:159], v[228:231], v[36:39], 0\n"
  "ds_read_u16 v172, v172\n"
  "v_cvt_pk_f16_f32 v194, v168, v169\n"
  "v_cvt_pk_f16_f32 v195, v170, v171\n"
  "v_pk_fma_f16 v208, v194, v194, s56\n"
  "v_mfma_f32_16x16x32_bf16 v[160:163], v[228:231], v[56:59], 0\n"
  "s_waitcnt lgkmcnt(3)\n"
  "ds_read_b128 v[228:231], v2 offset:24576\n"
  "v_pk_fma_f16 v209, v195, v195, s56\n"
  "v_lshlrev_b32_sdwa v175, 1, v175 dst_sel:DWORD dst_unused:UNUSED_PAD src0_sel:DWORD src1_sel:WORD_1\n"
  "v_mfma_f32_16x16x32_bf16 v[152:155], v[216:219], v[40:43], v[152:155]\n"
  "ds_read_u16_d16_hi v173, v173\n"
  "v_lshlrev_b32_sdwa v176, 1, v176 dst_sel:DWORD dst_unused:UNUSED_PAD src0_sel:DWORD src1_sel:WORD_1\n"
  "v_pk_mul_f16 v208, v194, v208\n"
  "v_pk_mul_f16 v209, v195, v209\n"
  "v_mfma_f32_16x16x32_bf16 v[156:159], v[216:219], v[44:47], v[156:159]\n"
  "ds_read_u16 v174, v174\n"
  "v_lshlrev_b32_sdwa v177, 1, v177 dst_sel:DWORD dst_unused:UNUSED_PAD src0_sel:DWORD src1_sel:WORD_1\n"
  "v_cvt_pk_bf16_f32 v8, v8, v9\n"
  "v_cvt_pk_bf16_f32 v9, v10, v11\n"
  "v_mfma_f32_16x16x32_bf16 v[160:163], v[216:219], v[64:67], v[160:163]\n"
  "s_waitcnt lgkmcnt(5)\n"
  "ds_read_b128 v[216:219], v2 offset:25600\n"
  "ds_read_u16_d16_hi v175, v175\n"
  "v_cvt_pk_bf16_f32 v10, v12, v13\n"
  "v_cvt_pk_bf16_f32 v11, v14, v15\n"
  "v_cvt_pk_bf16_f32 v16, v16, v17\n"
  "v_mfma_f32_16x16x32_bf16 v[152:155], v[220:223], v[48:51], v[152:155]\n"
  "ds_read_u16 v176, v176\n"
  "v_cvt_pk_bf16_f32 v17, v18, v19\n"
  "v_cvt_pk_bf16_f32 v18, v20, v21\n"
  "v_mfma_f32_16x16x32_bf16 v[156:159], v[220:223], v[52:55], v[156:159]\n"
  "ds_read_u16_d16_hi v177, v177\n"
  "v_cvt_pk_bf16_f32 v19, v22, v23\n"
  "v_cvt_pk_bf16_f32 v24, v24, v25\n"
  "v_cvt_pk_bf16_f32 v25, v26, v27\n"
  "v_mfma_f32_16x16x32_bf16 v[160:163], v[220:223], v[72:75], v[160:163]\n"
  "s_waitcnt lgkmcnt(8)\n"
  "ds_read_b128 v[220:223], v0 offset:18688\n"
  "v_cvt_pk_bf16_f32 v26, v28, v29\n"
  "v_cvt_pk_bf16_f32 v27, v30, v31\n"
  "buffer_store_dwordx4 v[8:11], v5, s[44:47], s61 offen offset:0 sc1\n"
  "buffer_store_dwordx4 v[16:19], v5, s[44:47], s61 offen offset:64 sc1\n"
  "buffer_store_dwordx4 v[24:27], v5, s[44:47], s61 offen offset:128 sc1\n"
  "v_lshlrev_b32_sdwa v178, 1, v178 dst_sel:DWORD dst_unused:UNUSED_PAD src0_sel:DWORD src1_sel:WORD_1\n"
  "v_mfma_f32_16x16x32_f16 v[140:143], v[224:227], v[164:167], v[92:95]\n"
  "ds_read_u16 v178, v178\n"
  "v_exp_f16 v212, v208\n"
  "v_exp_f16 v213, v209\n"
  "v_mfma_f32_16x16x32_f16 v[116:119], v[224:227], v[188:191], v[92:95]\n"
  "v_lshlrev_b32_sdwa v179, 1, v179 dst_sel:DWORD dst_unused:UNUSED_PAD src0_sel:DWORD src1_sel:WORD_1\n"
  "v_exp_f16_sdwa v212, v208 dst_sel:WORD_1 dst_unused:UNUSED_PRESERVE src0_sel:WORD_1\n"
  "v_mfma_f32_16x16x32_f16 v[92:95], v[224:227], v[184:187], v[92:95]\n"
  "s_waitcnt lgkmcnt(8)\n"
  "ds_read_b128 v[224:227], v0 offset:20736\n"
  "ds_read_u16_d16_hi v179, v179\n"
  "v_exp_f16_sdwa v213, v209 dst_sel:WORD_1 dst_unused:UNUSED_PRESERVE src0_sel:WORD_1\n"
  "v_mfma_f32_16x16x32_f16 v[144:147], v[228:231], v[164:167], v[96:99]\n"
  "v_pk_add_f16 v212, v212, 1.0 op_sel_hi:[1,0]\n"
  "v_pk_add_f16 v213, v213, 1.0 op_sel_hi:[1,0]\n"
  "v_mfma_f32_16x16x32_f16 v[120:123], v[228:231], v[188:191], v[96:99]\n"
  "v_rcp_f16 v208, v212\n"
  "v_rcp_f16 v209, v213\n"
  "v_mfma_f32_16x16x32_f16 v[96:99], v[228:231], v[184:187], v[96:99]\n"
  "s_waitcnt lgkmcnt(7)\n"
  "ds_read_b128 v[228:231], v0 offset:22784\n"
  "v_rcp_f16_sdwa v208, v212 dst_sel:WORD_1 dst_unused:UNUSED_PRESERVE src0_sel:WORD_1\n"
  "v_mfma_f32_16x16x32_f16 v[148:151], v[216:219], v[164:167], v[100:103]\n"
  "v_rcp_f16_sdwa v209, v213 dst_sel:WORD_1 dst_unused:UNUSED_PRESERVE src0_sel:WORD_1\n"
  "v_mfma_f32_16x16x32_f16 v[124:127], v[216:219], v[188:191], v[100:103]\n"
  "v_or_b32 v198, v172, v173\n"
  "v_pk_mul_f16 v194, v194, v208\n"
  "v_pk_mul_f16 v195, v195, v209\n"
  "v_mfma_f32_16x16x32_f16 v[100:103], v[216:219], v[184:187], v[100:103]\n"
  "s_waitcnt lgkmcnt(1)\n"
  "ds_read_b128 v[216:219], v2 offset:26624\n"
  "v_or_b32 v199, v174, v175\n"
  "v_or_b32 v182, v176, v177\n"
  "v_or_b32 v183, v178, v179\n"
  "s_setprio 1\n"
  "v_mfma_f32_16x16x32_bf16 v[168:171], v[220:223], v[32:35], 0\n"
  "v_lshlrev_b32_sdwa v156, 1, v156 dst_sel:DWORD dst_unused:UNUSED_PAD src0_sel:DWORD src1_sel:WORD_1\n"
  "v_lshlrev_b32_sdwa v157, 1, v157 dst_sel:DWORD dst_unused:UNUSED_PAD src0_sel:DWORD src1_sel:WORD_1\n"
  "v_mfma_f32_16x16x32_bf16 v[172:175], v[220:223], v[36:39], 0\n"
  "ds_read_u16 v156, v156\n"
  "v_lshlrev_b32_sdwa v158, 1, v158 dst_sel:DWORD dst_unused:UNUSED_PAD src0_sel:DWORD src1_sel:WORD_1\n"
  "v_cvt_pk_f16_f32 v184, v152, v153\n"
  "v_mfma_f32_16x16x32_bf16 v[176:179], v[220:223], v[56:59], 0\n"
  "ds_read_b128 v[220:223], v2 offset:27648\n"
  "v_cvt_pk_f16_f32 v185, v154, v155\n"
  "v_pk_fma_f16 v208, v184, v184, s56\n"
  "v_mfma_f32_16x16x32_bf16 v[168:171], v[224:227], v[40:43], v[168:171]\n"
  "ds_read_u16_d16_hi v157, v157\n"
  "v_pk_fma_f16 v209, v185, v185, s56\n"
  "v_lshlrev_b32_sdwa v159, 1, v159 dst_sel:DWORD dst_unused:UNUSED_PAD src0_sel:DWORD src1_sel:WORD_1\n"
  "v_mfma_f32_16x16x32_bf16 v[172:175], v[224:227], v[44:47], v[172:175]\n"
  "ds_read_u16 v158, v158\n"
  "v_lshlrev_b32_sdwa v160, 1, v160 dst_sel:DWORD dst_unused:UNUSED_PAD src0_sel:DWORD src1_sel:WORD_1\n"
  "v_pk_mul_f16 v208, v184, v208\n"
  "v_pk_mul_f16 v209, v185, v209\n"
  "v_mfma_f32_16x16x32_bf16 v[176:179], v[224:227], v[64:67], v[176:179]\n"
  "s_waitcnt lgkmcnt(5)\n"
  "ds_read_b128 v[224:227], v2 offset:28672\n"
  "ds_read_u16_d16_hi v159, v159\n"
  "v_lshlrev_b32_sdwa v161, 1, v161 dst_sel:DWORD dst_unused:UNUSED_PAD src0_sel:DWORD src1_sel:WORD_1\n"
  "v_lshlrev_b32_sdwa v162, 1, v162 dst_sel:DWORD dst_unused:UNUSED_PAD src0_sel:DWORD src1_sel:WORD_1\n"
  "v_mfma_f32_16x16x32_bf16 v[168:171], v[228:231], v[48:51], v[168:171]\n"
  "ds_read_u16 v160, v160\n"
  "v_exp_f16 v212, v208\n"
  "v_mfma_f32_16x16x32_bf16 v[172:175], v[228:231], v[52:55], v[172:175]\n"
  "ds_read_u16_d16_hi v161, v161\n"
  "v_exp_f16 v213, v209\n"
  "v_mfma_f32_16x16x32_bf16 v[176:179], v[228:231], v[72:75], v[176:179]\n"
  "s_waitcnt lgkmcnt(8)\n"
  "ds_read_b128 v[228:231], v0 offset:24576\n"
  "v_lshlrev_b32_sdwa v163, 1, v163 dst_sel:DWORD dst_unused:UNUSED_PAD src0_sel:DWORD src1_sel:WORD_1\n"
  "v_exp_f16_sdwa v212, v208 dst_sel:WORD_1 dst_unused:UNUSED_PRESERVE src0_sel:WORD_1\n"
  "v_mfma_f32_16x16x32_f16 v[80:83], v[216:219], v[192:195], v[80:83]\n"
  "ds_read_u16 v162, v162\n"
  "v_exp_f16_sdwa v213, v209 dst_sel:WORD_1 dst_unused:UNUSED_PRESERVE src0_sel:WORD_1\n"
  "v_mfma_f32_16x16x32_f16 v[104:107], v[216:219], v[196:199], v[104:107]\n"
  "ds_read_u16_d16_hi v163, v163\n"
  "v_pk_add_f16 v212, v212, 1.0 op_sel_hi:[1,0]\n"
  "v_mfma_f32_16x16x32_f16 v[128:131], v[216:219], v[180:183], v[128:131]\n"
  "s_waitcnt lgkmcnt(9)\n"
  "ds_read_b128 v[216:219], v0 offset:26624\n"
  "v_pk_add_f16 v213, v213, 1.0 op_sel_hi:[1,0]\n"
  "v_rcp_f16 v208, v212\n"
  "v_mfma_f32_16x16x32_f16 v[84:87], v[220:223], v[192:195], v[84:87]\n"
  "v_rcp_f16 v209, v213\n"
  "v_mfma_f32_16x16x32_f16 v[108:111], v[220:223], v[196:199], v[108:111]\n"
  "v_rcp_f16_sdwa v208, v212 dst_sel:WORD_1 dst_unused:UNUSED_PRESERVE src0_sel:WORD_1\n"
  "v_mfma_f32_16x16x32_f16 v[132:135], v[220:223], v[180:183], v[132:135]\n"
  "s_waitcnt lgkmcnt(6)\n"
  "ds_read_b128 v[220:223], v0 offset:28672\n"
  "v_rcp_f16_sdwa v209, v213 dst_sel:WORD_1 dst_unused:UNUSED_PRESERVE src0_sel:WORD_1\n"
  "v_mfma_f32_16x16x32_f16 v[88:91], v[224:227], v[192:195], v[88:91]\n"
  "v_or_b32 v188, v156, v157\n"
  "v_pk_mul_f16 v184, v184, v208\n"
  "v_mfma_f32_16x16x32_f16 v[112:115], v[224:227], v[196:199], v[112:115]\n"
  "v_pk_mul_f16 v185, v185, v209\n"
  "v_or_b32 v189, v158, v159\n"
  "v_mfma_f32_16x16x32_f16 v[136:139], v[224:227], v[180:183], v[136:139]\n"
  "s_waitcnt lgkmcnt(2)\n"
  "ds_read_b128 v[224:227], v2 offset:29696\n"
  "v_or_b32 v164, v160, v161\n"
  "v_or_b32 v165, v162, v163\n"
  "v_mfma_f32_16x16x32_bf16 v[152:155], v[228:231], v[32:35], 0\n"
  "v_lshlrev_b32_sdwa v172, 1, v172 dst_sel:DWORD dst_unused:UNUSED_PAD src0_sel:DWORD src1_sel:WORD_1\n"
  "v_lshlrev_b32_sdwa v173, 1, v173 dst_sel:DWORD dst_unused:UNUSED_PAD src0_sel:DWORD src1_sel:WORD_1\n"
  "v_mfma_f32_16x16x32_bf16 v[156:159], v[228:231], v[36:39], 0\n"
  "ds_read_u16 v172, v172\n"
  "v_lshlrev_b32_sdwa v174, 1, v174 dst_sel:DWORD dst_unused:UNUSED_PAD src0_sel:DWORD src1_sel:WORD_1\n"
  "v_cvt_pk_f16_f32 v186, v168, v169\n"
  "v_mfma_f32_16x16x32_bf16 v[160:163], v[228:231], v[56:59], 0\n"
  "s_waitcnt lgkmcnt(3)\n"
  "ds_read_b128 v[228:231], v2 offset:30720\n"
  "v_cvt_pk_f16_f32 v187, v170, v171\n"
  "v_pk_fma_f16 v208, v186, v186, s56\n"
  "v_mfma_f32_16x16x32_bf16 v[152:155], v[216:219], v[40:43], v[152:155]\n"
  "ds_read_u16_d16_hi v173, v173\n"
  "v_pk_fma_f16 v209, v187, v187, s56\n"
  "v_lshlrev_b32_sdwa v175, 1, v175 dst_sel:DWORD dst_unused:UNUSED_PAD src0_sel:DWORD src1_sel:WORD_1\n"
  "v_mfma_f32_16x16x32_bf16 v[156:159], v[216:219], v[44:47], v[156:159]\n"
  "ds_read_u16 v174, v174\n"
  "v_lshlrev_b32_sdwa v176, 1, v176 dst_sel:DWORD dst_unused:UNUSED_PAD src0_sel:DWORD src1_sel:WORD_1\n"
  "v_pk_mul_f16 v208, v186, v208\n"
  "v_pk_mul_f16 v209, v187, v209\n"
  "v_mfma_f32_16x16x32_bf16 v[160:163], v[216:219], v[64:67], v[160:163]\n"
  "s_waitcnt lgkmcnt(5)\n"
  "ds_read_b128 v[216:219], v2 offset:31744\n"
  "ds_read_u16_d16_hi v175, v175\n"
  "v_lshlrev_b32_sdwa v177, 1, v177 dst_sel:DWORD dst_unused:UNUSED_PAD src0_sel:DWORD src1_sel:WORD_1\n"
  "v_lshlrev_b32_sdwa v178, 1, v178 dst_sel:DWORD dst_unused:UNUSED_PAD src0_sel:DWORD src1_sel:WORD_1\n"
  "v_mfma_f32_16x16x32_bf16 v[152:155], v[220:223], v[48:51], v[152:155]\n"
  "ds_read_u16 v176, v176\n"
  "v_exp_f16 v212, v208\n"
  "v_mfma_f32_16x16x32_bf16 v[156:159], v[220:223], v[52:55], v[156:159]\n"
  "ds_read_u16_d16_hi v177, v177\n"
  "v_exp_f16 v213, v209\n"
  "v_mfma_f32_16x16x32_bf16 v[160:163], v[220:223], v[72:75], v[160:163]\n"
  "s_waitcnt lgkmcnt(8)\n"
  "ds_read_b128 v[220:223], v0 offset:24832\n"
  "v_lshlrev_b32_sdwa v179, 1, v179 dst_sel:DWORD dst_unused:UNUSED_PAD src0_sel:DWORD src1_sel:WORD_1\n"
  "v_exp_f16_sdwa v212, v208 dst_sel:WORD_1 dst_unused:UNUSED_PRESERVE src0_sel:WORD_1\n"
  "v_mfma_f32_16x16x32_f16 v[92:95], v[224:227], v[192:195], v[92:95]\n"
  "ds_read_u16 v178, v178\n"
  "v_exp_f16_sdwa v213, v209 dst_sel:WORD_1 dst_unused:UNUSED_PRESERVE src0_sel:WORD_1\n"
  "v_mfma_f32_16x16x32_f16 v[116:119], v[224:227], v[196:199], v[116:119]\n"
  "ds_read_u16_d16_hi v179, v179\n"
  "v_pk_add_f16 v212, v212, 1.0 op_sel_hi:[1,0]\n"
  "v_mfma_f32_16x16x32_f16 v[140:143], v[224:227], v[180:183], v[140:143]\n"
  "s_waitcnt lgkmcnt(9)\n"
  "ds_read_b128 v[224:227], v0 offset:26880\n"
  "v_pk_add_f16 v213, v213, 1.0 op_sel_hi:[1,0]\n"
  "v_rcp_f16 v208, v212\n"
  "v_mfma_f32_16x16x32_f16 v[96:99], v[228:231], v[192:195], v[96:99]\n"
  "v_rcp_f16 v209, v213\n"
  "v_mfma_f32_16x16x32_f16 v[120:123], v[228:231], v[196:199], v[120:123]\n"
  "v_rcp_f16_sdwa v208, v212 dst_sel:WORD_1 dst_unused:UNUSED_PRESERVE src0_sel:WORD_1\n"
  "v_mfma_f32_16x16x32_f16 v[144:147], v[228:231], v[180:183], v[144:147]\n"
  "s_waitcnt lgkmcnt(6)\n"
  "ds_read_b128 v[228:231], v0 offset:28928\n"
  "v_rcp_f16_sdwa v209, v213 dst_sel:WORD_1 dst_unused:UNUSED_PRESERVE src0_sel:WORD_1\n"
  "v_mfma_f32_16x16x32_f16 v[100:103], v[216:219], v[192:195], v[100:103]\n"
  "v_or_b32 v190, v172, v173\n"
  "v_pk_mul_f16 v186, v186, v208\n"
  "v_mfma_f32_16x16x32_f16 v[124:127], v[216:219], v[196:199], v[124:127]\n"
  "v_pk_mul_f16 v187, v187, v209\n"
  "v_or_b32 v191, v174, v175\n"
  "v_mfma_f32_16x16x32_f16 v[148:151], v[216:219], v[180:183], v[148:151]\n"
  "s_waitcnt lgkmcnt(2)\n"
  "ds_read_b128 v[216:219], v2 offset:32768\n"
  "v_or_b32 v166, v176, v177\n"
  "v_or_b32 v167, v178, v179\n"
  "v_mfma_f32_16x16x32_bf16 v[168:171], v[220:223], v[32:35], 0\n"
  "v_lshlrev_b32_sdwa v156, 1, v156 dst_sel:DWORD dst_unused:UNUSED_PAD src0_sel:DWORD src1_sel:WORD_1\n"
  "v_lshlrev_b32_sdwa v157, 1, v157 dst_sel:DWORD dst_unused:UNUSED_PAD src0_sel:DWORD src1_sel:WORD_1\n"
  "v_mfma_f32_16x16x32_bf16 v[172:175], v[220:223], v[36:39], 0\n"
  "v_lshlrev_b32_sdwa v158, 1, v158 dst_sel:DWORD dst_unused:UNUSED_PAD src0_sel:DWORD src1_sel:WORD_1\n"
  "v_mfma_f32_16x16x32_bf16 v[176:179], v[220:223], v[56:59], 0\n"
  "s_waitcnt lgkmcnt(2)\n"
  "ds_read_b128 v[220:223], v2 offset:33792\n"
  "ds_read_u16 v156, v156\n"
  "v_cvt_pk_f16_f32 v192, v152, v153\n"
  "v_cvt_pk_f16_f32 v193, v154, v155\n"
  "s_waitcnt vmcnt(14)\n"
  "v_mfma_f32_16x16x32_bf16 v[80:83], v[248:251], v[236:239], v[80:83]\n"
  "ds_read_u16_d16_hi v157, v157\n"
  "v_pk_fma_f16 v208, v192, v192, s56\n"
  "v_mfma_f32_16x16x32_bf16 v[84:87], v[252:255], v[236:239], v[84:87]\n"
  "v_pk_fma_f16 v209, v193, v193, s56\n"
  "v_lshlrev_b32_sdwa v159, 1, v159 dst_sel:DWORD dst_unused:UNUSED_PAD src0_sel:DWORD src1_sel:WORD_1\n"
  "v_mfma_f32_16x16x32_bf16 v[168:171], v[224:227], v[40:43], v[168:171]\n"
  "s_waitcnt lgkmcnt(4)\n"
  "ds_read_u16 v158, v158\n"
  "v_lshlrev_b32_sdwa v160, 1, v160 dst_sel:DWORD dst_unused:UNUSED_PAD src0_sel:DWORD src1_sel:WORD_1\n"
  "v_mfma_f32_16x16x32_bf16 v[172:175], v[224:227], v[44:47], v[172:175]\n"
  "v_pk_mul_f16 v208, v192, v208\n"
  "v_pk_mul_f16 v209, v193, v209\n"
  "v_mfma_f32_16x16x32_bf16 v[176:179], v[224:227], v[64:67], v[176:179]\n"
  "ds_read_b128 v[224:227], v2 offset:34816\n"
  "ds_read_u16_d16_hi v159, v159\n"
  "v_lshlrev_b32_sdwa v161, 1, v161 dst_sel:DWORD dst_unused:UNUSED_PAD src0_sel:DWORD src1_sel:WORD_1\n"
  "v_lshlrev_b32_sdwa v162, 1, v162 dst_sel:DWORD dst_unused:UNUSED_PAD src0_sel:DWORD src1_sel:WORD_1\n"
  "v_mfma_f32_16x16x32_bf16 v[168:171], v[228:231], v[48:51], v[168:171]\n"
  "ds_read_u16 v160, v160\n"
  "v_exp_f16 v212, v208\n"
  "v_mfma_f32_16x16x32_bf16 v[172:175], v[228:231], v[52:55], v[172:175]\n"
  "v_exp_f16 v213, v209\n"
  "v_mfma_f32_16x16x32_bf16 v[176:179], v[228:231], v[72:75], v[176:179]\n"
  "ds_read_b128 v[228:231], v0 offset:30720\n"
  "ds_read_u16_d16_hi v161, v161\n"
  "s_waitcnt vmcnt(13)\n"
  "v_mfma_f32_16x16x32_bf16 v[88:91], v[248:251], v[240:243], v[88:91]\n"
  "s_waitcnt lgkmcnt(9)\n"
  "ds_read_u16 v162, v162\n"
  "v_lshlrev_b32_sdwa v163, 1, v163 dst_sel:DWORD dst_unused:UNUSED_PAD src0_sel:DWORD src1_sel:WORD_1\n"
  "v_exp_f16_sdwa v212, v208 dst_sel:WORD_1 dst_unused:UNUSED_PRESERVE src0_sel:WORD_1\n"
  "v_mfma_f32_16x16x32_bf16 v[92:95], v[252:255], v[240:243], v[92:95]\n"
  "v_exp_f16_sdwa v213, v209 dst_sel:WORD_1 dst_unused:UNUSED_PRESERVE src0_sel:WORD_1\n"
  "v_mfma_f32_16x16x32_f16 v[80:83], v[216:219], v[184:187], v[80:83]\n"
  "ds_read_u16_d16_hi v163, v163\n"
  "v_mfma_f32_16x16x32_f16 v[104:107], v[216:219], v[188:191], v[104:107]\n"
  "s_waitcnt lgkmcnt(10)\n"
  "v_pk_add_f16 v212, v212, 1.0 op_sel_hi:[1,0]\n"
  "v_pk_add_f16 v213, v213, 1.0 op_sel_hi:[1,0]\n"
  "v_mfma_f32_16x16x32_f16 v[128:131], v[216:219], v[164:167], v[128:131]\n"
  "ds_read_b128 v[216:219], v0 offset:32768\n"
  "v_rcp_f16 v208, v212\n"
  "v_mfma_f32_16x16x32_f16 v[84:87], v[220:223], v[184:187], v[84:87]\n"
  "v_rcp_f16 v209, v213\n"
  "v_mfma_f32_16x16x32_f16 v[108:111], v[220:223], v[188:191], v[108:111]\n"
  "v_mfma_f32_16x16x32_f16 v[132:135], v[220:223], v[164:167], v[132:135]\n"
  "ds_read_b128 v[220:223], v0 offset:34816\n"
  "v_rcp_f16_sdwa v208, v212 dst_sel:WORD_1 dst_unused:UNUSED_PRESERVE src0_sel:WORD_1\n"
  "s_waitcnt vmcnt(12)\n"
  "v_mfma_f32_16x16x32_bf16 v[96:99], v[248:251], v[244:247], v[96:99]\n"
  "v_rcp_f16_sdwa v209, v213 dst_sel:WORD_1 dst_unused:UNUSED_PRESERVE src0_sel:WORD_1\n"
  "v_mfma_f32_16x16x32_bf16 v[100:103], v[252:255], v[244:247], v[100:103]\n"
  "s_waitcnt lgkmcnt(7)\n"
  "buffer_load_dwordx4 v[236:239], v5, s[40:43], s57 offen offset:0 \n"
  "buffer_load_dwordx4 v[240:243], v5, s[40:43], s57 offen offset:64 \n"
  "buffer_load_dwordx4 v[244:247], v5, s[40:43], s57 offen offset:128 \n"
  "v_or_b32 v196, v156, v157\n"
  "v_mfma_f32_16x16x32_f16 v[88:91], v[224:227], v[184:187], v[88:91]\n"
  "v_pk_mul_f16 v192, v192, v208\n"
  "v_pk_mul_f16 v193, v193, v209\n"
  "v_mfma_f32_16x16x32_f16 v[112:115], v[224:227], v[188:191], v[112:115]\n"
  "v_or_b32 v197, v158, v159\n"
  "v_mfma_f32_16x16x32_f16 v[136:139], v[224:227], v[164:167], v[136:139]\n"
  "s_waitcnt lgkmcnt(2)\n"
  "ds_read_b128 v[224:227], v2 offset:35840\n"
  "v_or_b32 v180, v160, v161\n"
  "v_or_b32 v181, v162, v163\n"
  "v_mfma_f32_16x16x32_bf16 v[152:155], v[228:231], v[32:35], 0\n"
  "v_lshlrev_b32_sdwa v172, 1, v172 dst_sel:DWORD dst_unused:UNUSED_PAD src0_sel:DWORD src1_sel:WORD_1\n"
  "v_lshlrev_b32_sdwa v173, 1, v173 dst_sel:DWORD dst_unused:UNUSED_PAD src0_sel:DWORD src1_sel:WORD_1\n"
  "v_mfma_f32_16x16x32_bf16 v[156:159], v[228:231], v[36:39], 0\n"
  "ds_read_u16 v172, v172\n"
  "v_lshlrev_b32_sdwa v174, 1, v174 dst_sel:DWORD dst_unused:UNUSED_PAD src0_sel:DWORD src1_sel:WORD_1\n"
  "v_cvt_pk_f16_f32 v194, v168, v169\n"
  "v_mfma_f32_16x16x32_bf16 v[160:163], v[228:231], v[56:59], 0\n"
  "s_waitcnt lgkmcnt(3)\n"
  "ds_read_b128 v[228:231], v2 offset:36864\n"
  "v_cvt_pk_f16_f32 v195, v170, v171\n"
  "v_pk_fma_f16 v208, v194, v194, s56\n"
  "v_mfma_f32_16x16x32_bf16 v[152:155], v[216:219], v[40:43], v[152:155]\n"
  "ds_read_u16_d16_hi v173, v173\n"
  "v_pk_fma_f16 v209, v195, v195, s56\n"
  "v_lshlrev_b32_sdwa v175, 1, v175 dst_sel:DWORD dst_unused:UNUSED_PAD src0_sel:DWORD src1_sel:WORD_1\n"
  "v_mfma_f32_16x16x32_bf16 v[156:159], v[216:219], v[44:47], v[156:159]\n"
  "ds_read_u16 v174, v174\n"
  "v_lshlrev_b32_sdwa v176, 1, v176 dst_sel:DWORD dst_unused:UNUSED_PAD src0_sel:DWORD src1_sel:WORD_1\n"
  "v_pk_mul_f16 v208, v194, v208\n"
  "v_pk_mul_f16 v209, v195, v209\n"
  "v_mfma_f32_16x16x32_bf16 v[160:163], v[216:219], v[64:67], v[160:163]\n"
  "s_waitcnt lgkmcnt(5)\n"
  "ds_read_b128 v[216:219], v2 offset:37888\n"
  "ds_read_u16_d16_hi v175, v175\n"
  "v_lshlrev_b32_sdwa v177, 1, v177 dst_sel:DWORD dst_unused:UNUSED_PAD src0_sel:DWORD src1_sel:WORD_1\n"
  "v_lshlrev_b32_sdwa v178, 1, v178 dst_sel:DWORD dst_unused:UNUSED_PAD src0_sel:DWORD src1_sel:WORD_1\n"
  "v_mfma_f32_16x16x32_bf16 v[152:155], v[220:223], v[48:51], v[152:155]\n"
  "ds_read_u16 v176, v176\n"
  "v_exp_f16 v212, v208\n"
  "v_mfma_f32_16x16x32_bf16 v[156:159], v[220:223], v[52:55], v[156:159]\n"
  "ds_read_u16_d16_hi v177, v177\n"
  "v_exp_f16 v213, v209\n"
  "v_mfma_f32_16x16x32_bf16 v[160:163], v[220:223], v[72:75], v[160:163]\n"
  "s_waitcnt lgkmcnt(8)\n"
  "ds_read_b128 v[220:223], v0 offset:30976\n"
  "v_lshlrev_b32_sdwa v179, 1, v179 dst_sel:DWORD dst_unused:UNUSED_PAD src0_sel:DWORD src1_sel:WORD_1\n"
  "v_exp_f16_sdwa v212, v208 dst_sel:WORD_1 dst_unused:UNUSED_PRESERVE src0_sel:WORD_1\n"
  "v_mfma_f32_16x16x32_f16 v[92:95], v[224:227], v[184:187], v[92:95]\n"
  "ds_read_u16 v178, v178\n"
  "v_exp_f16_sdwa v213, v209 dst_sel:WORD_1 dst_unused:UNUSED_PRESERVE src0_sel:WORD_1\n"
  "v_mfma_f32_16x16x32_f16 v[116:119], v[224:227], v[188:191], v[116:119]\n"
  "ds_read_u16_d16_hi v179, v179\n"
  "v_pk_add_f16 v212, v212, 1.0 op_sel_hi:[1,0]\n"
  "v_mfma_f32_16x16x32_f16 v[140:143], v[224:227], v[164:167], v[140:143]\n"
  "s_waitcnt lgkmcnt(9)\n"
  "ds_read_b128 v[224:227], v0 offset:33024\n"
  "v_pk_add_f16 v213, v213, 1.0 op_sel_hi:[1,0]\n"
  "v_rcp_f16 v208, v212\n"
  "v_mfma_f32_16x16x32_f16 v[96:99], v[228:231], v[184:187], v[96:99]\n"
  "v_rcp_f16 v209, v213\n"
  "v_mfma_f32_16x16x32_f16 v[120:123], v[228:231], v[188:191], v[120:123]\n"
  "v_rcp_f16_sdwa v208, v212 dst_sel:WORD_1 dst_unused:UNUSED_PRESERVE src0_sel:WORD_1\n"
  "v_mfma_f32_16x16x32_f16 v[144:147], v[228:231], v[164:167], v[144:147]\n"
  "s_waitcnt lgkmcnt(6)\n"
  "ds_read_b128 v[228:231], v0 offset:35072\n"
  "v_rcp_f16_sdwa v209, v213 dst_sel:WORD_1 dst_unused:UNUSED_PRESERVE src0_sel:WORD_1\n"
  "v_mfma_f32_16x16x32_f16 v[100:103], v[216:219], v[184:187], v[100:103]\n"
  "v_or_b32 v198, v172, v173\n"
  "v_pk_mul_f16 v194, v194, v208\n"
  "v_mfma_f32_16x16x32_f16 v[124:127], v[216:219], v[188:191], v[124:127]\n"
  "v_pk_mul_f16 v195, v195, v209\n"
  "v_or_b32 v199, v174, v175\n"
  "v_mfma_f32_16x16x32_f16 v[148:151], v[216:219], v[164:167], v[148:151]\n"
  "s_waitcnt lgkmcnt(2)\n"
  "ds_read_b128 v[216:219], v2 offset:38912\n"
  "v_or_b32 v182, v176, v177\n"
  "v_or_b32 v183, v178, v179\n"
  "v_mfma_f32_16x16x32_bf16 v[168:171], v[220:223], v[32:35], 0\n"
  "v_lshlrev_b32_sdwa v156, 1, v156 dst_sel:DWORD dst_unused:UNUSED_PAD src0_sel:DWORD src1_sel:WORD_1\n"
  "v_lshlrev_b32_sdwa v157, 1, v157 dst_sel:DWORD dst_unused:UNUSED_PAD src0_sel:DWORD src1_sel:WORD_1\n"
  "v_mfma_f32_16x16x32_bf16 v[172:175], v[220:223], v[36:39], 0\n"
  "ds_read_u16 v156, v156\n"
  "v_lshlrev_b32_sdwa v158, 1, v158 dst_sel:DWORD dst_unused:UNUSED_PAD src0_sel:DWORD src1_sel:WORD_1\n"
  "v_cvt_pk_f16_f32 v184, v152, v153\n"
  "v_mfma_f32_16x16x32_bf16 v[176:179], v[220:223], v[56:59], 0\n"
  "s_waitcnt lgkmcnt(3)\n"
  "ds_read_b128 v[220:223], v2 offset:39936\n"
  "v_cvt_pk_f16_f32 v185, v154, v155\n"
  "v_pk_fma_f16 v208, v184, v184, s56\n"
  "v_mfma_f32_16x16x32_bf16 v[168:171], v[224:227], v[40:43], v[168:171]\n"
  "ds_read_u16_d16_hi v157, v157\n"
  "v_pk_fma_f16 v209, v185, v185, s56\n"
  "v_lshlrev_b32_sdwa v159, 1, v159 dst_sel:DWORD dst_unused:UNUSED_PAD src0_sel:DWORD src1_sel:WORD_1\n"
  "v_mfma_f32_16x16x32_bf16 v[172:175], v[224:227], v[44:47], v[172:175]\n"
  "ds_read_u16 v158, v158\n"
  "v_lshlrev_b32_sdwa v160, 1, v160 dst_sel:DWORD dst_unused:UNUSED_PAD src0_sel:DWORD src1_sel:WORD_1\n"
  "v_pk_mul_f16 v208, v184, v208\n"
  "v_pk_mul_f16 v209, v185, v209\n"
  "v_mfma_f32_16x16x32_bf16 v[176:179], v[224:227], v[64:67], v[176:179]\n"
  "s_waitcnt lgkmcnt(5)\n"
  "ds_read_b128 v[224:227], v2 offset:40960\n"
  "ds_read_u16_d16_hi v159, v159\n"
  "v_lshlrev_b32_sdwa v161, 1, v161 dst_sel:DWORD dst_unused:UNUSED_PAD src0_sel:DWORD src1_sel:WORD_1\n"
  "v_lshlrev_b32_sdwa v162, 1, v162 dst_sel:DWORD dst_unused:UNUSED_PAD src0_sel:DWORD src1_sel:WORD_1\n"
  "v_mfma_f32_16x16x32_bf16 v[168:171], v[228:231], v[48:51], v[168:171]\n"
  "ds_read_u16 v160, v160\n"
  "v_exp_f16 v212, v208\n"
  "v_mfma_f32_16x16x32_bf16 v[172:175], v[228:231], v[52:55], v[172:175]\n"
  "ds_read_u16_d16_hi v161, v161\n"
  "v_exp_f16 v213, v209\n"
  "v_mfma_f32_16x16x32_bf16 v[176:179], v[228:231], v[72:75], v[176:179]\n"
  "s_waitcnt lgkmcnt(8)\n"
  "ds_read_b128 v[228:231], v0 offset:36864\n"
  "v_lshlrev_b32_sdwa v163, 1, v163 dst_sel:DWORD dst_unused:UNUSED_PAD src0_sel:DWORD src1_sel:WORD_1\n"
  "v_exp_f16_sdwa v212, v208 dst_sel:WORD_1 dst_unused:UNUSED_PRESERVE src0_sel:WORD_1\n"
  "v_mfma_f32_16x16x32_f16 v[80:83], v[216:219], v[192:195], v[80:83]\n"
  "ds_read_u16 v162, v162\n"
  "v_exp_f16_sdwa v213, v209 dst_sel:WORD_1 dst_unused:UNUSED_PRESERVE src0_sel:WORD_1\n"
  "v_mfma_f32_16x16x32_f16 v[104:107], v[216:219], v[196:199], v[104:107]\n"
  "ds_read_u16_d16_hi v163, v163\n"
  "v_pk_add_f16 v212, v212, 1.0 op_sel_hi:[1,0]\n"
  "v_mfma_f32_16x16x32_f16 v[128:131], v[216:219], v[180:183], v[128:131]\n"
  "s_waitcnt lgkmcnt(9)\n"
  "ds_read_b128 v[216:219], v0 offset:38912\n"
  "v_pk_add_f16 v213, v213, 1.0 op_sel_hi:[1,0]\n"
  "v_rcp_f16 v208, v212\n"
  "v_mfma_f32_16x16x32_f16 v[84:87], v[220:223], v[192:195], v[84:87]\n"
  "v_rcp_f16 v209, v213\n"
  "v_mfma_f32_16x16x32_f16 v[108:111], v[220:223], v[196:199], v[108:111]\n"
  "v_rcp_f16_sdwa v208, v212 dst_sel:WORD_1 dst_unused:UNUSED_PRESERVE src0_sel:WORD_1\n"
  "v_mfma_f32_16x16x32_f16 v[132:135], v[220:223], v[180:183], v[132:135]\n"
  "s_waitcnt lgkmcnt(6)\n"
  "ds_read_b128 v[220:223], v0 offset:40960\n"
  "v_rcp_f16_sdwa v209, v213 dst_sel:WORD_1 dst_unused:UNUSED_PRESERVE src0_sel:WORD_1\n"
  "v_mfma_f32_16x16x32_f16 v[88:91], v[224:227], v[192:195], v[88:91]\n"
  "v_or_b32 v188, v156, v157\n"
  "v_pk_mul_f16 v184, v184, v208\n"
  "v_mfma_f32_16x16x32_f16 v[112:115], v[224:227], v[196:199], v[112:115]\n"
  "v_pk_mul_f16 v185, v185, v209\n"
  "v_or_b32 v189, v158, v159\n"
  "v_mfma_f32_16x16x32_f16 v[136:139], v[224:227], v[180:183], v[136:139]\n"
  "s_waitcnt lgkmcnt(2)\n"
  "ds_read_b128 v[224:227], v2 offset:41984\n"
  "v_or_b32 v164, v160, v161\n"
  "v_or_b32 v165, v162, v163\n"
  "v_mfma_f32_16x16x32_bf16 v[152:155], v[228:231], v[32:35], 0\n"
  "v_lshlrev_b32_sdwa v172, 1, v172 dst_sel:DWORD dst_unused:UNUSED_PAD src0_sel:DWORD src1_sel:WORD_1\n"
  "v_lshlrev_b32_sdwa v173, 1, v173 dst_sel:DWORD dst_unused:UNUSED_PAD src0_sel:DWORD src1_sel:WORD_1\n"
  "v_mfma_f32_16x16x32_bf16 v[156:159], v[228:231], v[36:39], 0\n"
  "ds_read_u16 v172, v172\n"
  "v_lshlrev_b32_sdwa v174, 1, v174 dst_sel:DWORD dst_unused:UNUSED_PAD src0_sel:DWORD src1_sel:WORD_1\n"
  "v_cvt_pk_f16_f32 v186, v168, v169\n"
  "v_mfma_f32_16x16x32_bf16 v[160:163], v[228:231], v[56:59], 0\n"
  "s_waitcnt lgkmcnt(3)\n"
  "ds_read_b128 v[228:231], v2 offset:43008\n"
  "v_cvt_pk_f16_f32 v187, v170, v171\n"
  "v_pk_fma_f16 v208, v186, v186, s56\n"
  "v_mfma_f32_16x16x32_bf16 v[152:155], v[216:219], v[40:43], v[152:155]\n"
  "ds_read_u16_d16_hi v173, v173\n"
  "v_pk_fma_f16 v209, v187, v187, s56\n"
  "v_lshlrev_b32_sdwa v175, 1, v175 dst_sel:DWORD dst_unused:UNUSED_PAD src0_sel:DWORD src1_sel:WORD_1\n"
  "v_mfma_f32_16x16x32_bf16 v[156:159], v[216:219], v[44:47], v[156:159]\n"
  "ds_read_u16 v174, v174\n"
  "v_lshlrev_b32_sdwa v176, 1, v176 dst_sel:DWORD dst_unused:UNUSED_PAD src0_sel:DWORD src1_sel:WORD_1\n"
  "v_pk_mul_f16 v208, v186, v208\n"
  "v_pk_mul_f16 v209, v187, v209\n"
  "v_mfma_f32_16x16x32_bf16 v[160:163], v[216:219], v[64:67], v[160:163]\n"
  "s_waitcnt lgkmcnt(5)\n"
  "ds_read_b128 v[216:219], v2 offset:44032\n"
  "ds_read_u16_d16_hi v175, v175\n"
  "v_lshlrev_b32_sdwa v177, 1, v177 dst_sel:DWORD dst_unused:UNUSED_PAD src0_sel:DWORD src1_sel:WORD_1\n"
  "v_lshlrev_b32_sdwa v178, 1, v178 dst_sel:DWORD dst_unused:UNUSED_PAD src0_sel:DWORD src1_sel:WORD_1\n"
  "v_mfma_f32_16x16x32_bf16 v[152:155], v[220:223], v[48:51], v[152:155]\n"
  "ds_read_u16 v176, v176\n"
  "v_exp_f16 v212, v208\n"
  "v_mfma_f32_16x16x32_bf16 v[156:159], v[220:223], v[52:55], v[156:159]\n"
  "ds_read_u16_d16_hi v177, v177\n"
  "v_exp_f16 v213, v209\n"
  "v_mfma_f32_16x16x32_bf16 v[160:163], v[220:223], v[72:75], v[160:163]\n"
  "s_waitcnt lgkmcnt(8)\n"
  "ds_read_b128 v[220:223], v0 offset:37120\n"
  "v_lshlrev_b32_sdwa v179, 1, v179 dst_sel:DWORD dst_unused:UNUSED_PAD src0_sel:DWORD src1_sel:WORD_1\n"
  "v_exp_f16_sdwa v212, v208 dst_sel:WORD_1 dst_unused:UNUSED_PRESERVE src0_sel:WORD_1\n"
  "v_mfma_f32_16x16x32_f16 v[92:95], v[224:227], v[192:195], v[92:95]\n"
  "ds_read_u16 v178, v178\n"
  "v_exp_f16_sdwa v213, v209 dst_sel:WORD_1 dst_unused:UNUSED_PRESERVE src0_sel:WORD_1\n"
  "v_mfma_f32_16x16x32_f16 v[116:119], v[224:227], v[196:199], v[116:119]\n"
  "ds_read_u16_d16_hi v179, v179\n"
  "v_pk_add_f16 v212, v212, 1.0 op_sel_hi:[1,0]\n"
  "v_mfma_f32_16x16x32_f16 v[140:143], v[224:227], v[180:183], v[140:143]\n"
  "s_waitcnt lgkmcnt(9)\n"
  "ds_read_b128 v[224:227], v0 offset:39168\n"
  "v_pk_add_f16 v213, v213, 1.0 op_sel_hi:[1,0]\n"
  "v_rcp_f16 v208, v212\n"
  "v_mfma_f32_16x16x32_f16 v[96:99], v[228:231], v[192:195], v[96:99]\n"
  "v_rcp_f16 v209, v213\n"
  "v_mfma_f32_16x16x32_f16 v[120:123], v[228:231], v[196:199], v[120:123]\n"
  "v_rcp_f16_sdwa v208, v212 dst_sel:WORD_1 dst_unused:UNUSED_PRESERVE src0_sel:WORD_1\n"
  "v_mfma_f32_16x16x32_f16 v[144:147], v[228:231], v[180:183], v[144:147]\n"
  "s_waitcnt lgkmcnt(6)\n"
  "ds_read_b128 v[228:231], v0 offset:41216\n"
  "v_rcp_f16_sdwa v209, v213 dst_sel:WORD_1 dst_unused:UNUSED_PRESERVE src0_sel:WORD_1\n"
  "v_mfma_f32_16x16x32_f16 v[100:103], v[216:219], v[192:195], v[100:103]\n"
  "v_or_b32 v190, v172, v173\n"
  "v_pk_mul_f16 v186, v186, v208\n"
  "v_mfma_f32_16x16x32_f16 v[124:127], v[216:219], v[196:199], v[124:127]\n"
  "v_pk_mul_f16 v187, v187, v209\n"
  "v_or_b32 v191, v174, v175\n"
  "v_mfma_f32_16x16x32_f16 v[148:151], v[216:219], v[180:183], v[148:151]\n"
  "s_waitcnt lgkmcnt(2)\n"
  "ds_read_b128 v[216:219], v2 offset:45056\n"
  "v_or_b32 v166, v176, v177\n"
  "v_or_b32 v167, v178, v179\n"
  "v_mfma_f32_16x16x32_bf16 v[168:171], v[220:223], v[32:35], 0\n"
  "v_lshlrev_b32_sdwa v156, 1, v156 dst_sel:DWORD dst_unused:UNUSED_PAD src0_sel:DWORD src1_sel:WORD_1\n"
  "v_lshlrev_b32_sdwa v157, 1, v157 dst_sel:DWORD dst_unused:UNUSED_PAD src0_sel:DWORD src1_sel:WORD_1\n"
  "v_mfma_f32_16x16x32_bf16 v[172:175], v[220:223], v[36:39], 0\n"
  "ds_read_u16 v156, v156\n"
  "v_lshlrev_b32_sdwa v158, 1, v158 dst_sel:DWORD dst_unused:UNUSED_PAD src0_sel:DWORD src1_sel:WORD_1\n"
  "v_cvt_pk_f16_f32 v192, v152, v153\n"
  "v_mfma_f32_16x16x32_bf16 v[176:179], v[220:223], v[56:59], 0\n"
  "s_waitcnt lgkmcnt(3)\n"
  "ds_read_b128 v[220:223], v2 offset:46080\n"
  "v_cvt_pk_f16_f32 v193, v154, v155\n"
  "v_pk_fma_f16 v208, v192, v192, s56\n"
  "v_mfma_f32_16x16x32_bf16 v[168:171], v[224:227], v[40:43], v[168:171]\n"
  "ds_read_u16_d16_hi v157, v157\n"
  "v_pk_fma_f16 v209, v193, v193, s56\n"
  "v_lshlrev_b32_sdwa v159, 1, v159 dst_sel:DWORD dst_unused:UNUSED_PAD src0_sel:DWORD src1_sel:WORD_1\n"
  "v_mfma_f32_16x16x32_bf16 v[172:175], v[224:227], v[44:47], v[172:175]\n"
  "ds_read_u16 v158, v158\n"
  "v_lshlrev_b32_sdwa v160, 1, v160 dst_sel:DWORD dst_unused:UNUSED_PAD src0_sel:DWORD src1_sel:WORD_1\n"
  "v_pk_mul_f16 v208, v192, v208\n"
  "v_pk_mul_f16 v209, v193, v209\n"
  "v_mfma_f32_16x16x32_bf16 v[176:179], v[224:227], v[64:67], v[176:179]\n"
  "s_waitcnt lgkmcnt(5)\n"
  "ds_read_b128 v[224:227], v2 offset:47104\n"
  "ds_read_u16_d16_hi v159, v159\n"
  "v_lshlrev_b32_sdwa v161, 1, v161 dst_sel:DWORD dst_unused:UNUSED_PAD src0_sel:DWORD src1_sel:WORD_1\n"
  "v_lshlrev_b32_sdwa v162, 1, v162 dst_sel:DWORD dst_unused:UNUSED_PAD src0_sel:DWORD src1_sel:WORD_1\n"
  "v_mfma_f32_16x16x32_bf16 v[168:171], v[228:231], v[48:51], v[168:171]\n"
  "ds_read_u16 v160, v160\n"
  "v_exp_f16 v212, v208\n"
  "v_mfma_f32_16x16x32_bf16 v[172:175], v[228:231], v[52:55], v[172:175]\n"
  "ds_read_u16_d16_hi v161, v161\n"
  "v_exp_f16 v213, v209\n"
  "v_mfma_f32_16x16x32_bf16 v[176:179], v[228:231], v[72:75], v[176:179]\n"
  "s_waitcnt lgkmcnt(8)\n"
  "ds_read_b128 v[228:231], v0 offset:43008\n"
  "v_lshlrev_b32_sdwa v163, 1, v163 dst_sel:DWORD dst_unused:UNUSED_PAD src0_sel:DWORD src1_sel:WORD_1\n"
  "v_exp_f16_sdwa v212, v208 dst_sel:WORD_1 dst_unused:UNUSED_PRESERVE src0_sel:WORD_1\n"
  "v_mfma_f32_16x16x32_f16 v[80:83], v[216:219], v[184:187], v[80:83]\n"
  "ds_read_u16 v162, v162\n"
  "v_exp_f16_sdwa v213, v209 dst_sel:WORD_1 dst_unused:UNUSED_PRESERVE src0_sel:WORD_1\n"
  "v_mfma_f32_16x16x32_f16 v[104:107], v[216:219], v[188:191], v[104:107]\n"
  "ds_read_u16_d16_hi v163, v163\n"
  "v_pk_add_f16 v212, v212, 1.0 op_sel_hi:[1,0]\n"
  "v_mfma_f32_16x16x32_f16 v[128:131], v[216:219], v[164:167], v[128:131]\n"
  "s_waitcnt lgkmcnt(9)\n"
  "ds_read_b128 v[216:219], v0 offset:45056\n"
  "v_pk_add_f16 v213, v213, 1.0 op_sel_hi:[1,0]\n"
  "v_rcp_f16 v208, v212\n"
  "v_mfma_f32_16x16x32_f16 v[84:87], v[220:223], v[184:187], v[84:87]\n"
  "v_rcp_f16 v209, v213\n"
  "v_mfma_f32_16x16x32_f16 v[108:111], v[220:223], v[188:191], v[108:111]\n"
  "v_rcp_f16_sdwa v208, v212 dst_sel:WORD_1 dst_unused:UNUSED_PRESERVE src0_sel:WORD_1\n"
  "v_mfma_f32_16x16x32_f16 v[132:135], v[220:223], v[164:167], v[132:135]\n"
  "s_waitcnt lgkmcnt(6)\n"
  "ds_read_b128 v[220:223], v0 offset:47104\n"
  "v_rcp_f16_sdwa v209, v213 dst_sel:WORD_1 dst_unused:UNUSED_PRESERVE src0_sel:WORD_1\n"
  "v_mfma_f32_16x16x32_f16 v[88:91], v[224:227], v[184:187], v[88:91]\n"
  "v_or_b32 v196, v156, v157\n"
  "v_pk_mul_f16 v192, v192, v208\n"
  "v_mfma_f32_16x16x32_f16 v[112:115], v[224:227], v[188:191], v[112:115]\n"
  "v_pk_mul_f16 v193, v193, v209\n"
  "v_or_b32 v197, v158, v159\n"
  "v_mfma_f32_16x16x32_f16 v[136:139], v[224:227], v[164:167], v[136:139]\n"
  "s_waitcnt lgkmcnt(2)\n"
  "ds_read_b128 v[224:227], v2 offset:48128\n"
  "v_or_b32 v180, v160, v161\n"
  "v_or_b32 v181, v162, v163\n"
  "v_mfma_f32_16x16x32_bf16 v[152:155], v[228:231], v[32:35], 0\n"
  "v_lshlrev_b32_sdwa v172, 1, v172 dst_sel:DWORD dst_unused:UNUSED_PAD src0_sel:DWORD src1_sel:WORD_1\n"
  "v_lshlrev_b32_sdwa v173, 1, v173 dst_sel:DWORD dst_unused:UNUSED_PAD src0_sel:DWORD src1_sel:WORD_1\n"
  "v_mfma_f32_16x16x32_bf16 v[156:159], v[228:231], v[36:39], 0\n"
  "ds_read_u16 v172, v172\n"
  "v_lshlrev_b32_sdwa v174, 1, v174 dst_sel:DWORD dst_unused:UNUSED_PAD src0_sel:DWORD src1_sel:WORD_1\n"
  "v_cvt_pk_f16_f32 v194, v168, v169\n"
  "v_mfma_f32_16x16x32_bf16 v[160:163], v[228:231], v[56:59], 0\n"
  "s_waitcnt lgkmcnt(3)\n"
  "ds_read_b128 v[228:231], v2 offset:49152\n"
  "v_cvt_pk_f16_f32 v195, v170, v171\n"
  "v_pk_fma_f16 v208, v194, v194, s56\n"
  "v_mfma_f32_16x16x32_bf16 v[152:155], v[216:219], v[40:43], v[152:155]\n"
  "ds_read_u16_d16_hi v173, v173\n"
  "v_pk_fma_f16 v209, v195, v195, s56\n"
  "v_lshlrev_b32_sdwa v175, 1, v175 dst_sel:DWORD dst_unused:UNUSED_PAD src0_sel:DWORD src1_sel:WORD_1\n"
  "v_mfma_f32_16x16x32_bf16 v[156:159], v[216:219], v[44:47], v[156:159]\n"
  "ds_read_u16 v174, v174\n"
  "v_lshlrev_b32_sdwa v176, 1, v176 dst_sel:DWORD dst_unused:UNUSED_PAD src0_sel:DWORD src1_sel:WORD_1\n"
  "v_pk_mul_f16 v208, v194, v208\n"
  "v_pk_mul_f16 v209, v195, v209\n"
  "v_mfma_f32_16x16x32_bf16 v[160:163], v[216:219], v[64:67], v[160:163]\n"
  "s_waitcnt lgkmcnt(5)\n"
  "ds_read_b128 v[216:219], v2 offset:50176\n"
  "ds_read_u16_d16_hi v175, v175\n"
  "v_lshlrev_b32_sdwa v177, 1, v177 dst_sel:DWORD dst_unused:UNUSED_PAD src0_sel:DWORD src1_sel:WORD_1\n"
  "v_lshlrev_b32_sdwa v178, 1, v178 dst_sel:DWORD dst_unused:UNUSED_PAD src0_sel:DWORD src1_sel:WORD_1\n"
  "v_mfma_f32_16x16x32_bf16 v[152:155], v[220:223], v[48:51], v[152:155]\n"
  "ds_read_u16 v176, v176\n"
  "v_exp_f16 v212, v208\n"
  "v_mfma_f32_16x16x32_bf16 v[156:159], v[220:223], v[52:55], v[156:159]\n"
  "ds_read_u16_d16_hi v177, v177\n"
  "v_exp_f16 v213, v209\n"
  "v_mfma_f32_16x16x32_bf16 v[160:163], v[220:223], v[72:75], v[160:163]\n"
  "s_waitcnt lgkmcnt(8)\n"
  "ds_read_b128 v[220:223], v0 offset:43264\n"
  "v_lshlrev_b32_sdwa v179, 1, v179 dst_sel:DWORD dst_unused:UNUSED_PAD src0_sel:DWORD src1_sel:WORD_1\n"
  "v_exp_f16_sdwa v212, v208 dst_sel:WORD_1 dst_unused:UNUSED_PRESERVE src0_sel:WORD_1\n"
  "v_mfma_f32_16x16x32_f16 v[92:95], v[224:227], v[184:187], v[92:95]\n"
  "ds_read_u16 v178, v178\n"
  "v_exp_f16_sdwa v213, v209 dst_sel:WORD_1 dst_unused:UNUSED_PRESERVE src0_sel:WORD_1\n"
  "v_mfma_f32_16x16x32_f16 v[116:119], v[224:227], v[188:191], v[116:119]\n"
  "ds_read_u16_d16_hi v179, v179\n"
  "v_pk_add_f16 v212, v212, 1.0 op_sel_hi:[1,0]\n"
  "v_mfma_f32_16x16x32_f16 v[140:143], v[224:227], v[164:167], v[140:143]\n"
  "s_waitcnt lgkmcnt(9)\n"
  "ds_read_b128 v[224:227], v0 offset:45312\n"
  "v_pk_add_f16 v213, v213, 1.0 op_sel_hi:[1,0]\n"
  "v_rcp_f16 v208, v212\n"
  "v_mfma_f32_16x16x32_f16 v[96:99], v[228:231], v[184:187], v[96:99]\n"
  "v_rcp_f16 v209, v213\n"
  "v_mfma_f32_16x16x32_f16 v[120:123], v[228:231], v[188:191], v[120:123]\n"
  "v_rcp_f16_sdwa v208, v212 dst_sel:WORD_1 dst_unused:UNUSED_PRESERVE src0_sel:WORD_1\n"
  "v_mfma_f32_16x16x32_f16 v[144:147], v[228:231], v[164:167], v[144:147]\n"
  "s_waitcnt lgkmcnt(6)\n"
  "ds_read_b128 v[228:231], v0 offset:47360\n"
  "v_rcp_f16_sdwa v209, v213 dst_sel:WORD_1 dst_unused:UNUSED_PRESERVE src0_sel:WORD_1\n"
  "v_mfma_f32_16x16x32_f16 v[100:103], v[216:219], v[184:187], v[100:103]\n"
  "v_or_b32 v198, v172, v173\n"
  "v_pk_mul_f16 v194, v194, v208\n"
  "v_mfma_f32_16x16x32_f16 v[124:127], v[216:219], v[188:191], v[124:127]\n"
  "v_pk_mul_f16 v195, v195, v209\n"
  "v_or_b32 v199, v174, v175\n"
  "v_mfma_f32_16x16x32_f16 v[148:151], v[216:219], v[164:167], v[148:151]\n"
  "s_waitcnt lgkmcnt(2)\n"
  "ds_read_b128 v[216:219], v2 offset:51200\n"
  "v_or_b32 v182, v176, v177\n"
  "v_or_b32 v183, v178, v179\n"
  "v_mfma_f32_16x16x32_bf16 v[168:171], v[220:223], v[32:35], 0\n"
  "v_lshlrev_b32_sdwa v156, 1, v156 dst_sel:DWORD dst_unused:UNUSED_PAD src0_sel:DWORD src1_sel:WORD_1\n"
  "v_lshlrev_b32_sdwa v157, 1, v157 dst_sel:DWORD dst_unused:UNUSED_PAD src0_sel:DWORD src1_sel:WORD_1\n"
  "v_mfma_f32_16x16x32_bf16 v[172:175], v[220:223], v[36:39], 0\n"
  "v_lshlrev_b32_sdwa v158, 1, v158 dst_sel:DWORD dst_unused:UNUSED_PAD src0_sel:DWORD src1_sel:WORD_1\n"
  "v_mfma_f32_16x16x32_bf16 v[176:179], v[220:223], v[56:59], 0\n"
  "s_waitcnt lgkmcnt(2)\n"
  "ds_read_b128 v[220:223], v2 offset:52224\n"
  "ds_read_u16 v156, v156\n"
  "v_cvt_pk_f16_f32 v184, v152, v153\n"
  "v_cvt_pk_f16_f32 v185, v154, v155\n"
  "s_waitcnt vmcnt(14)\n"
  "v_mfma_f32_16x16x32_bf16 v[104:107], v[248:251], v[200:203], v[104:107]\n"
  "ds_read_u16_d16_hi v157, v157\n"
  "v_pk_fma_f16 v208, v184, v184, s56\n"
  "v_mfma_f32_16x16x32_bf16 v[108:111], v[252:255], v[200:203], v[108:111]\n"
  "v_pk_fma_f16 v209, v185, v185, s56\n"
  "v_lshlrev_b32_sdwa v159, 1, v159 dst_sel:DWORD dst_unused:UNUSED_PAD src0_sel:DWORD src1_sel:WORD_1\n"
  "v_mfma_f32_16x16x32_bf16 v[168:171], v[224:227], v[40:43], v[168:171]\n"
  "s_waitcnt lgkmcnt(4)\n"
  "ds_read_u16 v158, v158\n"
  "v_lshlrev_b32_sdwa v160, 1, v160 dst_sel:DWORD dst_unused:UNUSED_PAD src0_sel:DWORD src1_sel:WORD_1\n"
  "v_mfma_f32_16x16x32_bf16 v[172:175], v[224:227], v[44:47], v[172:175]\n"
  "v_pk_mul_f16 v208, v184, v208\n"
  "v_pk_mul_f16 v209, v185, v209\n"
  "v_mfma_f32_16x16x32_bf16 v[176:179], v[224:227], v[64:67], v[176:179]\n"
  "ds_read_b128 v[224:227], v2 offset:53248\n"
  "ds_read_u16_d16_hi v159, v159\n"
  "v_lshlrev_b32_sdwa v161, 1, v161 dst_sel:DWORD dst_unused:UNUSED_PAD src0_sel:DWORD src1_sel:WORD_1\n"
  "v_lshlrev_b32_sdwa v162, 1, v162 dst_sel:DWORD dst_unused:UNUSED_PAD src0_sel:DWORD src1_sel:WORD_1\n"
  "v_mfma_f32_16x16x32_bf16 v[168:171], v[228:231], v[48:51], v[168:171]\n"
  "ds_read_u16 v160, v160\n"
  "v_exp_f16 v212, v208\n"
  "v_mfma_f32_16x16x32_bf16 v[172:175], v[228:231], v[52:55], v[172:175]\n"
  "v_exp_f16 v213, v209\n"
  "v_mfma_f32_16x16x32_bf16 v[176:179], v[228:231], v[72:75], v[176:179]\n"
  "ds_read_b128 v[228:231], v0 offset:49152\n"
  "ds_read_u16_d16_hi v161, v161\n"
  "s_waitcnt vmcnt(13)\n"
  "v_mfma_f32_16x16x32_bf16 v[112:115], v[248:251], v[204:207], v[112:115]\n"
  "s_waitcnt lgkmcnt(9)\n"
  "ds_read_u16 v162, v162\n"
  "v_lshlrev_b32_sdwa v163, 1, v163 dst_sel:DWORD dst_unused:UNUSED_PAD src0_sel:DWORD src1_sel:WORD_1\n"
  "v_exp_f16_sdwa v212, v208 dst_sel:WORD_1 dst_unused:UNUSED_PRESERVE src0_sel:WORD_1\n"
  "v_mfma_f32_16x16x32_bf16 v[116:119], v[252:255], v[204:207], v[116:119]\n"
  "v_exp_f16_sdwa v213, v209 dst_sel:WORD_1 dst_unused:UNUSED_PRESERVE src0_sel:WORD_1\n"
  "v_mfma_f32_16x16x32_f16 v[80:83], v[216:219], v[192:195], v[80:83]\n"
  "ds_read_u16_d16_hi v163, v163\n"
  "v_mfma_f32_16x16x32_f16 v[104:107], v[216:219], v[196:199], v[104:107]\n"
  "s_waitcnt lgkmcnt(10)\n"
  "v_pk_add_f16 v212, v212, 1.0 op_sel_hi:[1,0]\n"
  "v_pk_add_f16 v213, v213, 1.0 op_sel_hi:[1,0]\n"
  "v_mfma_f32_16x16x32_f16 v[128:131], v[216:219], v[180:183], v[128:131]\n"
  "ds_read_b128 v[216:219], v0 offset:51200\n"
  "v_rcp_f16 v208, v212\n"
  "v_mfma_f32_16x16x32_f16 v[84:87], v[220:223], v[192:195], v[84:87]\n"
  "v_rcp_f16 v209, v213\n"
  "v_mfma_f32_16x16x32_f16 v[108:111], v[220:223], v[196:199], v[108:111]\n"
  "v_mfma_f32_16x16x32_f16 v[132:135], v[220:223], v[180:183], v[132:135]\n"
  "ds_read_b128 v[220:223], v0 offset:53248\n"
  "v_rcp_f16_sdwa v208, v212 dst_sel:WORD_1 dst_unused:UNUSED_PRESERVE src0_sel:WORD_1\n"
  "s_waitcnt vmcnt(12)\n"
  "v_mfma_f32_16x16x32_bf16 v[120:123], v[248:251], v[232:235], v[120:123]\n"
  "v_rcp_f16_sdwa v209, v213 dst_sel:WORD_1 dst_unused:UNUSED_PRESERVE src0_sel:WORD_1\n"
  "v_mfma_f32_16x16x32_bf16 v[124:127], v[252:255], v[232:235], v[124:127]\n"
  "s_waitcnt lgkmcnt(7)\n"
  "v_or_b32 v188, v156, v157\n"
  "v_mfma_f32_16x16x32_f16 v[88:91], v[224:227], v[192:195], v[88:91]\n"
  "v_pk_mul_f16 v184, v184, v208\n"
  "v_pk_mul_f16 v185, v185, v209\n"
  "v_mfma_f32_16x16x32_f16 v[112:115], v[224:227], v[196:199], v[112:115]\n"
  "v_or_b32 v189, v158, v159\n"
  "v_mfma_f32_16x16x32_f16 v[136:139], v[224:227], v[180:183], v[136:139]\n"
  "s_waitcnt lgkmcnt(2)\n"
  "ds_read_b128 v[224:227], v2 offset:54272\n"
  "v_or_b32 v164, v160, v161\n"
  "v_or_b32 v165, v162, v163\n"
  "v_mfma_f32_16x16x32_bf16 v[152:155], v[228:231], v[32:35], 0\n"
  "v_lshlrev_b32_sdwa v172, 1, v172 dst_sel:DWORD dst_unused:UNUSED_PAD src0_sel:DWORD src1_sel:WORD_1\n"
  "v_lshlrev_b32_sdwa v173, 1, v173 dst_sel:DWORD dst_unused:UNUSED_PAD src0_sel:DWORD src1_sel:WORD_1\n"
  "v_mfma_f32_16x16x32_bf16 v[156:159], v[228:231], v[36:39], 0\n"
  "ds_read_u16 v172, v172\n"
  "v_lshlrev_b32_sdwa v174, 1, v174 dst_sel:DWORD dst_unused:UNUSED_PAD src0_sel:DWORD src1_sel:WORD_1\n"
  "v_cvt_pk_f16_f32 v186, v168, v169\n"
  "v_mfma_f32_16x16x32_bf16 v[160:163], v[228:231], v[56:59], 0\n"
  "s_waitcnt lgkmcnt(3)\n"
  "ds_read_b128 v[228:231], v2 offset:55296\n"
  "v_cvt_pk_f16_f32 v187, v170, v171\n"
  "v_pk_fma_f16 v208, v186, v186, s56\n"
  "v_mfma_f32_16x16x32_bf16 v[152:155], v[216:219], v[40:43], v[152:155]\n"
  "ds_read_u16_d16_hi v173, v173\n"
  "v_pk_fma_f16 v209, v187, v187, s56\n"
  "v_lshlrev_b32_sdwa v175, 1, v175 dst_sel:DWORD dst_unused:UNUSED_PAD src0_sel:DWORD src1_sel:WORD_1\n"
  "v_mfma_f32_16x16x32_bf16 v[156:159], v[216:219], v[44:47], v[156:159]\n"
  "ds_read_u16 v174, v174\n"
  "v_lshlrev_b32_sdwa v176, 1, v176 dst_sel:DWORD dst_unused:UNUSED_PAD src0_sel:DWORD src1_sel:WORD_1\n"
  "v_pk_mul_f16 v208, v186, v208\n"
  "v_pk_mul_f16 v209, v187, v209\n"
  "v_mfma_f32_16x16x32_bf16 v[160:163], v[216:219], v[64:67], v[160:163]\n"
  "s_waitcnt lgkmcnt(5)\n"
  "ds_read_b128 v[216:219], v2 offset:56320\n"
  "ds_read_u16_d16_hi v175, v175\n"
  "v_lshlrev_b32_sdwa v177, 1, v177 dst_sel:DWORD dst_unused:UNUSED_PAD src0_sel:DWORD src1_sel:WORD_1\n"
  "v_lshlrev_b32_sdwa v178, 1, v178 dst_sel:DWORD dst_unused:UNUSED_PAD src0_sel:DWORD src1_sel:WORD_1\n"
  "v_mfma_f32_16x16x32_bf16 v[152:155], v[220:223], v[48:51], v[152:155]\n"
  "ds_read_u16 v176, v176\n"
  "v_exp_f16 v212, v208\n"
  "v_mfma_f32_16x16x32_bf16 v[156:159], v[220:223], v[52:55], v[156:159]\n"
  "ds_read_u16_d16_hi v177, v177\n"
  "v_exp_f16 v213, v209\n"
  "v_mfma_f32_16x16x32_bf16 v[160:163], v[220:223], v[72:75], v[160:163]\n"
  "s_waitcnt lgkmcnt(8)\n"
  "ds_read_b128 v[220:223], v0 offset:49408\n"
  "v_lshlrev_b32_sdwa v179, 1, v179 dst_sel:DWORD dst_unused:UNUSED_PAD src0_sel:DWORD src1_sel:WORD_1\n"
  "v_exp_f16_sdwa v212, v208 dst_sel:WORD_1 dst_unused:UNUSED_PRESERVE src0_sel:WORD_1\n"
  "v_mfma_f32_16x16x32_f16 v[92:95], v[224:227], v[192:195], v[92:95]\n"
  "ds_read_u16 v178, v178\n"
  "v_exp_f16_sdwa v213, v209 dst_sel:WORD_1 dst_unused:UNUSED_PRESERVE src0_sel:WORD_1\n"
  "v_mfma_f32_16x16x32_f16 v[116:119], v[224:227], v[196:199], v[116:119]\n"
  "ds_read_u16_d16_hi v179, v179\n"
  "v_pk_add_f16 v212, v212, 1.0 op_sel_hi:[1,0]\n"
  "v_mfma_f32_16x16x32_f16 v[140:143], v[224:227], v[180:183], v[140:143]\n"
  "s_waitcnt lgkmcnt(9)\n"
  "ds_read_b128 v[224:227], v0 offset:51456\n"
  "v_pk_add_f16 v213, v213, 1.0 op_sel_hi:[1,0]\n"
  "v_rcp_f16 v208, v212\n"
  "v_mfma_f32_16x16x32_f16 v[96:99], v[228:231], v[192:195], v[96:99]\n"
  "v_rcp_f16 v209, v213\n"
  "v_mfma_f32_16x16x32_f16 v[120:123], v[228:231], v[196:199], v[120:123]\n"
  "v_rcp_f16_sdwa v208, v212 dst_sel:WORD_1 dst_unused:UNUSED_PRESERVE src0_sel:WORD_1\n"
  "v_mfma_f32_16x16x32_f16 v[144:147], v[228:231], v[180:183], v[144:147]\n"
  "s_waitcnt lgkmcnt(6)\n"
  "ds_read_b128 v[228:231], v0 offset:53504\n"
  "v_rcp_f16_sdwa v209, v213 dst_sel:WORD_1 dst_unused:UNUSED_PRESERVE src0_sel:WORD_1\n"
  "v_mfma_f32_16x16x32_f16 v[100:103], v[216:219], v[192:195], v[100:103]\n"
  "v_or_b32 v190, v172, v173\n"
  "v_pk_mul_f16 v186, v186, v208\n"
  "v_mfma_f32_16x16x32_f16 v[124:127], v[216:219], v[196:199], v[124:127]\n"
  "v_pk_mul_f16 v187, v187, v209\n"
  "v_or_b32 v191, v174, v175\n"
  "v_mfma_f32_16x16x32_f16 v[148:151], v[216:219], v[180:183], v[148:151]\n"
  "s_waitcnt lgkmcnt(2)\n"
  "ds_read_b128 v[216:219], v2 offset:57344\n"
  "v_or_b32 v166, v176, v177\n"
  "v_or_b32 v167, v178, v179\n"
  "v_mfma_f32_16x16x32_bf16 v[168:171], v[220:223], v[32:35], 0\n"
  "v_lshlrev_b32_sdwa v156, 1, v156 dst_sel:DWORD dst_unused:UNUSED_PAD src0_sel:DWORD src1_sel:WORD_1\n"
  "v_lshlrev_b32_sdwa v157, 1, v157 dst_sel:DWORD dst_unused:UNUSED_PAD src0_sel:DWORD src1_sel:WORD_1\n"
  "v_mfma_f32_16x16x32_bf16 v[172:175], v[220:223], v[36:39], 0\n"
  "ds_read_u16 v156, v156\n"
  "v_lshlrev_b32_sdwa v158, 1, v158 dst_sel:DWORD dst_unused:UNUSED_PAD src0_sel:DWORD src1_sel:WORD_1\n"
  "v_cvt_pk_f16_f32 v192, v152, v153\n"
  "v_mfma_f32_16x16x32_bf16 v[176:179], v[220:223], v[56:59], 0\n"
  "s_waitcnt lgkmcnt(3)\n"
  "ds_read_b128 v[220:223], v2 offset:58368\n"
  "v_cvt_pk_f16_f32 v193, v154, v155\n"
  "v_pk_fma_f16 v208, v192, v192, s56\n"
  "v_mfma_f32_16x16x32_bf16 v[168:171], v[224:227], v[40:43], v[168:171]\n"
  "ds_read_u16_d16_hi v157, v157\n"
  "v_pk_fma_f16 v209, v193, v193, s56\n"
  "v_lshlrev_b32_sdwa v159, 1, v159 dst_sel:DWORD dst_unused:UNUSED_PAD src0_sel:DWORD src1_sel:WORD_1\n"
  "v_mfma_f32_16x16x32_bf16 v[172:175], v[224:227], v[44:47], v[172:175]\n"
  "ds_read_u16 v158, v158\n"
  "v_lshlrev_b32_sdwa v160, 1, v160 dst_sel:DWORD dst_unused:UNUSED_PAD src0_sel:DWORD src1_sel:WORD_1\n"
  "v_pk_mul_f16 v208, v192, v208\n"
  "v_pk_mul_f16 v209, v193, v209\n"
  "v_mfma_f32_16x16x32_bf16 v[176:179], v[224:227], v[64:67], v[176:179]\n"
  "s_waitcnt lgkmcnt(5)\n"
  "ds_read_b128 v[224:227], v2 offset:59392\n"
  "ds_read_u16_d16_hi v159, v159\n"
  "v_lshlrev_b32_sdwa v161, 1, v161 dst_sel:DWORD dst_unused:UNUSED_PAD src0_sel:DWORD src1_sel:WORD_1\n"
  "v_lshlrev_b32_sdwa v162, 1, v162 dst_sel:DWORD dst_unused:UNUSED_PAD src0_sel:DWORD src1_sel:WORD_1\n"
  "v_mfma_f32_16x16x32_bf16 v[168:171], v[228:231], v[48:51], v[168:171]\n"
  "ds_read_u16 v160, v160\n"
  "v_exp_f16 v212, v208\n"
  "v_mfma_f32_16x16x32_bf16 v[172:175], v[228:231], v[52:55], v[172:175]\n"
  "ds_read_u16_d16_hi v161, v161\n"
  "v_exp_f16 v213, v209\n"
  "v_mfma_f32_16x16x32_bf16 v[176:179], v[228:231], v[72:75], v[176:179]\n"
  "s_waitcnt lgkmcnt(8)\n"
  "ds_read_b128 v[228:231], v0 offset:55296\n"
  "v_lshlrev_b32_sdwa v163, 1, v163 dst_sel:DWORD dst_unused:UNUSED_PAD src0_sel:DWORD src1_sel:WORD_1\n"
  "v_exp_f16_sdwa v212, v208 dst_sel:WORD_1 dst_unused:UNUSED_PRESERVE src0_sel:WORD_1\n"
  "v_mfma_f32_16x16x32_f16 v[80:83], v[216:219], v[184:187], v[80:83]\n"
  "ds_read_u16 v162, v162\n"
  "v_exp_f16_sdwa v213, v209 dst_sel:WORD_1 dst_unused:UNUSED_PRESERVE src0_sel:WORD_1\n"
  "v_mfma_f32_16x16x32_f16 v[104:107], v[216:219], v[188:191], v[104:107]\n"
  "ds_read_u16_d16_hi v163, v163\n"
  "v_pk_add_f16 v212, v212, 1.0 op_sel_hi:[1,0]\n"
  "v_mfma_f32_16x16x32_f16 v[128:131], v[216:219], v[164:167], v[128:131]\n"
  "s_waitcnt lgkmcnt(9)\n"
  "ds_read_b128 v[216:219], v0 offset:57344\n"
  "v_pk_add_f16 v213, v213, 1.0 op_sel_hi:[1,0]\n"
  "v_rcp_f16 v208, v212\n"
  "v_mfma_f32_16x16x32_f16 v[84:87], v[220:223], v[184:187], v[84:87]\n"
  "v_rcp_f16 v209, v213\n"
  "v_mfma_f32_16x16x32_f16 v[108:111], v[220:223], v[188:191], v[108:111]\n"
  "v_rcp_f16_sdwa v208, v212 dst_sel:WORD_1 dst_unused:UNUSED_PRESERVE src0_sel:WORD_1\n"
  "v_mfma_f32_16x16x32_f16 v[132:135], v[220:223], v[164:167], v[132:135]\n"
  "s_waitcnt lgkmcnt(6)\n"
  "ds_read_b128 v[220:223], v0 offset:59392\n"
  "v_rcp_f16_sdwa v209, v213 dst_sel:WORD_1 dst_unused:UNUSED_PRESERVE src0_sel:WORD_1\n"
  "v_mfma_f32_16x16x32_f16 v[88:91], v[224:227], v[184:187], v[88:91]\n"
  "v_or_b32 v196, v156, v157\n"
  "v_pk_mul_f16 v192, v192, v208\n"
  "v_mfma_f32_16x16x32_f16 v[112:115], v[224:227], v[188:191], v[112:115]\n"
  "v_pk_mul_f16 v193, v193, v209\n"
  "v_or_b32 v197, v158, v159\n"
  "v_mfma_f32_16x16x32_f16 v[136:139], v[224:227], v[164:167], v[136:139]\n"
  "s_waitcnt lgkmcnt(2)\n"
  "ds_read_b128 v[224:227], v2 offset:60416\n"
  "v_or_b32 v180, v160, v161\n"
  "v_or_b32 v181, v162, v163\n"
  "v_mfma_f32_16x16x32_bf16 v[152:155], v[228:231], v[32:35], 0\n"
  "v_lshlrev_b32_sdwa v172, 1, v172 dst_sel:DWORD dst_unused:UNUSED_PAD src0_sel:DWORD src1_sel:WORD_1\n"
  "v_lshlrev_b32_sdwa v173, 1, v173 dst_sel:DWORD dst_unused:UNUSED_PAD src0_sel:DWORD src1_sel:WORD_1\n"
  "v_mfma_f32_16x16x32_bf16 v[156:159], v[228:231], v[36:39], 0\n"
  "ds_read_u16 v172, v172\n"
  "v_lshlrev_b32_sdwa v174, 1, v174 dst_sel:DWORD dst_unused:UNUSED_PAD src0_sel:DWORD src1_sel:WORD_1\n"
  "v_cvt_pk_f16_f32 v194, v168, v169\n"
  "v_mfma_f32_16x16x32_bf16 v[160:163], v[228:231], v[56:59], 0\n"
  "s_waitcnt lgkmcnt(3)\n"
  "ds_read_b128 v[228:231], v2 offset:61440\n"
  "v_cvt_pk_f16_f32 v195, v170, v171\n"
  "v_pk_fma_f16 v208, v194, v194, s56\n"
  "v_mfma_f32_16x16x32_bf16 v[152:155], v[216:219], v[40:43], v[152:155]\n"
  "ds_read_u16_d16_hi v173, v173\n"
  "v_pk_fma_f16 v209, v195, v195, s56\n"
  "v_lshlrev_b32_sdwa v175, 1, v175 dst_sel:DWORD dst_unused:UNUSED_PAD src0_sel:DWORD src1_sel:WORD_1\n"
  "v_mfma_f32_16x16x32_bf16 v[156:159], v[216:219], v[44:47], v[156:159]\n"
  "ds_read_u16 v174, v174\n"
  "v_lshlrev_b32_sdwa v176, 1, v176 dst_sel:DWORD dst_unused:UNUSED_PAD src0_sel:DWORD src1_sel:WORD_1\n"
  "v_pk_mul_f16 v208, v194, v208\n"
  "v_pk_mul_f16 v209, v195, v209\n"
  "v_mfma_f32_16x16x32_bf16 v[160:163], v[216:219], v[64:67], v[160:163]\n"
  "s_waitcnt lgkmcnt(5)\n"
  "ds_read_b128 v[216:219], v2 offset:62464\n"
  "ds_read_u16_d16_hi v175, v175\n"
  "v_lshlrev_b32_sdwa v177, 1, v177 dst_sel:DWORD dst_unused:UNUSED_PAD src0_sel:DWORD src1_sel:WORD_1\n"
  "v_lshlrev_b32_sdwa v178, 1, v178 dst_sel:DWORD dst_unused:UNUSED_PAD src0_sel:DWORD src1_sel:WORD_1\n"
  "v_mfma_f32_16x16x32_bf16 v[152:155], v[220:223], v[48:51], v[152:155]\n"
  "ds_read_u16 v176, v176\n"
  "v_exp_f16 v212, v208\n"
  "v_mfma_f32_16x16x32_bf16 v[156:159], v[220:223], v[52:55], v[156:159]\n"
  "ds_read_u16_d16_hi v177, v177\n"
  "v_exp_f16 v213, v209\n"
  "v_mfma_f32_16x16x32_bf16 v[160:163], v[220:223], v[72:75], v[160:163]\n"
  "s_waitcnt lgkmcnt(8)\n"
  "ds_read_b128 v[220:223], v0 offset:55552\n"
  "v_lshlrev_b32_sdwa v179, 1, v179 dst_sel:DWORD dst_unused:UNUSED_PAD src0_sel:DWORD src1_sel:WORD_1\n"
  "v_exp_f16_sdwa v212, v208 dst_sel:WORD_1 dst_unused:UNUSED_PRESERVE src0_sel:WORD_1\n"
  "v_mfma_f32_16x16x32_f16 v[92:95], v[224:227], v[184:187], v[92:95]\n"
  "ds_read_u16 v178, v178\n"
  "v_exp_f16_sdwa v213, v209 dst_sel:WORD_1 dst_unused:UNUSED_PRESERVE src0_sel:WORD_1\n"
  "v_mfma_f32_16x16x32_f16 v[116:119], v[224:227], v[188:191], v[116:119]\n"
  "ds_read_u16_d16_hi v179, v179\n"
  "v_pk_add_f16 v212, v212, 1.0 op_sel_hi:[1,0]\n"
  "v_mfma_f32_16x16x32_f16 v[140:143], v[224:227], v[164:167], v[140:143]\n"
  "s_waitcnt lgkmcnt(9)\n"
  "ds_read_b128 v[224:227], v0 offset:57600\n"
  "v_pk_add_f16 v213, v213, 1.0 op_sel_hi:[1,0]\n"
  "v_rcp_f16 v208, v212\n"
  "v_mfma_f32_16x16x32_f16 v[96:99], v[228:231], v[184:187], v[96:99]\n"
  "v_rcp_f16 v209, v213\n"
  "v_mfma_f32_16x16x32_f16 v[120:123], v[228:231], v[188:191], v[120:123]\n"
  "v_rcp_f16_sdwa v208, v212 dst_sel:WORD_1 dst_unused:UNUSED_PRESERVE src0_sel:WORD_1\n"
  "v_mfma_f32_16x16x32_f16 v[144:147], v[228:231], v[164:167], v[144:147]\n"
  "s_waitcnt lgkmcnt(6)\n"
  "ds_read_b128 v[228:231], v0 offset:59648\n"
  "v_rcp_f16_sdwa v209, v213 dst_sel:WORD_1 dst_unused:UNUSED_PRESERVE src0_sel:WORD_1\n"
  "v_mfma_f32_16x16x32_f16 v[100:103], v[216:219], v[184:187], v[100:103]\n"
  "v_or_b32 v198, v172, v173\n"
  "v_pk_mul_f16 v194, v194, v208\n"
  "v_mfma_f32_16x16x32_f16 v[124:127], v[216:219], v[188:191], v[124:127]\n"
  "v_pk_mul_f16 v195, v195, v209\n"
  "v_or_b32 v199, v174, v175\n"
  "v_mfma_f32_16x16x32_f16 v[148:151], v[216:219], v[164:167], v[148:151]\n"
  "s_waitcnt lgkmcnt(2)\n"
  "ds_read_b128 v[216:219], v2 offset:63488\n"
  "v_or_b32 v182, v176, v177\n"
  "v_or_b32 v183, v178, v179\n"
  "s_setprio 0\n"
  "v_mfma_f32_16x16x32_bf16 v[168:171], v[220:223], v[32:35], 0\n"
  "v_lshlrev_b32_sdwa v156, 1, v156 dst_sel:DWORD dst_unused:UNUSED_PAD src0_sel:DWORD src1_sel:WORD_1\n"
  "v_lshlrev_b32_sdwa v157, 1, v157 dst_sel:DWORD dst_unused:UNUSED_PAD src0_sel:DWORD src1_sel:WORD_1\n"
  "v_mfma_f32_16x16x32_bf16 v[172:175], v[220:223], v[36:39], 0\n"
  "ds_read_u16 v156, v156\n"
  "v_lshlrev_b32_sdwa v158, 1, v158 dst_sel:DWORD dst_unused:UNUSED_PAD src0_sel:DWORD src1_sel:WORD_1\n"
  "v_cvt_pk_f16_f32 v184, v152, v153\n"
  "v_mfma_f32_16x16x32_bf16 v[176:179], v[220:223], v[56:59], 0\n"
  "s_waitcnt lgkmcnt(3)\n"
  "ds_read_b128 v[220:223], v2 offset:64512\n"
  "v_cvt_pk_f16_f32 v185, v154, v155\n"
  "v_pk_fma_f16 v208, v184, v184, s56\n"
  "v_mfma_f32_16x16x32_bf16 v[168:171], v[224:227], v[40:43], v[168:171]\n"
  "ds_read_u16_d16_hi v157, v157\n"
  "v_pk_fma_f16 v209, v185, v185, s56\n"
  "v_lshlrev_b32_sdwa v159, 1, v159 dst_sel:DWORD dst_unused:UNUSED_PAD src0_sel:DWORD src1_sel:WORD_1\n"
  "v_mfma_f32_16x16x32_bf16 v[172:175], v[224:227], v[44:47], v[172:175]\n"
  "ds_read_u16 v158, v158\n"
  "v_lshlrev_b32_sdwa v160, 1, v160 dst_sel:DWORD dst_unused:UNUSED_PAD src0_sel:DWORD src1_sel:WORD_1\n"
  "v_pk_mul_f16 v208, v184, v208\n"
  "v_pk_mul_f16 v209, v185, v209\n"
  "v_mfma_f32_16x16x32_bf16 v[176:179], v[224:227], v[64:67], v[176:179]\n"
  "s_waitcnt lgkmcnt(5)\n"
  "ds_read_b128 v[224:227], v3 offset:0\n"
  "ds_read_u16_d16_hi v159, v159\n"
  "v_lshlrev_b32_sdwa v161, 1, v161 dst_sel:DWORD dst_unused:UNUSED_PAD src0_sel:DWORD src1_sel:WORD_1\n"
  "v_lshlrev_b32_sdwa v162, 1, v162 dst_sel:DWORD dst_unused:UNUSED_PAD src0_sel:DWORD src1_sel:WORD_1\n"
  "v_mfma_f32_16x16x32_bf16 v[168:171], v[228:231], v[48:51], v[168:171]\n"
  "ds_read_u16 v160, v160\n"
  "v_exp_f16 v212, v208\n"
  "v_mfma_f32_16x16x32_bf16 v[172:175], v[228:231], v[52:55], v[172:175]\n"
  "ds_read_u16_d16_hi v161, v161\n"
  "v_exp_f16 v213, v209\n"
  "v_mfma_f32_16x16x32_bf16 v[176:179], v[228:231], v[72:75], v[176:179]\n"
  "s_waitcnt lgkmcnt(8)\n"
  "ds_read_b128 v[228:231], v0 offset:61440\n"
  "v_lshlrev_b32_sdwa v163, 1, v163 dst_sel:DWORD dst_unused:UNUSED_PAD src0_sel:DWORD src1_sel:WORD_1\n"
  "v_exp_f16_sdwa v212, v208 dst_sel:WORD_1 dst_unused:UNUSED_PRESERVE src0_sel:WORD_1\n"
  "v_mfma_f32_16x16x32_f16 v[80:83], v[216:219], v[192:195], v[80:83]\n"
  "ds_read_u16 v162, v162\n"
  "v_exp_f16_sdwa v213, v209 dst_sel:WORD_1 dst_unused:UNUSED_PRESERVE src0_sel:WORD_1\n"
  "v_mfma_f32_16x16x32_f16 v[104:107], v[216:219], v[196:199], v[104:107]\n"
  "ds_read_u16_d16_hi v163, v163\n"
  "v_pk_add_f16 v212, v212, 1.0 op_sel_hi:[1,0]\n"
  "v_mfma_f32_16x16x32_f16 v[128:131], v[216:219], v[180:183], v[128:131]\n"
  "s_waitcnt lgkmcnt(9)\n"
  "ds_read_b128 v[216:219], v0 offset:63488\n"
  "v_pk_add_f16 v213, v213, 1.0 op_sel_hi:[1,0]\n"
  "v_rcp_f16 v208, v212\n"
  "v_mfma_f32_16x16x32_f16 v[84:87], v[220:223], v[192:195], v[84:87]\n"
  "v_rcp_f16 v209, v213\n"
  "v_mfma_f32_16x16x32_f16 v[108:111], v[220:223], v[196:199], v[108:111]\n"
  "v_rcp_f16_sdwa v208, v212 dst_sel:WORD_1 dst_unused:UNUSED_PRESERVE src0_sel:WORD_1\n"
  "v_mfma_f32_16x16x32_f16 v[132:135], v[220:223], v[180:183], v[132:135]\n"
  "s_waitcnt lgkmcnt(6)\n"
  "ds_read_b128 v[220:223], v1 offset:6144\n"
  "v_rcp_f16_sdwa v209, v213 dst_sel:WORD_1 dst_unused:UNUSED_PRESERVE src0_sel:WORD_1\n"
  "v_mfma_f32_16x16x32_f16 v[88:91], v[224:227], v[192:195], v[88:91]\n"
  "v_or_b32 v188, v156, v157\n"
  "v_pk_mul_f16 v184, v184, v208\n"
  "v_mfma_f32_16x16x32_f16 v[112:115], v[224:227], v[196:199], v[112:115]\n"
  "v_pk_mul_f16 v185, v185, v209\n"
  "v_or_b32 v189, v158, v159\n"
  "v_mfma_f32_16x16x32_f16 v[136:139], v[224:227], v[180:183], v[136:139]\n"
  "s_waitcnt lgkmcnt(2)\n"
  "ds_read_b128 v[224:227], v3 offset:1024\n"
  "v_or_b32 v164, v160, v161\n"
  "v_or_b32 v165, v162, v163\n"
  "v_mfma_f32_16x16x32_bf16 v[152:155], v[228:231], v[32:35], 0\n"
  "v_lshlrev_b32_sdwa v172, 1, v172 dst_sel:DWORD dst_unused:UNUSED_PAD src0_sel:DWORD src1_sel:WORD_1\n"
  "v_lshlrev_b32_sdwa v173, 1, v173 dst_sel:DWORD dst_unused:UNUSED_PAD src0_sel:DWORD src1_sel:WORD_1\n"
  "v_mfma_f32_16x16x32_bf16 v[156:159], v[228:231], v[36:39], 0\n"
  "ds_read_u16 v172, v172\n"
  "v_lshlrev_b32_sdwa v174, 1, v174 dst_sel:DWORD dst_unused:UNUSED_PAD src0_sel:DWORD src1_sel:WORD_1\n"
  "v_cvt_pk_f16_f32 v186, v168, v169\n"
  "v_mfma_f32_16x16x32_bf16 v[160:163], v[228:231], v[56:59], 0\n"
  "s_waitcnt lgkmcnt(3)\n"
  "ds_read_b128 v[228:231], v3 offset:2048\n"
  "v_cvt_pk_f16_f32 v187, v170, v171\n"
  "v_pk_fma_f16 v208, v186, v186, s56\n"
  "v_mfma_f32_16x16x32_bf16 v[152:155], v[216:219], v[40:43], v[152:155]\n"
  "ds_read_u16_d16_hi v173, v173\n"
  "v_pk_fma_f16 v209, v187, v187, s56\n"
  "v_lshlrev_b32_sdwa v175, 1, v175 dst_sel:DWORD dst_unused:UNUSED_PAD src0_sel:DWORD src1_sel:WORD_1\n"
  "v_mfma_f32_16x16x32_bf16 v[156:159], v[216:219], v[44:47], v[156:159]\n"
  "ds_read_u16 v174, v174\n"
  "v_lshlrev_b32_sdwa v176, 1, v176 dst_sel:DWORD dst_unused:UNUSED_PAD src0_sel:DWORD src1_sel:WORD_1\n"
  "v_pk_mul_f16 v208, v186, v208\n"
  "v_pk_mul_f16 v209, v187, v209\n"
  "v_mfma_f32_16x16x32_bf16 v[160:163], v[216:219], v[64:67], v[160:163]\n"
  "s_waitcnt lgkmcnt(5)\n"
  "ds_read_b128 v[216:219], v3 offset:3072\n"
  "ds_read_u16_d16_hi v175, v175\n"
  "v_lshlrev_b32_sdwa v177, 1, v177 dst_sel:DWORD dst_unused:UNUSED_PAD src0_sel:DWORD src1_sel:WORD_1\n"
  "v_lshlrev_b32_sdwa v178, 1, v178 dst_sel:DWORD dst_unused:UNUSED_PAD src0_sel:DWORD src1_sel:WORD_1\n"
  "v_mfma_f32_16x16x32_bf16 v[152:155], v[220:223], v[48:51], v[152:155]\n"
  "ds_read_u16 v176, v176\n"
  "v_exp_f16 v212, v208\n"
  "v_mfma_f32_16x16x32_bf16 v[156:159], v[220:223], v[52:55], v[156:159]\n"
  "ds_read_u16_d16_hi v177, v177\n"
  "v_exp_f16 v213, v209\n"
  "v_mfma_f32_16x16x32_bf16 v[160:163], v[220:223], v[72:75], v[160:163]\n"
  "s_waitcnt lgkmcnt(8)\n"
  "ds_read_b128 v[220:223], v0 offset:61696\n"
  "v_lshlrev_b32_sdwa v179, 1, v179 dst_sel:DWORD dst_unused:UNUSED_PAD src0_sel:DWORD src1_sel:WORD_1\n"
  "v_exp_f16_sdwa v212, v208 dst_sel:WORD_1 dst_unused:UNUSED_PRESERVE src0_sel:WORD_1\n"
  "v_mfma_f32_16x16x32_f16 v[92:95], v[224:227], v[192:195], v[92:95]\n"
  "ds_read_u16 v178, v178\n"
  "v_exp_f16_sdwa v213, v209 dst_sel:WORD_1 dst_unused:UNUSED_PRESERVE src0_sel:WORD_1\n"
  "v_mfma_f32_16x16x32_f16 v[116:119], v[224:227], v[196:199], v[116:119]\n"
  "ds_read_u16_d16_hi v179, v179\n"
  "v_pk_add_f16 v212, v212, 1.0 op_sel_hi:[1,0]\n"
  "v_mfma_f32_16x16x32_f16 v[140:143], v[224:227], v[180:183], v[140:143]\n"
  "s_waitcnt lgkmcnt(9)\n"
  "ds_read_b128 v[224:227], v0 offset:63744\n"
  "v_pk_add_f16 v213, v213, 1.0 op_sel_hi:[1,0]\n"
  "v_rcp_f16 v208, v212\n"
  "v_mfma_f32_16x16x32_f16 v[96:99], v[228:231], v[192:195], v[96:99]\n"
  "v_rcp_f16 v209, v213\n"
  "v_mfma_f32_16x16x32_f16 v[120:123], v[228:231], v[196:199], v[120:123]\n"
  "v_rcp_f16_sdwa v208, v212 dst_sel:WORD_1 dst_unused:UNUSED_PRESERVE src0_sel:WORD_1\n"
  "v_mfma_f32_16x16x32_f16 v[144:147], v[228:231], v[180:183], v[144:147]\n"
  "s_waitcnt lgkmcnt(6)\n"
  "ds_read_b128 v[228:231], v1 offset:6400\n"
  "v_rcp_f16_sdwa v209, v213 dst_sel:WORD_1 dst_unused:UNUSED_PRESERVE src0_sel:WORD_1\n"
  "v_mfma_f32_16x16x32_f16 v[100:103], v[216:219], v[192:195], v[100:103]\n"
  "v_or_b32 v190, v172, v173\n"
  "v_pk_mul_f16 v186, v186, v208\n"
  "v_mfma_f32_16x16x32_f16 v[124:127], v[216:219], v[196:199], v[124:127]\n"
  "v_pk_mul_f16 v187, v187, v209\n"
  "v_or_b32 v191, v174, v175\n"
  "v_mfma_f32_16x16x32_f16 v[148:151], v[216:219], v[180:183], v[148:151]\n"
  "s_waitcnt lgkmcnt(2)\n"
  "ds_read_b128 v[216:219], v3 offset:4096\n"
  "v_or_b32 v166, v176, v177\n"
  "v_or_b32 v167, v178, v179\n"
  "v_mfma_f32_16x16x32_bf16 v[168:171], v[220:223], v[32:35], 0\n"
  "v_lshlrev_b32_sdwa v156, 1, v156 dst_sel:DWORD dst_unused:UNUSED_PAD src0_sel:DWORD src1_sel:WORD_1\n"
  "v_lshlrev_b32_sdwa v157, 1, v157 dst_sel:DWORD dst_unused:UNUSED_PAD src0_sel:DWORD src1_sel:WORD_1\n"
  "v_mfma_f32_16x16x32_bf16 v[172:175], v[220:223], v[36:39], 0\n"
  "v_lshlrev_b32_sdwa v158, 1, v158 dst_sel:DWORD dst_unused:UNUSED_PAD src0_sel:DWORD src1_sel:WORD_1\n"
  "v_mfma_f32_16x16x32_bf16 v[176:179], v[220:223], v[56:59], 0\n"
  "s_waitcnt lgkmcnt(2)\n"
  "ds_read_b128 v[220:223], v3 offset:5120\n"
  "ds_read_u16 v156, v156\n"
  "v_cvt_pk_f16_f32 v192, v152, v153\n"
  "v_cvt_pk_f16_f32 v193, v154, v155\n"
  "s_waitcnt vmcnt(2)\n"
  "v_mfma_f32_16x16x32_bf16 v[128:131], v[248:251], v[236:239], v[128:131]\n"
  "ds_read_u16_d16_hi v157, v157\n"
  "v_pk_fma_f16 v208, v192, v192, s56\n"
  "v_mfma_f32_16x16x32_bf16 v[132:135], v[252:255], v[236:239], v[132:135]\n"
  "v_pk_fma_f16 v209, v193, v193, s56\n"
  "v_lshlrev_b32_sdwa v159, 1, v159 dst_sel:DWORD dst_unused:UNUSED_PAD src0_sel:DWORD src1_sel:WORD_1\n"
  "v_mfma_f32_16x16x32_bf16 v[168:171], v[224:227], v[40:43], v[168:171]\n"
  "s_waitcnt lgkmcnt(4)\n"
  "ds_read_u16 v158, v158\n"
  "v_lshlrev_b32_sdwa v160, 1, v160 dst_sel:DWORD dst_unused:UNUSED_PAD src0_sel:DWORD src1_sel:WORD_1\n"
  "v_mfma_f32_16x16x32_bf16 v[172:175], v[224:227], v[44:47], v[172:175]\n"
  "v_pk_mul_f16 v208, v192, v208\n"
  "v_pk_mul_f16 v209, v193, v209\n"
  "v_mfma_f32_16x16x32_bf16 v[176:179], v[224:227], v[64:67], v[176:179]\n"
  "ds_read_b128 v[224:227], v3 offset:6144\n"
  "ds_read_u16_d16_hi v159, v159\n"
  "v_lshlrev_b32_sdwa v161, 1, v161 dst_sel:DWORD dst_unused:UNUSED_PAD src0_sel:DWORD src1_sel:WORD_1\n"
  "v_lshlrev_b32_sdwa v162, 1, v162 dst_sel:DWORD dst_unused:UNUSED_PAD src0_sel:DWORD src1_sel:WORD_1\n"
  "v_mfma_f32_16x16x32_bf16 v[168:171], v[228:231], v[48:51], v[168:171]\n"
  "ds_read_u16 v160, v160\n"
  "v_exp_f16 v212, v208\n"
  "v_mfma_f32_16x16x32_bf16 v[172:175], v[228:231], v[52:55], v[172:175]\n"
  "v_exp_f16 v213, v209\n"
  "v_mfma_f32_16x16x32_bf16 v[176:179], v[228:231], v[72:75], v[176:179]\n"
  "ds_read_b128 v[228:231], v1 offset:8192\n"
  "ds_read_u16_d16_hi v161, v161\n"
  "s_waitcnt vmcnt(1)\n"
  "v_mfma_f32_16x16x32_bf16 v[136:139], v[248:251], v[240:243], v[136:139]\n"
  "s_waitcnt lgkmcnt(9)\n"
  "ds_read_u16 v162, v162\n"
  "v_lshlrev_b32_sdwa v163, 1, v163 dst_sel:DWORD dst_unused:UNUSED_PAD src0_sel:DWORD src1_sel:WORD_1\n"
  "v_exp_f16_sdwa v212, v208 dst_sel:WORD_1 dst_unused:UNUSED_PRESERVE src0_sel:WORD_1\n"
  "v_mfma_f32_16x16x32_bf16 v[140:143], v[252:255], v[240:243], v[140:143]\n"
  "v_exp_f16_sdwa v213, v209 dst_sel:WORD_1 dst_unused:UNUSED_PRESERVE src0_sel:WORD_1\n"
  "v_mfma_f32_16x16x32_f16 v[80:83], v[216:219], v[184:187], v[80:83]\n"
  "ds_read_u16_d16_hi v163, v163\n"
  "v_mfma_f32_16x16x32_f16 v[104:107], v[216:219], v[188:191], v[104:107]\n"
  "s_waitcnt lgkmcnt(10)\n"
  "v_pk_add_f16 v212, v212, 1.0 op_sel_hi:[1,0]\n"
  "v_pk_add_f16 v213, v213, 1.0 op_sel_hi:[1,0]\n"
  "v_mfma_f32_16x16x32_f16 v[128:131], v[216:219], v[164:167], v[128:131]\n"
  "ds_read_b128 v[216:219], v1 offset:10240\n"
  "v_rcp_f16 v208, v212\n"
  "v_mfma_f32_16x16x32_f16 v[84:87], v[220:223], v[184:187], v[84:87]\n"
  "v_rcp_f16 v209, v213\n"
  "v_mfma_f32_16x16x32_f16 v[108:111], v[220:223], v[188:191], v[108:111]\n"
  "v_mfma_f32_16x16x32_f16 v[132:135], v[220:223], v[164:167], v[132:135]\n"
  "ds_read_b128 v[220:223], v1 offset:12288\n"
  "v_rcp_f16_sdwa v208, v212 dst_sel:WORD_1 dst_unused:UNUSED_PRESERVE src0_sel:WORD_1\n"
  "s_waitcnt vmcnt(0)\n"
  "v_mfma_f32_16x16x32_bf16 v[144:147], v[248:251], v[244:247], v[144:147]\n"
  "v_rcp_f16_sdwa v209, v213 dst_sel:WORD_1 dst_unused:UNUSED_PRESERVE src0_sel:WORD_1\n"
  "v_mfma_f32_16x16x32_bf16 v[148:151], v[252:255], v[244:247], v[148:151]\n"
  "s_waitcnt lgkmcnt(7)\n"
  "v_or_b32 v196, v156, v157\n"
  "v_mfma_f32_16x16x32_f16 v[88:91], v[224:227], v[184:187], v[88:91]\n"
  "v_pk_mul_f16 v192, v192, v208\n"
  "v_pk_mul_f16 v193, v193, v209\n"
  "v_mfma_f32_16x16x32_f16 v[112:115], v[224:227], v[188:191], v[112:115]\n"
  "v_or_b32 v197, v158, v159\n"
  "v_mfma_f32_16x16x32_f16 v[136:139], v[224:227], v[164:167], v[136:139]\n"
  "s_waitcnt lgkmcnt(2)\n"
  "ds_read_b128 v[224:227], v3 offset:7168\n"
  "v_or_b32 v180, v160, v161\n"
  "v_or_b32 v181, v162, v163\n"
  "v_mfma_f32_16x16x32_bf16 v[152:155], v[228:231], v[32:35], 0\n"
  "v_lshlrev_b32_sdwa v172, 1, v172 dst_sel:DWORD dst_unused:UNUSED_PAD src0_sel:DWORD src1_sel:WORD_1\n"
  "v_lshlrev_b32_sdwa v173, 1, v173 dst_sel:DWORD dst_unused:UNUSED_PAD src0_sel:DWORD src1_sel:WORD_1\n"
  "v_mfma_f32_16x16x32_bf16 v[156:159], v[228:231], v[36:39], 0\n"
  "ds_read_u16 v172, v172\n"
  "v_lshlrev_b32_sdwa v174, 1, v174 dst_sel:DWORD dst_unused:UNUSED_PAD src0_sel:DWORD src1_sel:WORD_1\n"
  "v_cvt_pk_f16_f32 v194, v168, v169\n"
  "v_mfma_f32_16x16x32_bf16 v[160:163], v[228:231], v[56:59], 0\n"
  "s_waitcnt lgkmcnt(3)\n"
  "ds_read_b128 v[228:231], v3 offset:8192\n"
  "v_cvt_pk_f16_f32 v195, v170, v171\n"
  "v_pk_fma_f16 v208, v194, v194, s56\n"
  "v_mfma_f32_16x16x32_bf16 v[152:155], v[216:219], v[40:43], v[152:155]\n"
  "ds_read_u16_d16_hi v173, v173\n"
  "v_pk_fma_f16 v209, v195, v195, s56\n"
  "v_lshlrev_b32_sdwa v175, 1, v175 dst_sel:DWORD dst_unused:UNUSED_PAD src0_sel:DWORD src1_sel:WORD_1\n"
  "v_mfma_f32_16x16x32_bf16 v[156:159], v[216:219], v[44:47], v[156:159]\n"
  "ds_read_u16 v174, v174\n"
  "v_lshlrev_b32_sdwa v176, 1, v176 dst_sel:DWORD dst_unused:UNUSED_PAD src0_sel:DWORD src1_sel:WORD_1\n"
  "v_pk_mul_f16 v208, v194, v208\n"
  "v_pk_mul_f16 v209, v195, v209\n"
  "v_mfma_f32_16x16x32_bf16 v[160:163], v[216:219], v[64:67], v[160:163]\n"
  "s_waitcnt lgkmcnt(5)\n"
  "ds_read_b128 v[216:219], v3 offset:9216\n"
  "ds_read_u16_d16_hi v175, v175\n"
  "v_lshlrev_b32_sdwa v177, 1, v177 dst_sel:DWORD dst_unused:UNUSED_PAD src0_sel:DWORD src1_sel:WORD_1\n"
  "v_lshlrev_b32_sdwa v178, 1, v178 dst_sel:DWORD dst_unused:UNUSED_PAD src0_sel:DWORD src1_sel:WORD_1\n"
  "v_mfma_f32_16x16x32_bf16 v[152:155], v[220:223], v[48:51], v[152:155]\n"
  "ds_read_u16 v176, v176\n"
  "v_exp_f16 v212, v208\n"
  "v_mfma_f32_16x16x32_bf16 v[156:159], v[220:223], v[52:55], v[156:159]\n"
  "ds_read_u16_d16_hi v177, v177\n"
  "v_exp_f16 v213, v209\n"
  "v_mfma_f32_16x16x32_bf16 v[160:163], v[220:223], v[72:75], v[160:163]\n"
  "s_waitcnt lgkmcnt(8)\n"
  "ds_read_b128 v[220:223], v1 offset:8448\n"
  "v_lshlrev_b32_sdwa v179, 1, v179 dst_sel:DWORD dst_unused:UNUSED_PAD src0_sel:DWORD src1_sel:WORD_1\n"
  "v_exp_f16_sdwa v212, v208 dst_sel:WORD_1 dst_unused:UNUSED_PRESERVE src0_sel:WORD_1\n"
  "v_mfma_f32_16x16x32_f16 v[92:95], v[224:227], v[184:187], v[92:95]\n"
  "ds_read_u16 v178, v178\n"
  "v_exp_f16_sdwa v213, v209 dst_sel:WORD_1 dst_unused:UNUSED_PRESERVE src0_sel:WORD_1\n"
  "v_mfma_f32_16x16x32_f16 v[116:119], v[224:227], v[188:191], v[116:119]\n"
  "ds_read_u16_d16_hi v179, v179\n"
  "v_pk_add_f16 v212, v212, 1.0 op_sel_hi:[1,0]\n"
  "v_mfma_f32_16x16x32_f16 v[140:143], v[224:227], v[164:167], v[140:143]\n"
  "s_waitcnt lgkmcnt(9)\n"
  "ds_read_b128 v[224:227], v1 offset:10496\n"
  "v_pk_add_f16 v213, v213, 1.0 op_sel_hi:[1,0]\n"
  "v_rcp_f16 v208, v212\n"
  "v_mfma_f32_16x16x32_f16 v[96:99], v[228:231], v[184:187], v[96:99]\n"
  "v_rcp_f16 v209, v213\n"
  "v_mfma_f32_16x16x32_f16 v[120:123], v[228:231], v[188:191], v[120:123]\n"
  "v_rcp_f16_sdwa v208, v212 dst_sel:WORD_1 dst_unused:UNUSED_PRESERVE src0_sel:WORD_1\n"
  "v_mfma_f32_16x16x32_f16 v[144:147], v[228:231], v[164:167], v[144:147]\n"
  "s_waitcnt lgkmcnt(6)\n"
  "ds_read_b128 v[228:231], v1 offset:12544\n"
  "v_rcp_f16_sdwa v209, v213 dst_sel:WORD_1 dst_unused:UNUSED_PRESERVE src0_sel:WORD_1\n"
  "v_mfma_f32_16x16x32_f16 v[100:103], v[216:219], v[184:187], v[100:103]\n"
  "v_or_b32 v198, v172, v173\n"
  "v_pk_mul_f16 v194, v194, v208\n"
  "v_mfma_f32_16x16x32_f16 v[124:127], v[216:219], v[188:191], v[124:127]\n"
  "v_pk_mul_f16 v195, v195, v209\n"
  "v_or_b32 v199, v174, v175\n"
  "v_mfma_f32_16x16x32_f16 v[148:151], v[216:219], v[164:167], v[148:151]\n"
  "s_waitcnt lgkmcnt(2)\n"
  "ds_read_b128 v[216:219], v3 offset:10240\n"
  "v_or_b32 v182, v176, v177\n"
  "v_or_b32 v183, v178, v179\n"
  "v_mfma_f32_16x16x32_bf16 v[168:171], v[220:223], v[32:35], 0\n"
  "v_lshlrev_b32_sdwa v156, 1, v156 dst_sel:DWORD dst_unused:UNUSED_PAD src0_sel:DWORD src1_sel:WORD_1\n"
  "v_lshlrev_b32_sdwa v157, 1, v157 dst_sel:DWORD dst_unused:UNUSED_PAD src0_sel:DWORD src1_sel:WORD_1\n"
  "v_mfma_f32_16x16x32_bf16 v[172:175], v[220:223], v[36:39], 0\n"
  "ds_read_u16 v156, v156\n"
  "v_lshlrev_b32_sdwa v158, 1, v158 dst_sel:DWORD dst_unused:UNUSED_PAD src0_sel:DWORD src1_sel:WORD_1\n"
  "v_cvt_pk_f16_f32 v184, v152, v153\n"
  "v_mfma_f32_16x16x32_bf16 v[176:179], v[220:223], v[56:59], 0\n"
  "s_waitcnt lgkmcnt(3)\n"
  "ds_read_b128 v[220:223], v3 offset:11264\n"
  "v_cvt_pk_f16_f32 v185, v154, v155\n"
  "v_pk_fma_f16 v208, v184, v184, s56\n"
  "v_mfma_f32_16x16x32_bf16 v[168:171], v[224:227], v[40:43], v[168:171]\n"
  "ds_read_u16_d16_hi v157, v157\n"
  "v_pk_fma_f16 v209, v185, v185, s56\n"
  "v_lshlrev_b32_sdwa v159, 1, v159 dst_sel:DWORD dst_unused:UNUSED_PAD src0_sel:DWORD src1_sel:WORD_1\n"
  "v_mfma_f32_16x16x32_bf16 v[172:175], v[224:227], v[44:47], v[172:175]\n"
  "ds_read_u16 v158, v158\n"
  "v_lshlrev_b32_sdwa v160, 1, v160 dst_sel:DWORD dst_unused:UNUSED_PAD src0_sel:DWORD src1_sel:WORD_1\n"
  "v_pk_mul_f16 v208, v184, v208\n"
  "v_pk_mul_f16 v209, v185, v209\n"
  "v_mfma_f32_16x16x32_bf16 v[176:179], v[224:227], v[64:67], v[176:179]\n"
  "s_waitcnt lgkmcnt(5)\n"
  "ds_read_b128 v[224:227], v3 offset:12288\n"
  "ds_read_u16_d16_hi v159, v159\n"
  "v_lshlrev_b32_sdwa v161, 1, v161 dst_sel:DWORD dst_unused:UNUSED_PAD src0_sel:DWORD src1_sel:WORD_1\n"
  "v_lshlrev_b32_sdwa v162, 1, v162 dst_sel:DWORD dst_unused:UNUSED_PAD src0_sel:DWORD src1_sel:WORD_1\n"
  "v_mfma_f32_16x16x32_bf16 v[168:171], v[228:231], v[48:51], v[168:171]\n"
  "ds_read_u16 v160, v160\n"
  "v_exp_f16 v212, v208\n"
  "v_mfma_f32_16x16x32_bf16 v[172:175], v[228:231], v[52:55], v[172:175]\n"
  "ds_read_u16_d16_hi v161, v161\n"
  "v_exp_f16 v213, v209\n"
  "v_mfma_f32_16x16x32_bf16 v[176:179], v[228:231], v[72:75], v[176:179]\n"
  "s_waitcnt lgkmcnt(8)\n"
  "ds_read_b128 v[228:231], v1 offset:14336\n"
  "v_lshlrev_b32_sdwa v163, 1, v163 dst_sel:DWORD dst_unused:UNUSED_PAD src0_sel:DWORD src1_sel:WORD_1\n"
  "v_exp_f16_sdwa v212, v208 dst_sel:WORD_1 dst_unused:UNUSED_PRESERVE src0_sel:WORD_1\n"
  "v_mfma_f32_16x16x32_f16 v[80:83], v[216:219], v[192:195], v[80:83]\n"
  "ds_read_u16 v162, v162\n"
  "v_exp_f16_sdwa v213, v209 dst_sel:WORD_1 dst_unused:UNUSED_PRESERVE src0_sel:WORD_1\n"
  "v_mfma_f32_16x16x32_f16 v[104:107], v[216:219], v[196:199], v[104:107]\n"
  "ds_read_u16_d16_hi v163, v163\n"
  "v_pk_add_f16 v212, v212, 1.0 op_sel_hi:[1,0]\n"
  "v_mfma_f32_16x16x32_f16 v[128:131], v[216:219], v[180:183], v[128:131]\n"
  "s_waitcnt lgkmcnt(9)\n"
  "ds_read_b128 v[216:219], v1 offset:16384\n"
  "v_pk_add_f16 v213, v213, 1.0 op_sel_hi:[1,0]\n"
  "v_rcp_f16 v208, v212\n"
  "v_mfma_f32_16x16x32_f16 v[84:87], v[220:223], v[192:195], v[84:87]\n"
  "v_rcp_f16 v209, v213\n"
  "v_mfma_f32_16x16x32_f16 v[108:111], v[220:223], v[196:199], v[108:111]\n"
  "v_rcp_f16_sdwa v208, v212 dst_sel:WORD_1 dst_unused:UNUSED_PRESERVE src0_sel:WORD_1\n"
  "v_mfma_f32_16x16x32_f16 v[132:135], v[220:223], v[180:183], v[132:135]\n"
  "s_waitcnt lgkmcnt(6)\n"
  "ds_read_b128 v[220:223], v1 offset:18432\n"
  "v_rcp_f16_sdwa v209, v213 dst_sel:WORD_1 dst_unused:UNUSED_PRESERVE src0_sel:WORD_1\n"
  "v_mfma_f32_16x16x32_f16 v[88:91], v[224:227], v[192:195], v[88:91]\n"
  "v_or_b32 v188, v156, v157\n"
  "v_pk_mul_f16 v184, v184, v208\n"
  "v_mfma_f32_16x16x32_f16 v[112:115], v[224:227], v[196:199], v[112:115]\n"
  "v_pk_mul_f16 v185, v185, v209\n"
  "v_or_b32 v189, v158, v159\n"
  "v_mfma_f32_16x16x32_f16 v[136:139], v[224:227], v[180:183], v[136:139]\n"
  "s_waitcnt lgkmcnt(2)\n"
  "ds_read_b128 v[224:227], v3 offset:13312\n"
  "v_or_b32 v164, v160, v161\n"
  "v_or_b32 v165, v162, v163\n"
  "v_mfma_f32_16x16x32_bf16 v[152:155], v[228:231], v[32:35], 0\n"
  "v_lshlrev_b32_sdwa v172, 1, v172 dst_sel:DWORD dst_unused:UNUSED_PAD src0_sel:DWORD src1_sel:WORD_1\n"
  "v_lshlrev_b32_sdwa v173, 1, v173 dst_sel:DWORD dst_unused:UNUSED_PAD src0_sel:DWORD src1_sel:WORD_1\n"
  "v_mfma_f32_16x16x32_bf16 v[156:159], v[228:231], v[36:39], 0\n"
  "ds_read_u16 v172, v172\n"
  "v_lshlrev_b32_sdwa v174, 1, v174 dst_sel:DWORD dst_unused:UNUSED_PAD src0_sel:DWORD src1_sel:WORD_1\n"
  "v_cvt_pk_f16_f32 v186, v168, v169\n"
  "v_mfma_f32_16x16x32_bf16 v[160:163], v[228:231], v[56:59], 0\n"
  "s_waitcnt lgkmcnt(3)\n"
  "ds_read_b128 v[228:231], v3 offset:14336\n"
  "v_cvt_pk_f16_f32 v187, v170, v171\n"
  "v_pk_fma_f16 v208, v186, v186, s56\n"
  "v_mfma_f32_16x16x32_bf16 v[152:155], v[216:219], v[40:43], v[152:155]\n"
  "ds_read_u16_d16_hi v173, v173\n"
  "v_pk_fma_f16 v209, v187, v187, s56\n"
  "v_lshlrev_b32_sdwa v175, 1, v175 dst_sel:DWORD dst_unused:UNUSED_PAD src0_sel:DWORD src1_sel:WORD_1\n"
  "v_mfma_f32_16x16x32_bf16 v[156:159], v[216:219], v[44:47], v[156:159]\n"
  "ds_read_u16 v174, v174\n"
  "v_lshlrev_b32_sdwa v176, 1, v176 dst_sel:DWORD dst_unused:UNUSED_PAD src0_sel:DWORD src1_sel:WORD_1\n"
  "v_pk_mul_f16 v208, v186, v208\n"
  "v_pk_mul_f16 v209, v187, v209\n"
  "v_mfma_f32_16x16x32_bf16 v[160:163], v[216:219], v[64:67], v[160:163]\n"
  "s_waitcnt lgkmcnt(5)\n"
  "ds_read_b128 v[216:219], v3 offset:15360\n"
  "ds_read_u16_d16_hi v175, v175\n"
  "v_lshlrev_b32_sdwa v177, 1, v177 dst_sel:DWORD dst_unused:UNUSED_PAD src0_sel:DWORD src1_sel:WORD_1\n"
  "v_lshlrev_b32_sdwa v178, 1, v178 dst_sel:DWORD dst_unused:UNUSED_PAD src0_sel:DWORD src1_sel:WORD_1\n"
  "v_mfma_f32_16x16x32_bf16 v[152:155], v[220:223], v[48:51], v[152:155]\n"
  "ds_read_u16 v176, v176\n"
  "v_exp_f16 v212, v208\n"
  "v_mfma_f32_16x16x32_bf16 v[156:159], v[220:223], v[52:55], v[156:159]\n"
  "ds_read_u16_d16_hi v177, v177\n"
  "v_exp_f16 v213, v209\n"
  "v_mfma_f32_16x16x32_bf16 v[160:163], v[220:223], v[72:75], v[160:163]\n"
  "s_waitcnt lgkmcnt(8)\n"
  "ds_read_b128 v[220:223], v1 offset:14592\n"
  "v_lshlrev_b32_sdwa v179, 1, v179 dst_sel:DWORD dst_unused:UNUSED_PAD src0_sel:DWORD src1_sel:WORD_1\n"
  "v_exp_f16_sdwa v212, v208 dst_sel:WORD_1 dst_unused:UNUSED_PRESERVE src0_sel:WORD_1\n"
  "v_mfma_f32_16x16x32_f16 v[92:95], v[224:227], v[192:195], v[92:95]\n"
  "ds_read_u16 v178, v178\n"
  "v_exp_f16_sdwa v213, v209 dst_sel:WORD_1 dst_unused:UNUSED_PRESERVE src0_sel:WORD_1\n"
  "v_mfma_f32_16x16x32_f16 v[116:119], v[224:227], v[196:199], v[116:119]\n"
  "ds_read_u16_d16_hi v179, v179\n"
  "v_pk_add_f16 v212, v212, 1.0 op_sel_hi:[1,0]\n"
  "v_mfma_f32_16x16x32_f16 v[140:143], v[224:227], v[180:183], v[140:143]\n"
  "s_waitcnt lgkmcnt(9)\n"
  "ds_read_b128 v[224:227], v1 offset:16640\n"
  "v_pk_add_f16 v213, v213, 1.0 op_sel_hi:[1,0]\n"
  "v_rcp_f16 v208, v212\n"
  "v_mfma_f32_16x16x32_f16 v[96:99], v[228:231], v[192:195], v[96:99]\n"
  "v_rcp_f16 v209, v213\n"
  "v_mfma_f32_16x16x32_f16 v[120:123], v[228:231], v[196:199], v[120:123]\n"
  "v_rcp_f16_sdwa v208, v212 dst_sel:WORD_1 dst_unused:UNUSED_PRESERVE src0_sel:WORD_1\n"
  "v_mfma_f32_16x16x32_f16 v[144:147], v[228:231], v[180:183], v[144:147]\n"
  "s_waitcnt lgkmcnt(6)\n"
  "ds_read_b128 v[228:231], v1 offset:18688\n"
  "v_rcp_f16_sdwa v209, v213 dst_sel:WORD_1 dst_unused:UNUSED_PRESERVE src0_sel:WORD_1\n"
  "v_mfma_f32_16x16x32_f16 v[100:103], v[216:219], v[192:195], v[100:103]\n"
  "v_or_b32 v190, v172, v173\n"
  "v_pk_mul_f16 v186, v186, v208\n"
  "v_mfma_f32_16x16x32_f16 v[124:127], v[216:219], v[196:199], v[124:127]\n"
  "v_pk_mul_f16 v187, v187, v209\n"
  "v_or_b32 v191, v174, v175\n"
  "v_mfma_f32_16x16x32_f16 v[148:151], v[216:219], v[180:183], v[148:151]\n"
  "s_waitcnt lgkmcnt(2)\n"
  "ds_read_b128 v[216:219], v3 offset:16384\n"
  "v_or_b32 v166, v176, v177\n"
  "v_or_b32 v167, v178, v179\n"
  "v_mfma_f32_16x16x32_bf16 v[168:171], v[220:223], v[32:35], 0\n"
  "v_lshlrev_b32_sdwa v156, 1, v156 dst_sel:DWORD dst_unused:UNUSED_PAD src0_sel:DWORD src1_sel:WORD_1\n"
  "v_lshlrev_b32_sdwa v157, 1, v157 dst_sel:DWORD dst_unused:UNUSED_PAD src0_sel:DWORD src1_sel:WORD_1\n"
  "v_mfma_f32_16x16x32_bf16 v[172:175], v[220:223], v[36:39], 0\n"
  "ds_read_u16 v156, v156\n"
  "v_lshlrev_b32_sdwa v158, 1, v158 dst_sel:DWORD dst_unused:UNUSED_PAD src0_sel:DWORD src1_sel:WORD_1\n"
  "v_cvt_pk_f16_f32 v192, v152, v153\n"
  "v_mfma_f32_16x16x32_bf16 v[176:179], v[220:223], v[56:59], 0\n"
  "s_waitcnt lgkmcnt(3)\n"
  "ds_read_b128 v[220:223], v3 offset:17408\n"
  "v_cvt_pk_f16_f32 v193, v154, v155\n"
  "v_pk_fma_f16 v208, v192, v192, s56\n"
  "v_mfma_f32_16x16x32_bf16 v[168:171], v[224:227], v[40:43], v[168:171]\n"
  "ds_read_u16_d16_hi v157, v157\n"
  "v_pk_fma_f16 v209, v193, v193, s56\n"
  "v_lshlrev_b32_sdwa v159, 1, v159 dst_sel:DWORD dst_unused:UNUSED_PAD src0_sel:DWORD src1_sel:WORD_1\n"
  "v_mfma_f32_16x16x32_bf16 v[172:175], v[224:227], v[44:47], v[172:175]\n"
  "ds_read_u16 v158, v158\n"
  "v_lshlrev_b32_sdwa v160, 1, v160 dst_sel:DWORD dst_unused:UNUSED_PAD src0_sel:DWORD src1_sel:WORD_1\n"
  "v_pk_mul_f16 v208, v192, v208\n"
  "v_pk_mul_f16 v209, v193, v209\n"
  "v_mfma_f32_16x16x32_bf16 v[176:179], v[224:227], v[64:67], v[176:179]\n"
  "s_waitcnt lgkmcnt(5)\n"
  "ds_read_b128 v[224:227], v3 offset:18432\n"
  "ds_read_u16_d16_hi v159, v159\n"
  "v_lshlrev_b32_sdwa v161, 1, v161 dst_sel:DWORD dst_unused:UNUSED_PAD src0_sel:DWORD src1_sel:WORD_1\n"
  "v_lshlrev_b32_sdwa v162, 1, v162 dst_sel:DWORD dst_unused:UNUSED_PAD src0_sel:DWORD src1_sel:WORD_1\n"
  "v_mfma_f32_16x16x32_bf16 v[168:171], v[228:231], v[48:51], v[168:171]\n"
  "ds_read_u16 v160, v160\n"
  "v_exp_f16 v212, v208\n"
  "v_mfma_f32_16x16x32_bf16 v[172:175], v[228:231], v[52:55], v[172:175]\n"
  "ds_read_u16_d16_hi v161, v161\n"
  "v_exp_f16 v213, v209\n"
  "v_mfma_f32_16x16x32_bf16 v[176:179], v[228:231], v[72:75], v[176:179]\n"
  "s_waitcnt lgkmcnt(8)\n"
  "ds_read_b128 v[228:231], v3 offset:19456\n"
  "ds_read_b128 v[200:203], v3 offset:22528\n"
  "ds_read_b128 v[204:207], v3 offset:23552\n"
  "ds_read_b128 v[232:235], v3 offset:24576\n"
  "v_lshlrev_b32_sdwa v163, 1, v163 dst_sel:DWORD dst_unused:UNUSED_PAD src0_sel:DWORD src1_sel:WORD_1\n"
  "v_exp_f16_sdwa v212, v208 dst_sel:WORD_1 dst_unused:UNUSED_PRESERVE src0_sel:WORD_1\n"
  "v_mfma_f32_16x16x32_f16 v[80:83], v[216:219], v[184:187], v[80:83]\n"
  "ds_read_u16 v162, v162\n"
  "v_exp_f16_sdwa v213, v209 dst_sel:WORD_1 dst_unused:UNUSED_PRESERVE src0_sel:WORD_1\n"
  "v_mfma_f32_16x16x32_f16 v[104:107], v[216:219], v[188:191], v[104:107]\n"
  "ds_read_u16_d16_hi v163, v163\n"
  "v_pk_add_f16 v212, v212, 1.0 op_sel_hi:[1,0]\n"
  "v_mfma_f32_16x16x32_f16 v[128:131], v[216:219], v[164:167], v[128:131]\n"
  "s_waitcnt lgkmcnt(12)\n"
  "ds_read_b128 v[216:219], v3 offset:20480\n"
  "v_pk_add_f16 v213, v213, 1.0 op_sel_hi:[1,0]\n"
  "v_rcp_f16 v208, v212\n"
  "v_mfma_f32_16x16x32_f16 v[84:87], v[220:223], v[184:187], v[84:87]\n"
  "v_rcp_f16 v209, v213\n"
  "v_mfma_f32_16x16x32_f16 v[108:111], v[220:223], v[188:191], v[108:111]\n"
  "v_rcp_f16_sdwa v208, v212 dst_sel:WORD_1 dst_unused:UNUSED_PRESERVE src0_sel:WORD_1\n"
  "v_mfma_f32_16x16x32_f16 v[132:135], v[220:223], v[164:167], v[132:135]\n"
  "s_waitcnt lgkmcnt(9)\n"
  "ds_read_b128 v[220:223], v3 offset:21504\n"
  "v_rcp_f16_sdwa v209, v213 dst_sel:WORD_1 dst_unused:UNUSED_PRESERVE src0_sel:WORD_1\n"
  "v_mfma_f32_16x16x32_f16 v[88:91], v[224:227], v[184:187], v[88:91]\n"
  "v_or_b32 v196, v156, v157\n"
  "v_pk_mul_f16 v192, v192, v208\n"
  "v_mfma_f32_16x16x32_f16 v[112:115], v[224:227], v[188:191], v[112:115]\n"
  "v_pk_mul_f16 v193, v193, v209\n"
  "v_or_b32 v197, v158, v159\n"
  "v_mfma_f32_16x16x32_f16 v[136:139], v[224:227], v[164:167], v[136:139]\n"
  "s_waitcnt lgkmcnt(2)\n"
  "v_or_b32 v180, v160, v161\n"
  "v_or_b32 v181, v162, v163\n"
  "v_mfma_f32_16x16x32_f16 v[92:95], v[228:231], v[184:187], v[92:95]\n"
  "v_lshlrev_b32_sdwa v172, 1, v172 dst_sel:DWORD dst_unused:UNUSED_PAD src0_sel:DWORD src1_sel:WORD_1\n"
  "v_lshlrev_b32_sdwa v173, 1, v173 dst_sel:DWORD dst_unused:UNUSED_PAD src0_sel:DWORD src1_sel:WORD_1\n"
  "v_lshlrev_b32_sdwa v174, 1, v174 dst_sel:DWORD dst_unused:UNUSED_PAD src0_sel:DWORD src1_sel:WORD_1\n"
  "v_cvt_pk_f16_f32 v194, v168, v169\n"
  "v_mfma_f32_16x16x32_f16 v[116:119], v[228:231], v[188:191], v[116:119]\n"
  "ds_read_u16 v172, v172\n"
  "v_cvt_pk_f16_f32 v195, v170, v171\n"
  "v_pk_fma_f16 v208, v194, v194, s56\n"
  "v_pk_fma_f16 v209, v195, v195, s56\n"
  "v_lshlrev_b32_sdwa v175, 1, v175 dst_sel:DWORD dst_unused:UNUSED_PAD src0_sel:DWORD src1_sel:WORD_1\n"
  "v_mfma_f32_16x16x32_f16 v[140:143], v[228:231], v[164:167], v[140:143]\n"
  "s_waitcnt lgkmcnt(2)\n"
  "ds_read_u16_d16_hi v173, v173\n"
  "ds_read_u16 v174, v174\n"
  "ds_read_b128 v[236:239], v3 offset:25600\n"
  "ds_read_b128 v[240:243], v3 offset:26624\n"
  "ds_read_b128 v[244:247], v3 offset:27648\n"
  "v_lshlrev_b32_sdwa v176, 1, v176 dst_sel:DWORD dst_unused:UNUSED_PAD src0_sel:DWORD src1_sel:WORD_1\n"
  "v_pk_mul_f16 v208, v194, v208\n"
  "v_pk_mul_f16 v209, v195, v209\n"
  "v_lshlrev_b32_sdwa v177, 1, v177 dst_sel:DWORD dst_unused:UNUSED_PAD src0_sel:DWORD src1_sel:WORD_1\n"
  "v_lshlrev_b32_sdwa v178, 1, v178 dst_sel:DWORD dst_unused:UNUSED_PAD src0_sel:DWORD src1_sel:WORD_1\n"
  "v_mfma_f32_16x16x32_f16 v[96:99], v[216:219], v[184:187], v[96:99]\n"
  "ds_read_u16_d16_hi v175, v175\n"
  "ds_read_u16 v176, v176\n"
  "v_exp_f16 v212, v208\n"
  "v_exp_f16 v213, v209\n"
  "v_mfma_f32_16x16x32_f16 v[120:123], v[216:219], v[188:191], v[120:123]\n"
  "ds_read_u16_d16_hi v177, v177\n"
  "v_lshlrev_b32_sdwa v179, 1, v179 dst_sel:DWORD dst_unused:UNUSED_PAD src0_sel:DWORD src1_sel:WORD_1\n"
  "v_exp_f16_sdwa v212, v208 dst_sel:WORD_1 dst_unused:UNUSED_PRESERVE src0_sel:WORD_1\n"
  "v_exp_f16_sdwa v213, v209 dst_sel:WORD_1 dst_unused:UNUSED_PRESERVE src0_sel:WORD_1\n"
  "v_mfma_f32_16x16x32_f16 v[144:147], v[216:219], v[164:167], v[144:147]\n"
  "s_waitcnt lgkmcnt(7)\n"
  "ds_read_u16 v178, v178\n"
  "ds_read_u16_d16_hi v179, v179\n"
  "v_pk_add_f16 v212, v212, 1.0 op_sel_hi:[1,0]\n"
  "v_pk_add_f16 v213, v213, 1.0 op_sel_hi:[1,0]\n"
  "v_rcp_f16 v208, v212\n"
  "v_mfma_f32_16x16x32_f16 v[100:103], v[220:223], v[184:187], v[100:103]\n"
  "v_rcp_f16 v209, v213\n"
  "v_rcp_f16_sdwa v208, v212 dst_sel:WORD_1 dst_unused:UNUSED_PRESERVE src0_sel:WORD_1\n"
  "v_mfma_f32_16x16x32_f16 v[124:127], v[220:223], v[188:191], v[124:127]\n"
  "v_rcp_f16_sdwa v209, v213 dst_sel:WORD_1 dst_unused:UNUSED_PRESERVE src0_sel:WORD_1\n"
  "v_or_b32 v198, v172, v173\n"
  "v_pk_mul_f16 v194, v194, v208\n"
  "v_mfma_f32_16x16x32_f16 v[148:151], v[220:223], v[164:167], v[148:151]\n"
  "s_waitcnt lgkmcnt(0)\n"
  "v_pk_mul_f16 v195, v195, v209\n"
  "v_or_b32 v199, v174, v175\n"
  "v_or_b32 v182, v176, v177\n"
  "v_or_b32 v183, v178, v179\n"
  "v_mfma_f32_16x16x32_f16 v[80:83], v[200:203], v[192:195], v[80:83]\n"
  "v_mfma_f32_16x16x32_f16 v[84:87], v[204:207], v[192:195], v[84:87]\n"
  "v_mfma_f32_16x16x32_f16 v[88:91], v[232:235], v[192:195], v[88:91]\n"
  "v_mfma_f32_16x16x32_f16 v[92:95], v[236:239], v[192:195], v[92:95]\n"
  "v_mfma_f32_16x16x32_f16 v[96:99], v[240:243], v[192:195], v[96:99]\n"
  "v_mfma_f32_16x16x32_f16 v[100:103], v[244:247], v[192:195], v[100:103]\n"
  "v_mfma_f32_16x16x32_f16 v[104:107], v[200:203], v[196:199], v[104:107]\n"
  "v_mfma_f32_16x16x32_f16 v[108:111], v[204:207], v[196:199], v[108:111]\n"
  "s_nop 0\n"
  "v_cvt_pk_bf16_f32 v80, v80, v81\n"
  "v_cvt_pk_bf16_f32 v81, v82, v83\n"
  "v_mfma_f32_16x16x32_f16 v[112:115], v[232:235], v[196:199], v[112:115]\n"
  "v_cvt_pk_bf16_f32 v82, v84, v85\n"
  "v_cvt_pk_bf16_f32 v83, v86, v87\n"
  "v_mfma_f32_16x16x32_f16 v[116:119], v[236:239], v[196:199], v[116:119]\n"
  "v_cvt_pk_bf16_f32 v88, v88, v89\n"
  "v_cvt_pk_bf16_f32 v89, v90, v91\n"
  "v_mfma_f32_16x16x32_f16 v[120:123], v[240:243], v[196:199], v[120:123]\n"
  "v_cvt_pk_bf16_f32 v90, v92, v93\n"
  "v_cvt_pk_bf16_f32 v91, v94, v95\n"
  "v_mfma_f32_16x16x32_f16 v[124:127], v[244:247], v[196:199], v[124:127]\n"
  "v_cvt_pk_bf16_f32 v96, v96, v97\n"
  "v_cvt_pk_bf16_f32 v97, v98, v99\n"
  "v_cvt_pk_bf16_f32 v98, v100, v101\n"
  "v_cvt_pk_bf16_f32 v99, v102, v103\n"
  "buffer_store_dwordx4 v[80:83], v5, s[44:47], s59 offen offset:0 sc1\n"
  "buffer_store_dwordx4 v[88:91], v5, s[44:47], s59 offen offset:64 sc1\n"
  "buffer_store_dwordx4 v[96:99], v5, s[44:47], s59 offen offset:128 sc1\n"
  "v_mfma_f32_16x16x32_f16 v[128:131], v[200:203], v[180:183], v[128:131]\n"
  "v_mfma_f32_16x16x32_f16 v[132:135], v[204:207], v[180:183], v[132:135]\n"
  "v_cvt_pk_bf16_f32 v104, v104, v105\n"
  "v_cvt_pk_bf16_f32 v105, v106, v107\n"
  "v_mfma_f32_16x16x32_f16 v[136:139], v[232:235], v[180:183], v[136:139]\n"
  "v_cvt_pk_bf16_f32 v106, v108, v109\n"
  "v_cvt_pk_bf16_f32 v107, v110, v111\n"
  "v_mfma_f32_16x16x32_f16 v[140:143], v[236:239], v[180:183], v[140:143]\n"
  "v_cvt_pk_bf16_f32 v112, v112, v113\n"
  "v_cvt_pk_bf16_f32 v113, v114, v115\n"
  "v_mfma_f32_16x16x32_f16 v[144:147], v[240:243], v[180:183], v[144:147]\n"
  "v_cvt_pk_bf16_f32 v114, v116, v117\n"
  "v_cvt_pk_bf16_f32 v115, v118, v119\n"
  "v_mfma_f32_16x16x32_f16 v[148:151], v[244:247], v[180:183], v[148:151]\n"
  "v_cvt_pk_bf16_f32 v120, v120, v121\n"
  "v_cvt_pk_bf16_f32 v121, v122, v123\n"
  "v_cvt_pk_bf16_f32 v122, v124, v125\n"
  "v_cvt_pk_bf16_f32 v123, v126, v127\n"
  "buffer_store_dwordx4 v[104:107], v5, s[44:47], s63 offen offset:0 sc1\n"
  "buffer_store_dwordx4 v[112:115], v5, s[44:47], s63 offen offset:64 sc1\n"
  "buffer_store_dwordx4 v[120:123], v5, s[44:47], s63 offen offset:128 sc1\n"
  "v_cvt_pk_bf16_f32 v128, v128, v129\n"
  "v_cvt_pk_bf16_f32 v129, v130, v131\n"
  "v_cvt_pk_bf16_f32 v130, v132, v133\n"
  "v_cvt_pk_bf16_f32 v131, v134, v135\n"
  "v_cvt_pk_bf16_f32 v136, v136, v137\n"
  "v_cvt_pk_bf16_f32 v137, v138, v139\n"
  "v_cvt_pk_bf16_f32 v138, v140, v141\n"
  "v_cvt_pk_bf16_f32 v139, v142, v143\n"
  "v_cvt_pk_bf16_f32 v144, v144, v145\n"
  "v_cvt_pk_bf16_f32 v145, v146, v147\n"
  "v_cvt_pk_bf16_f32 v146, v148, v149\n"
  "v_cvt_pk_bf16_f32 v147, v150, v151\n"
  "buffer_store_dwordx4 v[128:131], v5, s[44:47], s57 offen offset:0 sc1\n"
  "buffer_store_dwordx4 v[136:139], v5, s[44:47], s57 offen offset:64 sc1\n"
  "buffer_store_dwordx4 v[144:147], v5, s[44:47], s57 offen offset:128 sc1\n"
  "s_endpgm\n"
  ".Lexit%=:\n"
  "s_waitcnt vmcnt(0) lgkmcnt(0)\n"
  "s_setprio 0\n"
  "s_mov_b32 m0, s71\n"
  "s_mov_b32 %0, s95\n"
  "s_mov_b32 %1, s74\n"
  : "=&s"(status), "=&s"(tag), "+s"(ka)
  : "s"((const void*)g_lut), "s"(w), "s"(X0 - 1), "s"(Y0), "s"(b)
  : "v0","v1","v2","v3","v4","v5","v6","v7","v8","v9","v10","v11","v12","v13","v14","v15","v16","v17","v18","v19","v20","v21","v22","v23","v24","v25","v26","v27","v28","v29","v30","v31","v32","v33","v34","v35","v36","v37","v38","v39","v40","v41","v42","v43","v44","v45","v46","v47","v48","v49","v50","v51","v52","v53","v54","v55","v56","v57","v58","v59","v60","v61","v62","v63","v64","v65","v66","v67","v68","v69","v70","v71","v72","v73","v74","v75","v76","v77","v78","v79","v80","v81","v82","v83","v84","v85","v86","v87","v88","v89","v90","v91","v92","v93","v94","v95","v96","v97","v98","v99","v100","v101","v102","v103","v104","v105","v106","v107","v108","v109","v110","v111","v112","v113","v114","v115","v116","v117","v118","v119","v120","v121","v122","v123","v124","v125","v126","v127","v128","v129","v130","v131","v132","v133","v134","v135","v136","v137","v138","v139","v140","v141","v142","v143","v144","v145","v146","v147","v148","v149","v150","v151","v152","v153","v154","v155","v156","v157","v158","v159","v160","v161","v162","v163","v164","v165","v166","v167","v168","v169","v170","v171","v172","v173","v174","v175","v176","v177","v178","v179","v180","v181","v182","v183","v184","v185","v186","v187","v188","v189","v190","v191","v192","v193","v194","v195","v196","v197","v198","v199","v200","v201","v202","v203","v204","v205","v206","v207","v208","v209","v210","v211","v212","v213","v214","v215","v216","v217","v218","v219","v220","v221","v222","v223","v224","v225","v226","v227","v228","v229","v230","v231","v232","v233","v234","v235","v236","v237","v238","v239","v240","v241","v242","v243","v244","v245","v246","v247","v248","v249","v250","v251","v252","v253","v254","v255","s8","s9","s10","s11","s12","s13","s14","s15","s16","s17","s18","s19","s20","s21","s22","s23","s40","s41","s42","s43","s44","s45","s46","s47","s48","s49","s50","s51","s52","s53","s54","s55","s56","s57","s58","s59","s60","s61","s62","s63","s64","s65","s66","s67","s68","s69","s70","s71","s72","s73","s74","s75","s76","s77","s78","s79","s80","s81","s82","s83","s84","s85","s86","s87","s88","s89","s90","s91","s92","s93","s94","s95","s96","s97","s98","s99","vcc","scc","memory");
  if (status == 0) return;

  const void *x = ka[0], *dw = ka[1], *w1 = ka[2], *w2 = ka[3], *gamma = ka[4], *beta = ka[5];
  void *out = ka[6], *ws = ka[7];
  const int lane = __builtin_amdgcn_mbcnt_hi(-1, __builtin_amdgcn_mbcnt_lo(-1, status >> 8));
  const int tid = w * 64 + lane;
  const unsigned short* xs = (const unsigned short*)x;
  float* sc = (float*)(smem + 160256);
  float* ln = (float*)smem + w * 512;
  float* hb = ln + 128;
  float* red = (float*)(smem + 32768);
  float h[6];
  __syncthreads();
  if (status == 1) {
    float ss[6] = {0.f, 0.f, 0.f, 0.f, 0.f, 0.f};
    for (int p = w; p < 1024; p += 8) {
      slow_hidden(xs, (const unsigned short*)dw, (const unsigned short*)w1, ln, Y0 + (p >> 5), X0 + (p & 31), lane, h);
#pragma unroll
      for (int i = 0; i < 6; ++i) ss[i] = fmaf(h[i], h[i], ss[i]);
    }
#pragma unroll
    for (int i = 0; i < 6; ++i) red[w * 384 + lane + 64 * i] = ss[i];
    __syncthreads();
    unsigned* wu = (unsigned*)ws;
    float* acc = (float*)((unsigned char*)ws + 8192);
    if (w == 0) {
      for (unsigned sp = 0; sp < (1u << 22) && __hip_atomic_load(wu + 256 / 4, __ATOMIC_RELAXED, __HIP_MEMORY_SCOPE_AGENT) != tag; ++sp) __builtin_amdgcn_s_sleep(2);
#pragma unroll
      for (int i = 0; i < 6; ++i) {
        float s = 0.f;
        for (int k = 0; k < 8; ++k) s += red[k * 384 + lane + 64 * i];
        (void)__hip_atomic_fetch_add(acc + (b & (4 - 1)) * 384 + lane + 64 * i, s, __ATOMIC_RELAXED, __HIP_MEMORY_SCOPE_AGENT);
      }
      asm volatile("s_waitcnt vmcnt(0)" ::: "memory");
      if (lane == 0) (void)__hip_atomic_fetch_add(wu + 512 / 4 + (b >> 4) * 64, 1u, __ATOMIC_RELAXED, __HIP_MEMORY_SCOPE_AGENT);
      for (unsigned sp = 0; sp < (1u << 22); ++sp) {
        const unsigned c = __hip_atomic_load(wu + 512 / 4 + (lane & 15) * 64, __ATOMIC_RELAXED, __HIP_MEMORY_SCOPE_AGENT);
        if (__all(c == 16u)) break;
        __builtin_amdgcn_s_sleep(8);
      }
      asm volatile("" ::: "memory");
      float gx[6], t = 0.f;
#pragma unroll
      for (int i = 0; i < 6; ++i) {
        float s = 0.f;
        for (int k = 0; k < 4; ++k) s += __hip_atomic_load(acc + k * 384 + lane + 64 * i, __ATOMIC_RELAXED, __HIP_MEMORY_SCOPE_AGENT);
        gx[i] = sqrtf(s + 1e-6f);
        t += gx[i];
      }
      const float mean = wsum(t) * (1.f / 384.f);
#pragma unroll
      for (int i = 0; i < 6; ++i) sc[lane + 64 * i] = 1.f + bf2f(((const unsigned short*)gamma)[lane + 64 * i]) * (gx[i] / (mean + 1e-6f));
      if (b == 0 && lane == 0) __hip_atomic_store(wu + 0 / 4, tag, __ATOMIC_RELAXED, __HIP_MEMORY_SCOPE_AGENT);
    }
  } else if (tid < 384) {
    sc[tid] *= -0.46849387884140015f;
  }
  __syncthreads();
  for (int p = w; p < 1024; p += 8) {
    const int py = Y0 + (p >> 5), px = X0 + (p & 31);
    slow_hidden(xs, (const unsigned short*)dw, (const unsigned short*)w1, ln, py, px, lane, h);
#pragma unroll
    for (int i = 0; i < 6; ++i) hb[lane + 64 * i] = fmaf(h[i], sc[lane + 64 * i], bf2f(((const unsigned short*)beta)[lane + 64 * i]));
    __builtin_amdgcn_fence(__ATOMIC_SEQ_CST, "workgroup");
    const size_t o = ((size_t)py * 512 + px) * 96;
    const unsigned short* w2s = (const unsigned short*)w2;
    const int c1 = lane < 32 ? 64 + lane : lane;
    float a0 = 0.f, a1 = 0.f;
    for (int e = 0; e < 384; ++e) {
      const float v = hb[e];
      a0 = fmaf(v, bf2f(w2s[e * 96 + lane]), a0);
      a1 = fmaf(v, bf2f(w2s[e * 96 + c1]), a1);
    }
    __hip_bfloat16* ob = (__hip_bfloat16*)out;
    ob[o + lane] = __float2bfloat16(a0 + bf2f(xs[o + lane]));
    if (lane < 32) ob[o + c1] = __float2bfloat16(a1 + bf2f(xs[o + c1]));
    __builtin_amdgcn_fence(__ATOMIC_SEQ_CST, "workgroup");
  }
}

extern "C" void kernel_launch(void* const* d_in, const int* in_sizes, int n_in, void* d_out, int out_size, void* d_ws,
                              size_t ws_size, hipStream_t stream) {
  (void)hipFuncSetAttribute((const void*)convnext_fused, hipFuncAttributeMaxDynamicSharedMemorySize, 160 * 1024);
  convnext_fused<<<256, 512, 160 * 1024, stream>>>(d_in[0], d_in[1], d_in[2], d_in[3], d_in[4], d_in[5], d_out, d_ws);
}
```
